```python
import torch

def _softmax(x, axis=-1):
    return torch.softmax(x, dim=axis)
def _gelu(x):
    return torch.nn.functional.gelu(x, approximate="tanh")
def _silu(x):
    return torch.nn.functional.silu(x)
def _sigmoid(x):
    return torch.sigmoid(x)
def _layernorm(x, axis=-1, eps=1e-5):
    m = x.mean(dim=axis, keepdim=True)
    v = x.var(dim=axis, keepdim=True, unbiased=False)
    return (x - m) / torch.sqrt(v + eps)
def _scatter_add(tgt, idx, src, axis=0):
    return tgt.clone().index_add_(axis, idx, src)
def _segment_sum(src, seg, n):
    out = torch.zeros((n,) + src.shape[1:], dtype=src.dtype)
    return out.index_add_(0, seg, src)
def _pad(x, pw):
    flat = tuple(v for pair in reversed(pw) for v in pair)
    return torch.nn.functional.pad(x, flat)

kernel_name = 'gated_mix__softmax_full_mha__geglu_ffn__prompt'

def setup_inputs():

    _w = (lambda a: (lambda t: t.to(torch.bfloat16) if t.is_floating_point() else t)(torch.as_tensor(a)))
    def _A_setup():
        B, S, D = 1, 2048, 576
        return {
            "x": _w((lambda *s: torch.randn(*s, dtype=torch.bfloat16) * 0.05)(B, S, D)),
            "wqkv": _w((lambda *s: torch.randn(*s, dtype=torch.bfloat16) * 0.8)(D, 3 * D)),
            "wo": _w((lambda *s: torch.randn(*s, dtype=torch.bfloat16) * 0.05)(D, D)),
        }
    _A_in = _A_setup()

    def _B_setup():
        B, S, D, F = 1, 2048, 576, 4608
        return {
            "x": _w((lambda *s: torch.randn(*s, dtype=torch.bfloat16) * 0.05)(B, S, D)),
            "w_gate": _w((lambda *s: torch.randn(*s, dtype=torch.bfloat16) * 0.1)(D, F)),
            "w_up": _w((lambda *s: torch.randn(*s, dtype=torch.bfloat16) * 0.1)(D, F)),
            "w_down": _w((lambda *s: torch.randn(*s, dtype=torch.bfloat16) * 0.1)(F, D)),
        }
    _B_in = _B_setup()

    return {
        "A_x": _A_in["x"],
        "A_wqkv": _A_in["wqkv"],
        "A_wo": _A_in["wo"],
        "B_x": _B_in["x"],
        "B_w_gate": _B_in["w_gate"],
        "B_w_up": _B_in["w_up"],
        "B_w_down": _B_in["w_down"],
        "gate_w": _w((lambda *s: torch.randn(*s, dtype=torch.bfloat16) * 0.05)(576) * 16.0),
    }

def reference(A_x, A_wqkv, A_wo, B_x, B_w_gate, B_w_up, B_w_down, gate_w):
    def _A_ref(x, wqkv, wo):
        B, S, D = x.shape
        H = 32; Dh = D // H
        qkv = (lambda x, *d: x.permute(*d))((x @ wqkv).reshape(B, S, 3, H, Dh), 2, 0, 3, 1, 4)
        q, k, v = qkv[0], qkv[1], qkv[2]
        scale = Dh ** -0.5
        scores = torch.einsum("bhqd,bhkd->bhqk", q, k) * scale
        p = _softmax(scores, axis=-1)
        o = torch.einsum("bhqk,bhkd->bhqd", p, v)
        return (lambda x, *d: x.permute(*d))(o, 0, 2, 1, 3).reshape(B, S, D) @ wo

    def _B_ref(x, w_gate, w_up, w_down):
        return (_gelu(x @ w_gate) * (x @ w_up)) @ w_down

    x = A_x
    g = _sigmoid((x * gate_w).sum(-1, keepdims=True))
    a = _A_ref(x, A_wqkv, A_wo)
    b = _B_ref(x, B_w_gate, B_w_up, B_w_down)
    return g * a + (1 - g) * b
```

```cpp
#include <hip/hip_runtime.h>
#include <hip/hip_bf16.h>
typedef __hip_bfloat16 bf;
static constexpr int S=2048, D=576, F=4608, H=32, DH=18;
__device__ __forceinline__ float b2f(bf x){return __bfloat162float(x);}
template<typename TA> __device__ __forceinline__ float ld(const TA* p);
template<> __device__ __forceinline__ float ld<float>(const float* p){return *p;}
template<> __device__ __forceinline__ float ld<bf>(const bf* p){return b2f(*p);}
template<typename TA>
__global__ void ngemm(const TA* A, const bf* B, float* C, int M, int N, int K){
  __shared__ float As[16][17], Bs[16][17];
  int tx=threadIdx.x&15, ty=threadIdx.x>>4; int r=blockIdx.y*16+ty, c=blockIdx.x*16+tx; float acc=0;
  for(int k0=0;k0<K;k0+=16){ As[ty][tx]=ld<TA>(A+(size_t)r*K+k0+tx); Bs[ty][tx]=b2f(B[(size_t)(k0+ty)*N+c]); __syncthreads();
    for(int k=0;k<16;k++) acc+=As[ty][k]*Bs[k][tx]; __syncthreads(); }
  C[(size_t)r*N+c]=acc;
}
__global__ void nattn(const float* qkv, float* o){
  int h=blockIdx.y, q=blockIdx.x*64+threadIdx.x; float qv[DH], acc[DH]; float m=-1e30f,l=0;
  for(int d=0;d<DH;d++){ qv[d]=qkv[(size_t)q*3*D+h*DH+d]*0.23570226039551584f; acc[d]=0; }
  for(int k=0;k<S;k++){ const float* kp=qkv+(size_t)k*3*D+D+h*DH; float s=0; for(int d=0;d<DH;d++) s+=qv[d]*kp[d];
    float mn=fmaxf(m,s), a=__expf(m-mn), p=__expf(s-mn); l=l*a+p; for(int d=0;d<DH;d++) acc[d]=acc[d]*a+p*kp[D+d]; m=mn; }
  for(int d=0;d<DH;d++) o[(size_t)q*D+h*DH+d]=acc[d]/l;
}
__global__ void ngeglu(float* g, const float* u, int n){ int i=blockIdx.x*256+threadIdx.x; if(i<n){ float x=g[i]; g[i]=0.5f*x*(1.f+tanhf(0.7978845608028654f*(x+0.044715f*x*x*x)))*u[i]; } }
__global__ void nfinal(const bf* x, const bf* gw, const float* a, const float* b, bf* out){
  int r=blockIdx.x; __shared__ float red[64]; float s=0; for(int i=threadIdx.x;i<D;i+=64) s+=b2f(x[r*D+i])*b2f(gw[i]);
  red[threadIdx.x]=s; __syncthreads(); s=0; for(int i=0;i<64;i++) s+=red[i]; float g=1.f/(1.f+__expf(-s));
  for(int i=threadIdx.x;i<D;i+=64) out[r*D+i]=__float2bfloat16(g*a[r*D+i]+(1.f-g)*b[r*D+i]);
}
static void naive_launch(void* const* d_in, void* d_out, void* d_ws, hipStream_t stream){
  const bf* x=(const bf*)d_in[0]; const bf* wqkv=(const bf*)d_in[1]; const bf* wo=(const bf*)d_in[2];
  const bf* wg=(const bf*)d_in[4]; const bf* wu=(const bf*)d_in[5]; const bf* wd=(const bf*)d_in[6]; const bf* gw=(const bf*)d_in[7];
  float* qkv=(float*)d_ws; float* o=qkv+(size_t)S*3*D; float* a=o+(size_t)S*D; float* b=a+(size_t)S*D; float* g=b+(size_t)S*D; float* u=g+(size_t)S*F;
  ngemm<bf><<<dim3(3*D/16,S/16),256,0,stream>>>(x,wqkv,qkv,S,3*D,D);
  nattn<<<dim3(S/64,H),64,0,stream>>>(qkv,o);
  ngemm<float><<<dim3(D/16,S/16),256,0,stream>>>(o,wo,a,S,D,D);
  ngemm<bf><<<dim3(F/16,S/16),256,0,stream>>>(x,wg,g,S,F,D);
  ngemm<bf><<<dim3(F/16,S/16),256,0,stream>>>(x,wu,u,S,F,D);
  ngeglu<<<S*F/256,256,0,stream>>>(g,u,S*F);
  ngemm<float><<<dim3(D/16,S/16),256,0,stream>>>(g,wd,b,S,D,F);
  nfinal<<<S,64,0,stream>>>(x,gw,a,b,(bf*)d_out);
}
extern "C" void kernel_launch(void* const* d_in, const int* in_sizes, int n_in, void* d_out, int out_size, void* d_ws, size_t ws_size, hipStream_t stream){ naive_launch(d_in,d_out,d_ws,stream); }
```

```cpp
#include <hip/hip_runtime.h>
#include <hip/hip_bf16.h>
#define DI __device__ __forceinline__
#define LAS __attribute__((address_space(3)))
#define GAS __attribute__((address_space(1)))
typedef float f32x16 __attribute__((ext_vector_type(16)));
typedef float f32x4 __attribute__((ext_vector_type(4)));
typedef float f32x2 __attribute__((ext_vector_type(2)));
typedef __bf16 bf16x8 __attribute__((ext_vector_type(8)));
typedef __bf16 bf16x4 __attribute__((ext_vector_type(4)));
typedef __bf16 bf16x2 __attribute__((ext_vector_type(2)));
typedef unsigned u32x2 __attribute__((ext_vector_type(2)));
typedef unsigned u32x4 __attribute__((ext_vector_type(4)));
typedef unsigned u32x8 __attribute__((ext_vector_type(8)));
typedef unsigned u32x16 __attribute__((ext_vector_type(16)));
constexpr unsigned OFF_Q = 1048576u;
constexpr unsigned OFF_K = 3407872u;
constexpr unsigned OFF_V = 5767168u;
constexpr unsigned OFF_O = 8126464u;
constexpr unsigned OFF_H = 10485760u;
constexpr unsigned OFF_G = 12582912u;
constexpr unsigned OFF_P = 12648448u;
constexpr unsigned WS_USED = 31522816u;
constexpr unsigned Z0 = 98304u;
constexpr unsigned CH0 = 122880u;
constexpr unsigned CHS = 38912u;
constexpr unsigned OEXT = 10223616u;
constexpr unsigned FL_QKV = 0x1000u, FL_H = 0x4000u, FL_O = 0x7000u, FL_P = 0xA000u, FL_ALIVE = 0xD000u, FL_M = 0x10000u, FL_STRIDE = 64u;
constexpr unsigned LDS_BYTES = 163840u;
constexpr unsigned SPIN_CAP = 1u << 18;

DI unsigned cvtpk(float a, float b) { f32x2 v = {a, b}; return __builtin_bit_cast(unsigned, __builtin_convertvector(v, bf16x2)); }
DI float bflo(unsigned u) { return __uint_as_float(u << 16); }
DI float bfhi(unsigned u) { return __uint_as_float(u & 0xffff0000u); }
DI int lane_id() { int l; asm volatile("v_mbcnt_lo_u32_b32 %0, -1, 0\n\tv_mbcnt_hi_u32_b32 %0, -1, %0" : "=v"(l)); __builtin_assume(l >= 0 && l < 64); return l; }

__global__ void __launch_bounds__(512, 2)
fused_kernel(const char* x, const char* wqkv, const char* wo, const char* wg, const char* wu, const char* wd, const char* gw, char* out, char* ws) {
  {
    u32x4 st; unsigned tid = threadIdx.x;
    asm volatile(
".macro M d,a,b\nv_mfma_f32_16x16x32_bf16 v[\\d:\\d+3], v[\\a:\\a+3], v[\\b:\\b+3], v[\\d:\\d+3]\n.endm\n.macro Z d,a,b\n"
"v_mfma_f32_16x16x32_bf16 v[\\d:\\d+3], v[\\a:\\a+3], v[\\b:\\b+3], 0\n.endm\n.macro R d,a,o\nds_read_b64_tr_b16 v[\\d:\\d+1], v\\a offset:\\o\n.endm\n"
".macro W n\ns_waitcnt lgkmcnt(\\n)\n.endm\n.macro E r\nv_exp_f32 v\\r, v\\r\n.endm\n98:\ns_setprio 3\ns_load_dword s72, s[52:53], 0x0\n"
"v_readfirstlane_b32 s71, v240\nv_and_b32 v240, 63, v240\nv_lshrrev_b32 v241, 3, v240\nv_lshrrev_b32 v242, 4, v240\nv_and_b32 v243, 7, v240\n"
"s_lshr_b32 s71, s71, 6\ns_and_b32 s74, s84, 1\ns_lshl_b32 s74, s74, 3\ns_bfe_u32 s61, s84, 0x30004\ns_or_b32 s74, s74, s61\ns_and_b32 s75, s84, 6\n"
"s_lshr_b32 s61, s84, 7\ns_add_u32 s75, s75, s61\nv_xor_b32 v242, v242, v243\nv_mul_u32_u24 v216, 0x900, v241\nv_lshl_add_u32 v216, v242, 4, v216\n"
"v_add_u32 v217, 0x4800, v216\nv_xor_b32 v217, 64, v217\ns_lshr_b32 s61, s71, 1\ns_lshl_b32 s61, s61, 5\ns_and_b32 s62, s71, 1\ns_add_u32 s61, s61, s62\n"
"s_lshl_b32 s67, s74, 7\ns_add_u32 s67, s67, s61\ns_mul_i32 s67, s67, 1152\ns_lshl_b32 s85, s71, 11\ns_mov_b32 s58, 2359296\ns_mov_b32 s59, 0x20000\n"
"s_add_u32 m0, s85, 114688\ns_nop 0\nbuffer_load_dwordx4 v216, s[56:59], s67 offen offset:0 lds\ns_add_u32 m0, s85, 115712\ns_nop 0\n"
"buffer_load_dwordx4 v217, s[56:59], s67 offen offset:0 lds\ns_add_u32 m0, s85, 130944\ns_nop 0\n"
"buffer_load_dwordx4 v216, s[56:59], s67 offen offset:128 lds\ns_add_u32 m0, s85, 131968\ns_nop 0\n"
"buffer_load_dwordx4 v217, s[56:59], s67 offen offset:128 lds\ns_add_u32 m0, s85, 147200\ns_nop 0\n"
"buffer_load_dwordx4 v216, s[56:59], s67 offen offset:256 lds\ns_add_u32 m0, s85, 148224\ns_nop 0\n"
"buffer_load_dwordx4 v217, s[56:59], s67 offen offset:256 lds\ns_cmp_lt_u32 s71, 4\ns_cbranch_scc1 90f\ns_getpc_b64 s[96:97]\n"
"s_sub_u32 s96, s96, 4+(.-98b)-8\ns_subb_u32 s97, s97, 0\ns_mov_b32 s98, 99f-98b+5632\ns_mov_b32 s99, 0x20000\ns_and_b32 s69, s84, 0xf8\n"
"s_lshr_b32 s69, s69, 1\ns_add_u32 s69, s69, s71\ns_sub_u32 s69, s69, 4\ns_mul_i32 s69, s69, 1280\nv_mbcnt_lo_u32_b32 v183, -1, 0\n"
"v_lshlrev_b32 v183, 6, v183\ns_mov_b64 exec, 0xfffff\nbuffer_load_dword v232, v183, s[96:99], s69 offen\ns_mov_b64 exec, -1\n90:\ns_setprio 2\n"
"s_lshl_b32 s60, s71, 10\ns_lshl_b32 s62, s62, 1\nv_lshrrev_b32 v242, 2, v240\nv_and_b32 v242, 4, v242\nv_xor_b32 v242, v242, v243\n"
"v_xor_b32 v242, s62, v242\nv_lshlrev_b32 v242, 4, v242\ns_lshl_b32 s61, s71, 3\nv_add_u32 v241, s61, v241\nv_mul_u32_u24 v249, 0x2400, v241\n"
"v_add_u32 v249, v249, v242\nv_mul_u32_u24 v250, 0xd80, v241\nv_add_u32 v250, v250, v242\ns_mul_i32 s62, s75, 1152\ns_bfe_u32 s76, s40, 0x30007\n"
"s_add_u32 s76, s76, s75\ns_sub_u32 s76, 3, s76\ns_and_b32 s76, s76, 7\ns_add_u32 s76, s76, 1\ns_and_b32 s77, s74, 7\ns_and_b32 s77, s77, 1\n"
"s_lshl_b32 s77, s77, 1\ns_and_b32 s65, s84, 1\ns_add_u32 s77, s77, s65\ns_add_u32 s65, s76, s77\ns_add_u32 s65, s65, 1\ns_cmp_ge_u32 s65, 9\n"
"s_cselect_b32 s69, 9, 0\ns_sub_u32 s65, s65, s69\ns_lshl_b32 s65, s65, 7\ns_add_u32 s65, s65, s62\ns_mul_i32 s63, s75, 384\ns_lshl_b32 s64, s75, 7\n"
"s_add_u32 s64, s64, 1920\ns_add_u32 s61, s63, 384\ns_cmp_lt_u32 s75, 6\ns_cselect_b32 s64, s64, s61\ns_cselect_b32 s63, s63, s61\n"
"s_and_b32 s66, s74, 7\ns_and_b32 s66, s66, 1\ns_and_b32 s61, s84, 1\ns_lshl_b32 s61, s61, 1\ns_add_u32 s66, s66, s61\ns_mul_i32 s61, s66, 43\n"
"s_lshr_b32 s61, s61, 7\ns_mul_i32 s61, s61, 3\ns_sub_u32 s66, s66, s61\ns_mul_i32 s66, s66, 128\ns_add_u32 s66, s66, s63\ns_mov_b32 s90, 589824\n"
"s_mov_b32 s94, 5308416\ns_mov_b32 s95, 0x20000\ns_bitcmp1_b32 s84, 3\ns_cselect_b64 vcc, -1, 0\ns_cselect_b64 s[92:93], s[48:49], s[40:41]\n"
"s_cselect_b32 s94, 1990656, s94\ns_cselect_b32 s90, 221184, s90\ns_cselect_b32 s91, s66, s65\nv_cndmask_b32 v220, v249, v250, vcc\ns_mov_b32 s80, s91\n"
"s_add_u32 m0, s60, 0\ns_nop 0\nbuffer_load_dwordx4 v220, s[92:95], s91 offen lds\ns_add_u32 s91, s91, s90\ns_add_u32 m0, s60, 8192\ns_nop 0\n"
"buffer_load_dwordx4 v220, s[92:95], s91 offen lds\ns_add_u32 s91, s91, s90\ns_add_u32 m0, s60, 16384\ns_nop 0\n"
"buffer_load_dwordx4 v220, s[92:95], s91 offen lds\ns_add_u32 s91, s91, s90\ns_setprio 1\ns_mov_b32 s54, 31522816\ns_mov_b32 s55, 0x20000\n"
"s_lshr_b32 s61, s90, 6\nv_mul_u32_u24 v182, s61, v240\ns_and_b32 s81, s74, 7\ns_lshl_b32 s81, s81, 3\ns_add_u32 s81, s81, s71\ns_mul_i32 s81, s81, 9\n"
"s_mul_i32 s61, s61, s81\ns_add_u32 s61, s61, s80\ns_mov_b64 exec, 0x1ff\ns_bitcmp1_b32 s84, 3\ns_cselect_b64 s[96:97], s[48:49], s[44:45]\n"
"s_cselect_b32 s69, 128, 0\ns_mov_b64 s[98:99], s[94:95]\ns_add_u32 s61, s61, s69\ns_mov_b64 exec, -1\ns_add_u32 m0, s85, 65152\ns_nop 0\n"
"buffer_load_dwordx4 v216, s[56:59], s67 offen offset:384 lds\ns_add_u32 m0, s85, 66176\ns_nop 0\n"
"buffer_load_dwordx4 v217, s[56:59], s67 offen offset:384 lds\ns_add_u32 m0, s85, 81408\ns_nop 0\n"
"buffer_load_dwordx4 v216, s[56:59], s67 offen offset:512 lds\ns_add_u32 m0, s85, 82432\ns_nop 0\n"
"buffer_load_dwordx4 v217, s[56:59], s67 offen offset:512 lds\ns_add_u32 m0, s85, 97664\ns_nop 0\n"
"buffer_load_dwordx4 v216, s[56:59], s67 offen offset:640 lds\ns_add_u32 m0, s85, 98688\ns_nop 0\n"
"buffer_load_dwordx4 v217, s[56:59], s67 offen offset:640 lds\ns_setprio 0\ns_add_u32 m0, s60, 24576\ns_nop 0\n"
"buffer_load_dwordx4 v220, s[92:95], s91 offen lds\ns_add_u32 s91, s91, s90\ns_add_u32 m0, s60, 32768\ns_nop 0\n"
"buffer_load_dwordx4 v220, s[92:95], s91 offen lds\ns_add_u32 s91, s91, s90\ns_add_u32 m0, s60, 40960\ns_nop 0\n"
"buffer_load_dwordx4 v220, s[92:95], s91 offen lds\ns_add_u32 s91, s91, s90\ns_add_u32 m0, s60, 49152\ns_nop 0\n"
"buffer_load_dwordx4 v220, s[92:95], s91 offen lds\ns_add_u32 s91, s91, s90\ns_add_u32 m0, s60, 57344\ns_nop 0\n"
"buffer_load_dwordx4 v220, s[92:95], s91 offen lds\ns_add_u32 s91, s91, s90\ns_mov_b32 s42, 5308416\ns_mov_b32 s43, 0x20000\ns_mov_b32 s46, 5308416\n"
"s_mov_b32 s47, 0x20000\nv_and_b32 v241, 15, v240\nv_lshrrev_b32 v242, 4, v240\nv_lshrrev_b32 v243, 1, v241\nv_xor_b32 v243, v243, v242\n"
"v_lshlrev_b32 v243, 4, v243\nv_lshl_add_u32 v218, v241, 7, v243\ns_and_b32 s61, s71, 3\ns_lshl_b32 s61, s61, 12\ns_add_u32 s61, s61, 114688\n"
"v_add_u32 v218, s61, v218\nv_xor_b32 v219, 64, v218\ns_waitcnt vmcnt(11)\ns_barrier\nds_read_b128 v[0:3], v218 offset:0\n"
"ds_read_b128 v[4:7], v218 offset:2048\nds_read_b128 v[8:11], v219 offset:0\nds_read_b128 v[12:15], v219 offset:2048\n"
"ds_read_b128 v[16:19], v218 offset:16384\nds_read_b128 v[20:23], v218 offset:18432\nds_read_b128 v[24:27], v219 offset:16384\n"
"ds_read_b128 v[28:31], v219 offset:18432\nds_read_b128 v[32:35], v218 offset:32768\nds_read_b128 v[36:39], v218 offset:34816\n"
"ds_read_b128 v[40:43], v219 offset:32768\nds_read_b128 v[44:47], v219 offset:34816\nv_lshrrev_b32 v241, 2, v241\nv_lshl_add_u32 v244, v242, 3, v241\n"
"v_lshlrev_b32 v244, 7, v244\nv_and_b32 v241, 2, v241\nv_and_b32 v242, 1, v242\nv_or_b32 v241, v241, v242\ns_lshr_b32 s61, s71, 2\n"
"s_lshl_b32 s61, s61, 1\nv_xor_b32 v241, s61, v241\nv_lshl_add_u32 v244, v241, 5, v244\nv_and_b32 v241, 3, v240\nv_lshl_add_u32 v244, v241, 3, v244\n"
"v_xor_b32 v245, 32, v244\nv_add_u32 v246, 61440, v244\nv_add_u32 v247, 61440, v245\ns_mov_b32 s70, 0xbdd2d3e8\nv_mov_b32 v248, 0xc0135761\n"
"v_subrev_u32 v221, 110592, v250\nv_subrev_u32 v222, 294912, v249\ns_mov_b32 s90, s67\ns_xor_b32 s91, s85, 8192\ns_sub_u32 s92, s91, s85\n"
"s_mul_i32 s92, s92, 9\ns_add_u32 s92, s92, s67\ns_lshl_b32 s61, s74, 3\ns_add_u32 s61, s61, s75\ns_lshl_b32 s68, s61, 14\ns_add_u32 s68, s68, 10485760\n"
"s_lshl_b32 s73, s61, 6\ns_cmp_ge_u32 s71, 4\ns_cselect_b64 vcc, -1, 0\nW 0\ns_barrier\ns_add_u32 s72, s72, 1\ns_bitcmp1_b32 s84, 3\ns_cbranch_scc1 96f\n"
"s_mov_b32 s67, s65\ns_mul_i32 s63, s75, 663552\ns_mul_i32 s65, s73, 2304\ns_add_u32 s65, s65, 12648448\ns_add_u32 s66, s74, s75\n"
"s_add_u32 s69, s73, 16384\ns_add_u32 s73, s73, 122880\ns_mov_b32 s80, s74\ns_mov_b32 s50, 1990656\ns_mov_b32 s51, 0x20000\nR 176,244,0\nR 178,244,512\n"
"R 180,244,4096\nR 182,244,4608\nR 184,245,0\nR 186,245,512\nR 188,245,4096\nR 190,245,4608\ns_sub_u32 s75, s73, 69632\ns_cmp_lg_u32 s71, 0\n"
"s_cbranch_scc1 63f\ns_mov_b64 exec, 1\nv_mov_b32 v254, s72\nbuffer_store_dword v254, off, s[52:55], s75 sc1\ns_mov_b64 exec, -1\n63:\n"
"s_cbranch_vccz 100f\ns_add_u32 m0, s85, 113920\ns_nop 0\nbuffer_load_dwordx4 v216, s[56:59], s90 offen offset:768 lds\ns_add_u32 m0, s85, 114944\n"
"s_nop 0\nbuffer_load_dwordx4 v217, s[56:59], s90 offen offset:768 lds\ns_add_u32 m0, s91, 113920\ns_nop 0\n"
"buffer_load_dwordx4 v216, s[56:59], s92 offen offset:768 lds\ns_add_u32 m0, s91, 114944\ns_nop 0\n"
"buffer_load_dwordx4 v217, s[56:59], s92 offen offset:768 lds\ns_add_u32 m0, s85, 130176\ns_nop 0\n"
"buffer_load_dwordx4 v216, s[56:59], s90 offen offset:896 lds\ns_add_u32 m0, s85, 131200\ns_nop 0\n"
"buffer_load_dwordx4 v217, s[56:59], s90 offen offset:896 lds\n100:\nW 4\nZ 144,176,0\nR 192,244,8192\nM 144,180,8\nR 194,244,8704\nZ 148,176,4\n"
"R 196,244,12288\nM 148,180,12\nR 198,244,12800\nW 4\nZ 152,184,0\nR 176,245,8192\nM 152,188,8\nR 178,245,8704\nZ 156,184,4\nR 180,245,12288\n"
"M 156,188,12\nR 182,245,12800\nW 4\nM 144,192,16\nR 184,244,16384\nM 144,196,24\nR 186,244,16896\nM 148,192,20\nR 188,244,20480\nM 148,196,28\n"
"R 190,244,20992\nW 4\nM 152,176,16\nR 192,245,16384\nM 152,180,24\nR 194,245,16896\nM 156,176,20\nR 196,245,20480\nM 156,180,28\nR 198,245,20992\nW 4\n"
"M 144,184,32\nM 144,188,40\nM 148,184,36\nM 148,188,44\nW 0\nM 152,192,32\nM 152,196,40\nM 156,192,36\nM 156,196,44\nv_lshrrev_b32 v241, 3, v240\n"
"v_and_b32 v242, 2, v241\nv_lshrrev_b32 v243, 2, v241\nv_or_b32 v242, v242, v243\nv_lshlrev_b32 v242, 1, v242\nv_and_b32 v243, 7, v240\n"
"v_xor_b32 v242, v242, v243\ns_lshl_b32 s74, s71, 3\nv_add_u32 v241, s74, v241\nv_mul_u32_u24 v250, 0x480, v241\nv_lshl_add_u32 v250, v242, 4, v250\n"
"s_lshl_b32 s74, s71, 11\ns_add_u32 s74, s74, 98304\nv_lshlrev_b32 v251, 4, v240\nv_add_u32 v251, s74, v251\ns_cbranch_vccnz 101f\n"
"s_add_u32 m0, s85, 130176\ns_nop 0\nbuffer_load_dwordx4 v216, s[56:59], s90 offen offset:896 lds\ns_add_u32 m0, s85, 131200\ns_nop 0\n"
"buffer_load_dwordx4 v217, s[56:59], s90 offen offset:896 lds\ns_add_u32 m0, s85, 146432\ns_nop 0\n"
"buffer_load_dwordx4 v216, s[56:59], s90 offen offset:1024 lds\ns_add_u32 m0, s85, 147456\ns_nop 0\n"
"buffer_load_dwordx4 v217, s[56:59], s90 offen offset:1024 lds\ns_add_u32 m0, s91, 146432\ns_nop 0\n"
"buffer_load_dwordx4 v216, s[56:59], s92 offen offset:1024 lds\ns_add_u32 m0, s91, 147456\ns_nop 0\n"
"buffer_load_dwordx4 v217, s[56:59], s92 offen offset:1024 lds\n101:\ns_cbranch_vccnz 102f\ns_waitcnt vmcnt(6)\n102:\ns_cbranch_vccz 103f\n"
"s_waitcnt vmcnt(6)\n103:\nv_add_u32 v218, 0xffff4000, v218\nv_add_u32 v219, 0xffff4000, v219\ns_barrier\nds_read_b128 v[48:51], v218 offset:0\n"
"ds_read_b128 v[52:55], v218 offset:2048\nds_read_b128 v[56:59], v219 offset:0\nds_read_b128 v[60:63], v219 offset:2048\n"
"ds_read_b128 v[64:67], v218 offset:16384\nds_read_b128 v[68:71], v218 offset:18432\nds_read_b128 v[72:75], v219 offset:16384\n"
"ds_read_b128 v[76:79], v219 offset:18432\nds_read_b128 v[80:83], v218 offset:32768\nds_read_b128 v[84:87], v218 offset:34816\n"
"ds_read_b128 v[88:91], v219 offset:32768\nds_read_b128 v[92:95], v219 offset:34816\nW 0\nR 176,244,24576\nR 178,244,25088\nR 180,244,28672\n"
"R 182,244,29184\nR 184,245,24576\nR 186,245,25088\nR 188,245,28672\nR 190,245,29184\ns_barrier\ns_cbranch_vccz 104f\ns_add_u32 m0, s60, 65536\n"
"s_add_u32 s61, s67, 4718592\nbuffer_load_dwordx4 v249, s[40:43], s61 offen lds\ns_sub_u32 m0, m0, 4096\ns_nop 0\n"
"buffer_load_dwordx4 v222, s[40:43], s61 offen lds\ns_add_u32 m0, s60, 73728\ns_add_u32 s61, s67, 0\nbuffer_load_dwordx4 v249, s[44:47], s61 offen lds\n"
"s_sub_u32 m0, m0, 4096\ns_nop 0\nbuffer_load_dwordx4 v222, s[44:47], s61 offen lds\ns_add_u32 m0, s60, 81920\ns_add_u32 s61, s67, 589824\n"
"buffer_load_dwordx4 v249, s[44:47], s61 offen lds\ns_sub_u32 m0, m0, 4096\ns_nop 0\nbuffer_load_dwordx4 v222, s[44:47], s61 offen lds\n104:\nW 4\n"
"M 144,176,48\nR 192,244,32768\nM 144,180,56\nR 194,244,33280\nM 148,176,52\nR 196,244,36864\nM 148,180,60\nR 198,244,37376\nW 4\nM 152,184,48\n"
"R 176,245,32768\nM 152,188,56\nR 178,245,33280\nM 156,184,52\nR 180,245,36864\nM 156,188,60\nR 182,245,37376\nW 4\nM 144,192,64\nR 184,244,40960\n"
"M 144,196,72\nR 186,244,41472\nM 148,192,68\nR 188,244,45056\nM 148,196,76\nR 190,244,45568\nW 4\nM 152,176,64\nR 192,245,40960\nM 152,180,72\n"
"R 194,245,41472\nM 156,176,68\nR 196,245,45056\nM 156,180,76\nR 198,245,45568\nW 4\nM 144,184,80\nM 144,188,88\nM 148,184,84\nM 148,188,92\nW 0\n"
"M 152,192,80\nM 152,196,88\nM 156,192,84\nM 156,196,92\nv_lshrrev_b32 v241, 2, v240\nv_and_b32 v242, 2, v241\nv_bfe_u32 v243, v240, 4, 1\n"
"v_or_b32 v242, v242, v243\nv_lshlrev_b32 v252, 7, v241\nv_lshl_add_u32 v252, v242, 5, v252\nv_and_b32 v242, 3, v240\n"
"v_lshl_add_u32 v252, v242, 3, v252\ns_lshr_b32 s74, s71, 2\ns_mul_i32 s75, s74, 4160\nv_xor_b32 v252, s75, v252\ns_lshl_b32 s74, s74, 13\n"
"s_and_b32 s75, s71, 3\ns_lshl_b32 s75, s75, 8\ns_add_u32 s74, s74, s75\nv_lshrrev_b32 v241, 4, v240\nv_lshlrev_b32 v253, 10, v241\n"
"v_and_b32 v241, 15, v240\nv_lshl_add_u32 v253, v241, 4, v253\nv_add_u32 v253, s74, v253\ns_sub_u32 s90, s67, s62\ns_mul_i32 s90, s90, 576\n"
"s_cbranch_vccnz 105f\ns_add_u32 m0, s60, 90112\ns_add_u32 s61, s67, 1179648\nbuffer_load_dwordx4 v249, s[44:47], s61 offen lds\ns_add_u32 m0, m0, 4096\n"
"s_add_u32 s61, s61, 294912\nbuffer_load_dwordx4 v249, s[44:47], s61 offen lds\ns_add_u32 m0, s60, 0\ns_add_u32 s61, s67, 1769472\n"
"buffer_load_dwordx4 v249, s[44:47], s61 offen lds\ns_add_u32 m0, m0, 4096\ns_add_u32 s61, s61, 294912\n"
"buffer_load_dwordx4 v249, s[44:47], s61 offen lds\ns_add_u32 m0, s60, 8192\ns_add_u32 s61, s67, 2359296\n"
"buffer_load_dwordx4 v249, s[44:47], s61 offen lds\ns_add_u32 m0, m0, 4096\ns_add_u32 s61, s61, 294912\n"
"buffer_load_dwordx4 v249, s[44:47], s61 offen lds\ns_add_u32 m0, s60, 16384\ns_add_u32 s61, s67, 2949120\n"
"buffer_load_dwordx4 v249, s[44:47], s61 offen lds\ns_add_u32 m0, m0, 4096\ns_add_u32 s61, s61, 294912\n"
"buffer_load_dwordx4 v249, s[44:47], s61 offen lds\n105:\ns_cbranch_vccnz 106f\ns_waitcnt vmcnt(8)\n106:\ns_cbranch_vccz 107f\ns_waitcnt vmcnt(0)\n107:\n"
"v_add_u32 v218, 0xc000, v218\nv_add_u32 v219, 0xc000, v219\ns_barrier\nds_read_b128 v[96:99], v218 offset:0\nds_read_b128 v[100:103], v218 offset:2048\n"
"ds_read_b128 v[104:107], v219 offset:0\nds_read_b128 v[108:111], v219 offset:2048\nds_read_b128 v[112:115], v218 offset:16384\n"
"ds_read_b128 v[116:119], v218 offset:18432\nds_read_b128 v[120:123], v219 offset:16384\nds_read_b128 v[124:127], v219 offset:18432\n"
"ds_read_b128 v[128:131], v218 offset:32768\nds_read_b128 v[132:135], v218 offset:34816\nds_read_b128 v[136:139], v219 offset:32768\n"
"ds_read_b128 v[140:143], v219 offset:34816\nW 0\nR 176,244,49152\nR 178,244,49664\nR 180,244,53248\nR 182,244,53760\nR 184,245,49152\nR 186,245,49664\n"
"R 188,245,53248\nR 190,245,53760\ns_barrier\ns_cbranch_vccz 108f\ns_add_u32 m0, s60, 98304\ns_add_u32 s61, s67, 3538944\n"
"buffer_load_dwordx4 v249, s[44:47], s61 offen lds\ns_sub_u32 m0, m0, 4096\ns_nop 0\nbuffer_load_dwordx4 v222, s[44:47], s61 offen lds\n"
"s_add_u32 m0, s60, 106496\ns_add_u32 s61, s67, 4128768\nbuffer_load_dwordx4 v249, s[44:47], s61 offen lds\ns_sub_u32 m0, m0, 4096\ns_nop 0\n"
"buffer_load_dwordx4 v222, s[44:47], s61 offen lds\ns_add_u32 m0, s60, 114688\ns_add_u32 s61, s67, 4718592\n"
"buffer_load_dwordx4 v249, s[44:47], s61 offen lds\ns_sub_u32 m0, m0, 4096\ns_nop 0\nbuffer_load_dwordx4 v222, s[44:47], s61 offen lds\n108:\nW 4\n"
"M 144,176,96\nR 192,244,57344\nM 144,180,104\nR 194,244,57856\nM 148,176,100\nR 196,244,61440\nM 148,180,108\nR 198,244,61952\nW 4\nM 152,184,96\n"
"R 176,245,57344\nM 152,188,104\nR 178,245,57856\nM 156,184,100\nR 180,245,61440\nM 156,188,108\nR 182,245,61952\nW 4\nM 144,192,112\nR 184,246,4096\n"
"M 144,196,120\nR 186,246,4608\nM 148,192,116\nR 188,246,8192\nM 148,196,124\nR 190,246,8704\nW 4\nM 152,176,112\nR 192,247,4096\nM 152,180,120\n"
"R 194,247,4608\nM 156,176,116\nR 196,247,8192\nM 156,180,124\nR 198,247,8704\nW 4\nM 144,184,128\nR 176,246,12288\nM 144,188,136\nR 178,246,12800\n"
"M 148,184,132\nR 180,246,16384\nM 148,188,140\nR 182,246,16896\nW 4\nM 152,192,128\nR 184,247,12288\nM 152,196,136\nR 186,247,12800\nM 156,192,132\n"
"R 188,247,16384\nM 156,196,140\nR 190,247,16896\ns_cbranch_vccz 109f\ns_waitcnt vmcnt(6)\ns_barrier\n109:\ns_add_u32 s77, s77, 1\ns_cmp_eq_u32 s77, 4\n"
"s_cselect_b32 s77, 0, s77\ns_add_u32 s67, s76, s77\ns_add_u32 s67, s67, 1\ns_cmp_ge_u32 s67, 9\ns_cselect_b32 s61, 9, 0\ns_sub_u32 s67, s67, s61\n"
"s_mul_i32 s91, s67, 73728\ns_lshl_b32 s67, s67, 7\ns_add_u32 s67, s67, s62\ns_add_u32 m0, s60, 24576\ns_add_u32 s61, s67, 0\n"
"buffer_load_dwordx4 v249, s[40:43], s61 offen lds\nv_mul_f32 v240, v144, v144\nv_mul_f32 v241, v145, v145\nv_mul_f32 v242, v146, v146\n"
"v_mul_f32 v243, v147, v147\nv_fma_f32 v240, v240, s70, v248\nv_fma_f32 v241, v241, s70, v248\nv_fma_f32 v242, v242, s70, v248\n"
"s_add_u32 m0, s60, 32768\ns_add_u32 s61, s67, 589824\nbuffer_load_dwordx4 v249, s[40:43], s61 offen lds\nv_fma_f32 v243, v243, s70, v248\n"
"v_mul_f32 v240, v240, v144\nv_mul_f32 v241, v241, v145\nv_mul_f32 v242, v242, v146\nv_mul_f32 v243, v243, v147\nE 240\nE 241\nE 242\n"
"s_add_u32 m0, s60, 40960\ns_add_u32 s61, s67, 1179648\nbuffer_load_dwordx4 v249, s[40:43], s61 offen lds\nE 243\nv_add_f32 v240, 1.0, v240\n"
"v_add_f32 v241, 1.0, v241\nv_add_f32 v242, 1.0, v242\nv_add_f32 v243, 1.0, v243\nv_rcp_f32 v240, v240\nv_rcp_f32 v241, v241\nv_rcp_f32 v242, v242\n"
"s_cbranch_vccnz 110f\ns_waitcnt vmcnt(5)\ns_barrier\n110:\nW 4\nZ 160,176,0\nR 192,246,20480\nM 160,180,8\nR 194,246,20992\nZ 164,176,4\n"
"R 196,246,24576\nM 164,180,12\nR 198,246,25088\nW 4\nZ 168,184,0\nR 176,247,20480\nM 168,188,8\nR 178,247,20992\nZ 172,184,4\nR 180,247,24576\n"
"M 172,188,12\nR 182,247,25088\nW 4\nM 160,192,16\nR 184,246,28672\nM 160,196,24\nR 186,246,29184\nM 164,192,20\nR 188,246,32768\nM 164,196,28\n"
"R 190,246,33280\nW 4\nM 168,176,16\nR 192,247,28672\nM 168,180,24\nR 194,247,29184\nM 172,176,20\nR 196,247,32768\nM 172,180,28\nR 198,247,33280\nW 4\n"
"M 160,184,32\nR 176,244,0\nM 160,188,40\nR 178,244,512\nM 164,184,36\nR 180,244,4096\nM 164,188,44\nR 182,244,4608\nW 4\nM 168,192,32\nR 184,245,0\n"
"M 168,196,40\nR 186,245,512\nM 172,192,36\nR 188,245,4096\nM 172,196,44\nR 190,245,4608\ns_cbranch_vccz 111f\ns_waitcnt vmcnt(5)\ns_barrier\n111:\n"
"s_add_u32 m0, s60, 49152\ns_add_u32 s61, s67, 1769472\nbuffer_load_dwordx4 v249, s[40:43], s61 offen lds\nv_rcp_f32 v243, v243\n"
"v_mul_f32 v144, v240, v144\nv_mul_f32 v145, v241, v145\nv_mul_f32 v146, v242, v146\nv_mul_f32 v147, v243, v147\nv_mul_f32 v240, v148, v148\n"
"v_mul_f32 v241, v149, v149\ns_add_u32 m0, s60, 57344\ns_add_u32 s61, s67, 2359296\nbuffer_load_dwordx4 v249, s[40:43], s61 offen lds\n"
"v_mul_f32 v242, v150, v150\nv_mul_f32 v243, v151, v151\nv_fma_f32 v240, v240, s70, v248\nv_fma_f32 v241, v241, s70, v248\n"
"v_fma_f32 v242, v242, s70, v248\nv_fma_f32 v243, v243, s70, v248\nv_mul_f32 v240, v240, v148\nv_mul_f32 v241, v241, v149\ns_add_u32 m0, s60, 65536\n"
"s_add_u32 s61, s67, 2949120\nbuffer_load_dwordx4 v249, s[40:43], s61 offen lds\nv_mul_f32 v242, v242, v150\nv_mul_f32 v243, v243, v151\nE 240\nE 241\n"
"E 242\nE 243\nv_add_f32 v240, 1.0, v240\nv_add_f32 v241, 1.0, v241\ns_cbranch_vccnz 112f\ns_waitcnt vmcnt(6)\ns_barrier\n112:\nW 4\nM 160,176,48\n"
"R 192,244,8192\nM 160,180,56\nR 194,244,8704\nM 164,176,52\nR 196,244,12288\nM 164,180,60\nR 198,244,12800\nW 4\nM 168,184,48\nR 176,245,8192\n"
"M 168,188,56\nR 178,245,8704\nM 172,184,52\nR 180,245,12288\nM 172,188,60\nR 182,245,12800\nW 4\nM 160,192,64\nR 184,244,16384\nM 160,196,72\n"
"R 186,244,16896\nM 164,192,68\nR 188,244,20480\nM 164,196,76\nR 190,244,20992\nW 4\nM 168,176,64\nR 192,245,16384\nM 168,180,72\nR 194,245,16896\n"
"M 172,176,68\nR 196,245,20480\nM 172,180,76\nR 198,245,20992\nW 4\nM 160,184,80\nR 176,246,36864\nM 160,188,88\nR 178,246,37376\nM 164,184,84\n"
"R 180,246,40960\nM 164,188,92\nR 182,246,41472\nW 4\nM 168,192,80\nR 184,247,36864\nM 168,196,88\nR 186,247,37376\nM 172,192,84\nR 188,247,40960\n"
"M 172,196,92\nR 190,247,41472\ns_cbranch_vccz 113f\ns_waitcnt vmcnt(4)\ns_barrier\n113:\ns_add_u32 m0, s60, 73728\ns_add_u32 s61, s67, 3538944\n"
"buffer_load_dwordx4 v249, s[40:43], s61 offen lds\nv_add_f32 v242, 1.0, v242\nv_add_f32 v243, 1.0, v243\nv_rcp_f32 v240, v240\nv_rcp_f32 v241, v241\n"
"v_rcp_f32 v242, v242\nv_rcp_f32 v243, v243\nv_mul_f32 v148, v240, v148\ns_add_u32 m0, s60, 81920\ns_add_u32 s61, s67, 4128768\n"
"buffer_load_dwordx4 v249, s[40:43], s61 offen lds\nv_mul_f32 v149, v241, v149\nv_mul_f32 v150, v242, v150\nv_mul_f32 v151, v243, v151\n"
"v_mul_f32 v240, v152, v152\nv_mul_f32 v241, v153, v153\nv_mul_f32 v242, v154, v154\nv_mul_f32 v243, v155, v155\nv_fma_f32 v240, v240, s70, v248\n"
"s_add_u32 m0, s60, 90112\ns_add_u32 s61, s67, 4718592\nbuffer_load_dwordx4 v249, s[40:43], s61 offen lds\nv_fma_f32 v241, v241, s70, v248\n"
"v_fma_f32 v242, v242, s70, v248\nv_fma_f32 v243, v243, s70, v248\nv_mul_f32 v240, v240, v152\nv_mul_f32 v241, v241, v153\nv_mul_f32 v242, v242, v154\n"
"v_mul_f32 v243, v243, v155\nv_mbcnt_lo_u32_b32 v254, -1, 0\nv_mul_u32_u24 v254, 0x2400, v254\ns_mul_i32 s74, s81, 9216\ns_add_u32 s74, s74, s62\n"
"s_lshl_b32 s75, s76, 7\ns_add_u32 s74, s74, s75\ns_mov_b64 exec, 0x1ff\nbuffer_load_dword v254, v254, s[40:43], s74 offen\ns_mov_b64 exec, -1\n"
"s_cbranch_vccnz 114f\ns_waitcnt vmcnt(8)\ns_barrier\n114:\nW 4\nM 160,176,96\nR 192,246,45056\nM 160,180,104\nR 194,246,45568\nM 164,176,100\n"
"R 196,246,49152\nM 164,180,108\nR 198,246,49664\nW 4\nM 168,184,96\nR 176,247,45056\nM 168,188,104\nR 178,247,45568\nM 172,184,100\nR 180,247,49152\n"
"M 172,188,108\nR 182,247,49664\nW 4\nM 160,192,112\nR 184,246,53248\nM 160,196,120\nR 186,246,53760\nM 164,192,116\nR 188,246,57344\nM 164,196,124\n"
"R 190,246,57856\nW 4\nM 168,176,112\nR 192,247,53248\nM 168,180,120\nR 194,247,53760\nM 172,176,116\nR 196,247,57344\nM 172,180,124\nR 198,247,57856\n"
"W 4\nM 160,184,128\nR 176,244,24576\nM 160,188,136\nR 178,244,25088\nM 164,184,132\nR 180,244,28672\nM 164,188,140\nR 182,244,29184\nW 4\n"
"M 168,192,128\nR 184,245,24576\nM 168,196,136\nR 186,245,25088\nM 172,192,132\nR 188,245,28672\nM 172,196,140\nR 190,245,29184\ns_cbranch_vccz 115f\n"
"s_waitcnt vmcnt(5)\ns_barrier\n115:\ns_add_u32 m0, s60, 0\ns_add_u32 s61, s67, 0\nbuffer_load_dwordx4 v249, s[44:47], s61 offen lds\ns_nop 2\n"
"v_mul_f32 v144, v144, v160\nv_mul_f32 v145, v145, v161\nv_mul_f32 v146, v146, v162\nv_mul_f32 v147, v147, v163\nv_mul_f32 v148, v148, v164\n"
"v_mul_f32 v149, v149, v165\nv_mul_f32 v150, v150, v166\ns_add_u32 m0, s60, 8192\ns_add_u32 s61, s67, 589824\n"
"buffer_load_dwordx4 v249, s[44:47], s61 offen lds\nv_mul_f32 v151, v151, v167\nv_cvt_pk_bf16_f32 v224, v144, v145\nv_cvt_pk_bf16_f32 v225, v146, v147\n"
"v_cvt_pk_bf16_f32 v228, v148, v149\nv_cvt_pk_bf16_f32 v229, v150, v151\nE 240\nE 241\nE 242\ns_add_u32 m0, s60, 16384\ns_add_u32 s61, s67, 1179648\n"
"buffer_load_dwordx4 v249, s[44:47], s61 offen lds\nE 243\nv_add_f32 v240, 1.0, v240\nv_add_f32 v241, 1.0, v241\nv_add_f32 v242, 1.0, v242\n"
"v_add_f32 v243, 1.0, v243\nv_rcp_f32 v240, v240\nv_rcp_f32 v241, v241\nv_rcp_f32 v242, v242\ns_cbranch_vccnz 116f\ns_waitcnt vmcnt(8)\ns_barrier\n116:\n"
"W 4\nZ 144,176,0\nR 192,244,32768\nM 144,180,8\nR 194,244,33280\nZ 148,176,4\nR 196,244,36864\nM 148,180,12\nR 198,244,37376\nW 4\nZ 160,184,0\n"
"R 176,245,32768\nM 160,188,8\nR 178,245,33280\nZ 164,184,4\nR 180,245,36864\nM 164,188,12\nR 182,245,37376\nW 4\nM 144,192,16\nR 184,244,40960\n"
"M 144,196,24\nR 186,244,41472\nM 148,192,20\nR 188,244,45056\nM 148,196,28\nR 190,244,45568\nW 4\nM 160,176,16\nR 192,245,40960\nM 160,180,24\n"
"R 194,245,41472\nM 164,176,20\nR 196,245,45056\nM 164,180,28\nR 198,245,45568\nW 4\nM 144,184,32\nR 176,244,49152\nM 144,188,40\nR 178,244,49664\n"
"M 148,184,36\nR 180,244,53248\nM 148,188,44\nR 182,244,53760\nW 4\nM 160,192,32\nR 184,245,49152\nM 160,196,40\nR 186,245,49664\nM 164,192,36\n"
"R 188,245,53248\nM 164,196,44\nR 190,245,53760\ns_cbranch_vccz 117f\ns_waitcnt vmcnt(5)\ns_barrier\n117:\ns_add_u32 m0, s60, 98304\n"
"s_add_u32 s61, s67, 1769472\nbuffer_load_dwordx4 v249, s[44:47], s61 offen lds\nv_rcp_f32 v243, v243\nv_mul_f32 v152, v240, v152\n"
"v_mul_f32 v153, v241, v153\nv_mul_f32 v154, v242, v154\nv_mul_f32 v155, v243, v155\nv_mul_f32 v240, v156, v156\nv_mul_f32 v241, v157, v157\n"
"v_mul_f32 v242, v158, v158\ns_add_u32 m0, s60, 106496\ns_add_u32 s61, s67, 2359296\nbuffer_load_dwordx4 v249, s[44:47], s61 offen lds\n"
"v_mul_f32 v243, v159, v159\nv_fma_f32 v240, v240, s70, v248\nv_fma_f32 v241, v241, s70, v248\nv_fma_f32 v242, v242, s70, v248\n"
"v_fma_f32 v243, v243, s70, v248\nv_mul_f32 v240, v240, v156\nv_mul_f32 v241, v241, v157\nv_mul_f32 v242, v242, v158\ns_add_u32 m0, s60, 114688\n"
"s_add_u32 s61, s67, 2949120\nbuffer_load_dwordx4 v249, s[44:47], s61 offen lds\nv_mul_f32 v243, v243, v159\nE 240\nE 241\nE 242\nE 243\n"
"v_add_f32 v240, 1.0, v240\nv_add_f32 v241, 1.0, v241\nv_mbcnt_lo_u32_b32 v254, -1, 0\nv_mul_u32_u24 v254, 0x2400, v254\ns_mul_i32 s74, s81, 9216\n"
"s_add_u32 s74, s74, s62\ns_lshl_b32 s75, s76, 7\ns_add_u32 s74, s74, s75\ns_mov_b64 exec, 0x1ff\nbuffer_load_dword v254, v254, s[44:47], s74 offen\n"
"s_mov_b64 exec, -1\ns_cbranch_vccnz 118f\ns_waitcnt vmcnt(9)\ns_barrier\n118:\nW 4\nM 144,176,48\nR 192,244,57344\nM 144,180,56\nR 194,244,57856\n"
"M 148,176,52\nR 196,244,61440\nM 148,180,60\nR 198,244,61952\nW 4\nM 160,184,48\nR 176,245,57344\nM 160,188,56\nR 178,245,57856\nM 164,184,52\n"
"R 180,245,61440\nM 164,188,60\nR 182,245,61952\nW 4\nM 144,192,64\nR 184,246,4096\nM 144,196,72\nR 186,246,4608\nM 148,192,68\nR 188,246,8192\n"
"M 148,196,76\nR 190,246,8704\nW 4\nM 160,176,64\nR 192,247,4096\nM 160,180,72\nR 194,247,4608\nM 164,176,68\nR 196,247,8192\nM 164,180,76\n"
"R 198,247,8704\nW 4\nM 144,184,80\nR 176,246,12288\nM 144,188,88\nR 178,246,12800\nM 148,184,84\nR 180,246,16384\nM 148,188,92\nR 182,246,16896\nW 4\n"
"M 160,192,80\nR 184,247,12288\nM 160,196,88\nR 186,247,12800\nM 164,192,84\nR 188,247,16384\nM 164,196,92\nR 190,247,16896\ns_cbranch_vccz 119f\n"
"s_waitcnt vmcnt(5)\ns_barrier\n119:\ns_add_u32 m0, s60, 24576\ns_add_u32 s61, s67, 3538944\nbuffer_load_dwordx4 v249, s[44:47], s61 offen lds\n"
"v_add_f32 v242, 1.0, v242\nv_add_f32 v243, 1.0, v243\nv_rcp_f32 v240, v240\nv_rcp_f32 v241, v241\nv_rcp_f32 v242, v242\nv_rcp_f32 v243, v243\n"
"v_mul_f32 v156, v240, v156\ns_add_u32 m0, s60, 32768\ns_add_u32 s61, s67, 4128768\nbuffer_load_dwordx4 v249, s[44:47], s61 offen lds\n"
"v_mul_f32 v157, v241, v157\nv_mul_f32 v158, v242, v158\nv_mul_f32 v159, v243, v159\nv_mul_f32 v152, v152, v168\nv_mul_f32 v153, v153, v169\n"
"v_mul_f32 v154, v154, v170\nv_mul_f32 v155, v155, v171\ns_add_u32 m0, s60, 40960\ns_add_u32 s61, s67, 4718592\n"
"buffer_load_dwordx4 v249, s[44:47], s61 offen lds\nv_mul_f32 v156, v156, v172\nv_mul_f32 v157, v157, v173\nv_mul_f32 v158, v158, v174\n"
"v_mul_f32 v159, v159, v175\nv_cvt_pk_bf16_f32 v226, v152, v153\nv_cvt_pk_bf16_f32 v227, v154, v155\nv_cvt_pk_bf16_f32 v230, v156, v157\n"
"v_cvt_pk_bf16_f32 v231, v158, v159\ns_cbranch_vccnz 120f\ns_waitcnt vmcnt(8)\ns_barrier\n120:\nW 4\nM 144,176,96\nR 192,246,20480\nM 144,180,104\n"
"R 194,246,20992\nM 148,176,100\nR 196,246,24576\nM 148,180,108\nR 198,246,25088\nW 4\nM 160,184,96\nR 176,247,20480\nM 160,188,104\nR 178,247,20992\n"
"M 164,184,100\nR 180,247,24576\nM 164,188,108\nR 182,247,25088\nW 4\nM 144,192,112\nR 184,246,28672\nM 144,196,120\nR 186,246,29184\nM 148,192,116\n"
"R 188,246,32768\nM 148,196,124\nR 190,246,33280\nW 4\nM 160,176,112\nR 192,247,28672\nM 160,180,120\nR 194,247,29184\nM 164,176,116\nR 196,247,32768\n"
"M 164,180,124\nR 198,247,33280\nW 4\nM 144,184,128\nR 176,244,0\nM 144,188,136\nR 178,244,512\nM 148,184,132\nR 180,244,4096\nM 148,188,140\n"
"R 182,244,4608\nW 4\nM 160,192,128\nR 184,245,0\nM 160,196,136\nR 186,245,512\nM 164,192,132\nR 188,245,4096\nM 164,196,140\nR 190,245,4608\n"
"s_cbranch_vccz 121f\ns_waitcnt vmcnt(5)\ns_barrier\n121:\ns_add_u32 s77, s77, 1\ns_cmp_eq_u32 s77, 4\ns_cselect_b32 s77, 0, s77\n"
"s_add_u32 s67, s76, s77\ns_add_u32 s67, s67, 1\ns_cmp_ge_u32 s67, 9\ns_cselect_b32 s61, 9, 0\ns_sub_u32 s67, s67, s61\ns_mul_i32 s92, s67, 73728\n"
"s_lshl_b32 s67, s67, 7\ns_add_u32 s67, s67, s62\ns_add_u32 m0, s60, 49152\ns_add_u32 s61, s67, 0\nbuffer_load_dwordx4 v249, s[40:43], s61 offen lds\n"
"v_mul_f32 v240, v144, v144\nv_mul_f32 v241, v145, v145\nv_mul_f32 v242, v146, v146\nv_mul_f32 v243, v147, v147\nv_fma_f32 v240, v240, s70, v248\n"
"v_fma_f32 v241, v241, s70, v248\nv_fma_f32 v242, v242, s70, v248\ns_add_u32 m0, s60, 57344\ns_add_u32 s61, s67, 589824\n"
"buffer_load_dwordx4 v249, s[40:43], s61 offen lds\nv_fma_f32 v243, v243, s70, v248\nv_mul_f32 v240, v240, v144\nv_mul_f32 v241, v241, v145\n"
"v_mul_f32 v242, v242, v146\nv_mul_f32 v243, v243, v147\nE 240\nE 241\nE 242\ns_add_u32 m0, s60, 65536\ns_add_u32 s61, s67, 1179648\n"
"buffer_load_dwordx4 v249, s[40:43], s61 offen lds\nE 243\nv_add_f32 v240, 1.0, v240\nv_add_f32 v241, 1.0, v241\nv_add_f32 v242, 1.0, v242\n"
"v_add_f32 v243, 1.0, v243\nv_rcp_f32 v240, v240\nv_rcp_f32 v241, v241\nv_rcp_f32 v242, v242\ns_cbranch_vccnz 122f\ns_waitcnt vmcnt(8)\ns_barrier\n122:\n"
"W 4\nZ 152,176,0\nR 192,244,8192\nM 152,180,8\nR 194,244,8704\nZ 156,176,4\nR 196,244,12288\nM 156,180,12\nR 198,244,12800\nW 4\nZ 168,184,0\n"
"R 176,245,8192\nM 168,188,8\nR 178,245,8704\nZ 172,184,4\nR 180,245,12288\nM 172,188,12\nR 182,245,12800\nW 4\nM 152,192,16\nR 184,244,16384\n"
"M 152,196,24\nR 186,244,16896\nM 156,192,20\nR 188,244,20480\nM 156,196,28\nR 190,244,20992\nW 4\nM 168,176,16\nR 192,245,16384\nM 168,180,24\n"
"R 194,245,16896\nM 172,176,20\nR 196,245,20480\nM 172,180,28\nR 198,245,20992\nW 4\nM 152,184,32\nR 176,246,36864\nM 152,188,40\nR 178,246,37376\n"
"M 156,184,36\nR 180,246,40960\nM 156,188,44\nR 182,246,41472\nW 4\nM 168,192,32\nR 184,247,36864\nM 168,196,40\nR 186,247,37376\nM 172,192,36\n"
"R 188,247,40960\nM 172,196,44\nR 190,247,41472\ns_cbranch_vccz 123f\ns_waitcnt vmcnt(4)\ns_barrier\n123:\ns_add_u32 m0, s60, 73728\n"
"s_add_u32 s61, s67, 1769472\nbuffer_load_dwordx4 v249, s[40:43], s61 offen lds\nv_rcp_f32 v243, v243\nv_mul_f32 v144, v240, v144\n"
"v_mul_f32 v145, v241, v145\nv_mul_f32 v146, v242, v146\nv_mul_f32 v147, v243, v147\nv_mul_f32 v240, v148, v148\nv_mul_f32 v241, v149, v149\n"
"s_add_u32 m0, s60, 81920\ns_add_u32 s61, s67, 2359296\nbuffer_load_dwordx4 v249, s[40:43], s61 offen lds\nv_mul_f32 v242, v150, v150\n"
"v_mul_f32 v243, v151, v151\nv_fma_f32 v240, v240, s70, v248\nv_fma_f32 v241, v241, s70, v248\nv_fma_f32 v242, v242, s70, v248\n"
"v_fma_f32 v243, v243, s70, v248\nv_mul_f32 v240, v240, v148\nv_mul_f32 v241, v241, v149\ns_add_u32 m0, s60, 90112\ns_add_u32 s61, s67, 2949120\n"
"buffer_load_dwordx4 v249, s[40:43], s61 offen lds\nv_mul_f32 v242, v242, v150\nv_mul_f32 v243, v243, v151\nE 240\nE 241\nE 242\nE 243\n"
"v_add_f32 v240, 1.0, v240\nv_add_f32 v241, 1.0, v241\ns_cbranch_vccnz 124f\ns_waitcnt vmcnt(7)\ns_barrier\n124:\nW 4\nM 152,176,48\nR 192,246,45056\n"
"M 152,180,56\nR 194,246,45568\nM 156,176,52\nR 196,246,49152\nM 156,180,60\nR 198,246,49664\nW 4\nM 168,184,48\nR 176,247,45056\nM 168,188,56\n"
"R 178,247,45568\nM 172,184,52\nR 180,247,49152\nM 172,188,60\nR 182,247,49664\nW 4\nM 152,192,64\nR 184,246,53248\nM 152,196,72\nR 186,246,53760\n"
"M 156,192,68\nR 188,246,57344\nM 156,196,76\nR 190,246,57856\nW 4\nM 168,176,64\nR 192,247,53248\nM 168,180,72\nR 194,247,53760\nM 172,176,68\n"
"R 196,247,57344\nM 172,180,76\nR 198,247,57856\nW 4\nM 152,184,80\nR 176,244,24576\nM 152,188,88\nR 178,244,25088\nM 156,184,84\nR 180,244,28672\n"
"M 156,188,92\nR 182,244,29184\nW 4\nM 168,192,80\nR 184,245,24576\nM 168,196,88\nR 186,245,25088\nM 172,192,84\nR 188,245,28672\nM 172,196,92\n"
"R 190,245,29184\ns_cbranch_vccz 125f\ns_waitcnt vmcnt(4)\ns_barrier\n125:\ns_add_u32 m0, s60, 0\ns_add_u32 s61, s67, 3538944\n"
"buffer_load_dwordx4 v249, s[40:43], s61 offen lds\nv_add_f32 v242, 1.0, v242\nv_add_f32 v243, 1.0, v243\nv_rcp_f32 v240, v240\nv_rcp_f32 v241, v241\n"
"v_rcp_f32 v242, v242\nv_rcp_f32 v243, v243\nv_mul_f32 v148, v240, v148\ns_add_u32 m0, s60, 8192\ns_add_u32 s61, s67, 4128768\n"
"buffer_load_dwordx4 v249, s[40:43], s61 offen lds\nv_mul_f32 v149, v241, v149\nv_mul_f32 v150, v242, v150\nv_mul_f32 v151, v243, v151\n"
"v_mul_f32 v240, v160, v160\nv_mul_f32 v241, v161, v161\nv_mul_f32 v242, v162, v162\nv_mul_f32 v243, v163, v163\ns_add_u32 m0, s60, 16384\n"
"s_add_u32 s61, s67, 4718592\nbuffer_load_dwordx4 v249, s[40:43], s61 offen lds\nv_fma_f32 v240, v240, s70, v248\nv_fma_f32 v241, v241, s70, v248\n"
"v_fma_f32 v242, v242, s70, v248\nv_fma_f32 v243, v243, s70, v248\nv_mul_f32 v240, v240, v160\nv_mul_f32 v241, v241, v161\nv_mul_f32 v242, v242, v162\n"
"v_mul_f32 v243, v243, v163\ns_cbranch_vccnz 126f\ns_waitcnt vmcnt(7)\ns_barrier\n126:\nW 4\nM 152,176,96\nR 192,244,32768\nM 152,180,104\n"
"R 194,244,33280\nM 156,176,100\nR 196,244,36864\nM 156,180,108\nR 198,244,37376\nW 4\nM 168,184,96\nR 176,245,32768\nM 168,188,104\nR 178,245,33280\n"
"M 172,184,100\nR 180,245,36864\nM 172,188,108\nR 182,245,37376\nW 4\nM 152,192,112\nR 184,244,40960\nM 152,196,120\nR 186,244,41472\nM 156,192,116\n"
"R 188,244,45056\nM 156,196,124\nR 190,244,45568\nW 4\nM 168,176,112\nR 192,245,40960\nM 168,180,120\nR 194,245,41472\nM 172,176,116\nR 196,245,45056\n"
"M 172,180,124\nR 198,245,45568\nW 4\nM 152,184,128\nR 176,244,49152\nM 152,188,136\nR 178,244,49664\nM 156,184,132\nR 180,244,53248\nM 156,188,140\n"
"R 182,244,53760\nW 4\nM 168,192,128\nR 184,245,49152\nM 168,196,136\nR 186,245,49664\nM 172,192,132\nR 188,245,53248\nM 172,196,140\nR 190,245,53760\n"
"s_cbranch_vccz 127f\ns_waitcnt vmcnt(4)\ns_barrier\n127:\ns_add_u32 m0, s60, 98304\ns_add_u32 s61, s67, 0\n"
"buffer_load_dwordx4 v249, s[44:47], s61 offen lds\ns_nop 2\nv_mul_f32 v144, v144, v152\nv_mul_f32 v145, v145, v153\nv_mul_f32 v146, v146, v154\n"
"v_mul_f32 v147, v147, v155\nv_mul_f32 v148, v148, v156\nv_mul_f32 v149, v149, v157\nv_mul_f32 v150, v150, v158\ns_add_u32 m0, s60, 106496\n"
"s_add_u32 s61, s67, 589824\nbuffer_load_dwordx4 v249, s[44:47], s61 offen lds\nv_mul_f32 v151, v151, v159\nv_cvt_pk_bf16_f32 v232, v144, v145\n"
"v_cvt_pk_bf16_f32 v233, v146, v147\nv_cvt_pk_bf16_f32 v236, v148, v149\nv_cvt_pk_bf16_f32 v237, v150, v151\nE 240\nE 241\nE 242\n"
"s_add_u32 m0, s60, 114688\ns_add_u32 s61, s67, 1179648\nbuffer_load_dwordx4 v249, s[44:47], s61 offen lds\nE 243\nv_add_f32 v240, 1.0, v240\n"
"v_add_f32 v241, 1.0, v241\nv_add_f32 v242, 1.0, v242\nv_add_f32 v243, 1.0, v243\nv_rcp_f32 v240, v240\nv_rcp_f32 v241, v241\nv_rcp_f32 v242, v242\n"
"s_cbranch_vccnz 128f\ns_waitcnt vmcnt(7)\ns_barrier\n128:\nW 4\nZ 144,176,0\nR 192,244,57344\nM 144,180,8\nR 194,244,57856\nZ 148,176,4\n"
"R 196,244,61440\nM 148,180,12\nR 198,244,61952\nW 4\nZ 152,184,0\nR 176,245,57344\nM 152,188,8\nR 178,245,57856\nZ 156,184,4\nR 180,245,61440\n"
"M 156,188,12\nR 182,245,61952\nW 4\nM 144,192,16\nR 184,246,4096\nM 144,196,24\nR 186,246,4608\nM 148,192,20\nR 188,246,8192\nM 148,196,28\n"
"R 190,246,8704\nW 4\nM 152,176,16\nR 192,247,4096\nM 152,180,24\nR 194,247,4608\nM 156,176,20\nR 196,247,8192\nM 156,180,28\nR 198,247,8704\nW 4\n"
"M 144,184,32\nR 176,246,12288\nM 144,188,40\nR 178,246,12800\nM 148,184,36\nR 180,246,16384\nM 148,188,44\nR 182,246,16896\nW 4\nM 152,192,32\n"
"R 184,247,12288\nM 152,196,40\nR 186,247,12800\nM 156,192,36\nR 188,247,16384\nM 156,196,44\nR 190,247,16896\ns_cbranch_vccz 129f\ns_waitcnt vmcnt(4)\n"
"s_barrier\n129:\ns_add_u32 m0, s60, 24576\ns_add_u32 s61, s67, 1769472\nbuffer_load_dwordx4 v249, s[44:47], s61 offen lds\nv_rcp_f32 v243, v243\n"
"v_mul_f32 v160, v240, v160\nv_mul_f32 v161, v241, v161\nv_mul_f32 v162, v242, v162\nv_mul_f32 v163, v243, v163\nv_mul_f32 v240, v164, v164\n"
"v_mul_f32 v241, v165, v165\ns_add_u32 m0, s60, 32768\ns_add_u32 s61, s67, 2359296\nbuffer_load_dwordx4 v249, s[44:47], s61 offen lds\n"
"v_mul_f32 v242, v166, v166\nv_mul_f32 v243, v167, v167\nv_fma_f32 v240, v240, s70, v248\nv_fma_f32 v241, v241, s70, v248\n"
"v_fma_f32 v242, v242, s70, v248\nv_fma_f32 v243, v243, s70, v248\nv_mul_f32 v240, v240, v164\nv_mul_f32 v241, v241, v165\ns_add_u32 m0, s60, 40960\n"
"s_add_u32 s61, s67, 2949120\nbuffer_load_dwordx4 v249, s[44:47], s61 offen lds\nv_mul_f32 v242, v242, v166\nv_mul_f32 v243, v243, v167\nE 240\nE 241\n"
"E 242\nE 243\nv_add_f32 v240, 1.0, v240\nv_add_f32 v241, 1.0, v241\ns_cbranch_vccnz 130f\ns_waitcnt vmcnt(7)\ns_barrier\n130:\nW 4\nM 144,176,48\n"
"R 192,246,20480\nM 144,180,56\nR 194,246,20992\nM 148,176,52\nR 196,246,24576\nM 148,180,60\nR 198,246,25088\nW 4\nM 152,184,48\nR 176,247,20480\n"
"M 152,188,56\nR 178,247,20992\nM 156,184,52\nR 180,247,24576\nM 156,188,60\nR 182,247,25088\nW 4\nM 144,192,64\nR 184,246,28672\nM 144,196,72\n"
"R 186,246,29184\nM 148,192,68\nR 188,246,32768\nM 148,196,76\nR 190,246,33280\nW 4\nM 152,176,64\nR 192,247,28672\nM 152,180,72\nR 194,247,29184\n"
"M 156,176,68\nR 196,247,32768\nM 156,180,76\nR 198,247,33280\nW 4\nM 144,184,80\nR 176,244,0\nM 144,188,88\nR 178,244,512\nM 148,184,84\n"
"R 180,244,4096\nM 148,188,92\nR 182,244,4608\nW 4\nM 152,192,80\nR 184,245,0\nM 152,196,88\nR 186,245,512\nM 156,192,84\nR 188,245,4096\nM 156,196,92\n"
"R 190,245,4608\ns_cbranch_vccz 131f\ns_waitcnt vmcnt(4)\ns_barrier\n131:\ns_add_u32 m0, s60, 49152\ns_add_u32 s61, s67, 3538944\n"
"buffer_load_dwordx4 v249, s[44:47], s61 offen lds\nv_add_f32 v242, 1.0, v242\nv_add_f32 v243, 1.0, v243\nv_rcp_f32 v240, v240\nv_rcp_f32 v241, v241\n"
"v_rcp_f32 v242, v242\nv_rcp_f32 v243, v243\nv_mul_f32 v164, v240, v164\ns_add_u32 m0, s60, 57344\ns_add_u32 s61, s67, 4128768\n"
"buffer_load_dwordx4 v249, s[44:47], s61 offen lds\nv_mul_f32 v165, v241, v165\nv_mul_f32 v166, v242, v166\nv_mul_f32 v167, v243, v167\n"
"v_mul_f32 v160, v160, v168\nv_mul_f32 v161, v161, v169\nv_mul_f32 v162, v162, v170\nv_mul_f32 v163, v163, v171\ns_add_u32 m0, s60, 65536\n"
"s_add_u32 s61, s67, 4718592\nbuffer_load_dwordx4 v249, s[44:47], s61 offen lds\nv_mul_f32 v164, v164, v172\nv_mul_f32 v165, v165, v173\n"
"v_mul_f32 v166, v166, v174\nv_mul_f32 v167, v167, v175\nv_cvt_pk_bf16_f32 v234, v160, v161\nv_cvt_pk_bf16_f32 v235, v162, v163\n"
"v_cvt_pk_bf16_f32 v238, v164, v165\nv_cvt_pk_bf16_f32 v239, v166, v167\ns_cbranch_vccnz 132f\ns_waitcnt vmcnt(7)\ns_barrier\n132:\nW 4\nM 144,176,96\n"
"R 192,244,8192\nM 144,180,104\nR 194,244,8704\nM 148,176,100\nR 196,244,12288\nM 148,180,108\nR 198,244,12800\nW 4\nM 152,184,96\nR 176,245,8192\n"
"M 152,188,104\nR 178,245,8704\nM 156,184,100\nR 180,245,12288\nM 156,188,108\nR 182,245,12800\nW 4\nM 144,192,112\nR 184,244,16384\nM 144,196,120\n"
"R 186,244,16896\nM 148,192,116\nR 188,244,20480\nM 148,196,124\nR 190,244,20992\nW 4\nM 152,176,112\nR 192,245,16384\nM 152,180,120\nR 194,245,16896\n"
"M 156,176,116\nR 196,245,20480\nM 156,180,124\nR 198,245,20992\nW 4\nM 144,184,128\nR 176,246,36864\nM 144,188,136\nR 178,246,37376\nM 148,184,132\n"
"R 180,246,40960\nM 148,188,140\nR 182,246,41472\nW 4\nM 152,192,128\nR 184,247,36864\nM 152,196,136\nR 186,247,37376\nM 156,192,132\nR 188,247,40960\n"
"M 156,196,140\nR 190,247,41472\ns_cbranch_vccz 133f\ns_waitcnt vmcnt(4)\ns_barrier\nds_write_b128 v251, v[224:227] offset:32768\n"
"ds_write_b128 v251, v[228:231] offset:33792\nds_write_b128 v251, v[232:235] offset:49152\nds_write_b128 v251, v[236:239] offset:50176\n133:\n"
"s_add_u32 s77, s77, 1\ns_cmp_eq_u32 s77, 4\ns_cselect_b32 s77, 0, s77\ns_add_u32 s67, s76, s77\ns_add_u32 s67, s67, 1\ns_cmp_ge_u32 s67, 9\n"
"s_cselect_b32 s61, 9, 0\ns_sub_u32 s67, s67, s61\ns_mul_i32 s93, s67, 73728\ns_lshl_b32 s67, s67, 7\ns_add_u32 s67, s67, s62\ns_add_u32 m0, s60, 73728\n"
"s_add_u32 s61, s67, 0\nbuffer_load_dwordx4 v249, s[40:43], s61 offen lds\nv_mul_f32 v240, v144, v144\nv_mul_f32 v241, v145, v145\n"
"v_mul_f32 v242, v146, v146\nv_mul_f32 v243, v147, v147\nv_fma_f32 v240, v240, s70, v248\nv_fma_f32 v241, v241, s70, v248\n"
"v_fma_f32 v242, v242, s70, v248\ns_add_u32 m0, s60, 81920\ns_add_u32 s61, s67, 589824\nbuffer_load_dwordx4 v249, s[40:43], s61 offen lds\n"
"v_fma_f32 v243, v243, s70, v248\nv_mul_f32 v240, v240, v144\nv_mul_f32 v241, v241, v145\nv_mul_f32 v242, v242, v146\nv_mul_f32 v243, v243, v147\nE 240\n"
"E 241\nE 242\ns_add_u32 m0, s60, 90112\ns_add_u32 s61, s67, 1179648\nbuffer_load_dwordx4 v249, s[40:43], s61 offen lds\nE 243\n"
"v_add_f32 v240, 1.0, v240\nv_add_f32 v241, 1.0, v241\nv_add_f32 v242, 1.0, v242\nv_add_f32 v243, 1.0, v243\nv_rcp_f32 v240, v240\nv_rcp_f32 v241, v241\n"
"v_rcp_f32 v242, v242\ns_cbranch_vccnz 134f\ns_waitcnt vmcnt(7)\ns_barrier\nds_write_b128 v251, v[224:227] offset:32768\n"
"ds_write_b128 v251, v[228:231] offset:33792\nds_write_b128 v251, v[232:235] offset:49152\nds_write_b128 v251, v[236:239] offset:50176\n134:\nW 4\n"
"Z 160,176,0\nR 192,246,45056\nM 160,180,8\nR 194,246,45568\nZ 164,176,4\nR 196,246,49152\nM 164,180,12\nR 198,246,49664\nW 4\nZ 168,184,0\n"
"R 176,247,45056\nM 168,188,8\nR 178,247,45568\nZ 172,184,4\nR 180,247,49152\nM 172,188,12\nR 182,247,49664\nW 4\nM 160,192,16\nR 184,246,53248\n"
"M 160,196,24\nR 186,246,53760\nM 164,192,20\nR 188,246,57344\nM 164,196,28\nR 190,246,57856\nW 4\nM 168,176,16\nR 192,247,53248\nM 168,180,24\n"
"R 194,247,53760\nM 172,176,20\nR 196,247,57344\nM 172,180,28\nR 198,247,57856\nW 4\nM 160,184,32\nR 176,244,24576\nM 160,188,40\nR 178,244,25088\n"
"M 164,184,36\nR 180,244,28672\nM 164,188,44\nR 182,244,29184\nW 4\nM 168,192,32\nR 184,245,24576\nM 168,196,40\nR 186,245,25088\nM 172,192,36\n"
"R 188,245,28672\nM 172,196,44\nR 190,245,29184\ns_cbranch_vccz 135f\ns_waitcnt vmcnt(4)\ns_barrier\n135:\ns_add_u32 m0, s60, 0\n"
"s_add_u32 s61, s67, 1769472\nbuffer_load_dwordx4 v249, s[40:43], s61 offen lds\nv_rcp_f32 v243, v243\nv_mul_f32 v144, v240, v144\n"
"v_mul_f32 v145, v241, v145\nv_mul_f32 v146, v242, v146\nv_mul_f32 v147, v243, v147\nv_mul_f32 v240, v148, v148\nv_mul_f32 v241, v149, v149\n"
"s_add_u32 m0, s60, 8192\ns_add_u32 s61, s67, 2359296\nbuffer_load_dwordx4 v249, s[40:43], s61 offen lds\nv_mul_f32 v242, v150, v150\n"
"v_mul_f32 v243, v151, v151\nv_fma_f32 v240, v240, s70, v248\nv_fma_f32 v241, v241, s70, v248\nv_fma_f32 v242, v242, s70, v248\n"
"v_fma_f32 v243, v243, s70, v248\nv_mul_f32 v240, v240, v148\nv_mul_f32 v241, v241, v149\ns_add_u32 m0, s60, 16384\ns_add_u32 s61, s67, 2949120\n"
"buffer_load_dwordx4 v249, s[40:43], s61 offen lds\nv_mul_f32 v242, v242, v150\nv_mul_f32 v243, v243, v151\nE 240\nE 241\nE 242\nE 243\n"
"v_add_f32 v240, 1.0, v240\nv_add_f32 v241, 1.0, v241\ns_cbranch_vccnz 136f\ns_waitcnt vmcnt(7)\ns_barrier\n136:\nW 4\nM 160,176,48\nR 192,244,32768\n"
"M 160,180,56\nR 194,244,33280\nM 164,176,52\nR 196,244,36864\nM 164,180,60\nR 198,244,37376\nW 4\nM 168,184,48\nR 176,245,32768\nM 168,188,56\n"
"R 178,245,33280\nM 172,184,52\nR 180,245,36864\nM 172,188,60\nR 182,245,37376\nW 4\nM 160,192,64\nR 184,244,40960\nM 160,196,72\nR 186,244,41472\n"
"M 164,192,68\nR 188,244,45056\nM 164,196,76\nR 190,244,45568\nW 4\nM 168,176,64\nR 192,245,40960\nM 168,180,72\nR 194,245,41472\nM 172,176,68\n"
"R 196,245,45056\nM 172,180,76\nR 198,245,45568\nW 4\nM 160,184,80\nR 176,244,49152\nM 160,188,88\nR 178,244,49664\nM 164,184,84\nR 180,244,53248\n"
"M 164,188,92\nR 182,244,53760\nW 4\nM 168,192,80\nR 184,245,49152\nM 168,196,88\nR 186,245,49664\nM 172,192,84\nR 188,245,53248\nM 172,196,92\n"
"R 190,245,53760\ns_cbranch_vccz 137f\ns_waitcnt vmcnt(4)\ns_barrier\n137:\ns_add_u32 m0, s60, 98304\ns_add_u32 s61, s67, 3538944\n"
"buffer_load_dwordx4 v249, s[40:43], s61 offen lds\nv_add_f32 v242, 1.0, v242\nv_add_f32 v243, 1.0, v243\nv_rcp_f32 v240, v240\nv_rcp_f32 v241, v241\n"
"v_rcp_f32 v242, v242\nv_rcp_f32 v243, v243\nv_mul_f32 v148, v240, v148\ns_add_u32 m0, s60, 106496\ns_add_u32 s61, s67, 4128768\n"
"buffer_load_dwordx4 v249, s[40:43], s61 offen lds\nv_mul_f32 v149, v241, v149\nv_mul_f32 v150, v242, v150\nv_mul_f32 v151, v243, v151\n"
"v_mul_f32 v240, v152, v152\nv_mul_f32 v241, v153, v153\nv_mul_f32 v242, v154, v154\nv_mul_f32 v243, v155, v155\ns_add_u32 m0, s60, 114688\n"
"s_add_u32 s61, s67, 4718592\nbuffer_load_dwordx4 v249, s[40:43], s61 offen lds\nv_fma_f32 v240, v240, s70, v248\nv_fma_f32 v241, v241, s70, v248\n"
"v_fma_f32 v242, v242, s70, v248\nv_fma_f32 v243, v243, s70, v248\nv_mul_f32 v240, v240, v152\nv_mul_f32 v241, v241, v153\nv_mul_f32 v242, v242, v154\n"
"v_mul_f32 v243, v243, v155\ns_cbranch_vccnz 138f\ns_waitcnt vmcnt(7)\ns_barrier\n138:\nW 4\nM 160,176,96\nR 192,244,57344\nM 160,180,104\n"
"R 194,244,57856\nM 164,176,100\nR 196,244,61440\nM 164,180,108\nR 198,244,61952\nW 4\nM 168,184,96\nR 176,245,57344\nM 168,188,104\nR 178,245,57856\n"
"M 172,184,100\nR 180,245,61440\nM 172,188,108\nR 182,245,61952\nW 4\nM 160,192,112\nR 184,246,4096\nM 160,196,120\nR 186,246,4608\nM 164,192,116\n"
"R 188,246,8192\nM 164,196,124\nR 190,246,8704\nW 4\nM 168,176,112\nR 192,247,4096\nM 168,180,120\nR 194,247,4608\nM 172,176,116\nR 196,247,8192\n"
"M 172,180,124\nR 198,247,8704\nW 4\nM 160,184,128\nR 176,246,12288\nM 160,188,136\nR 178,246,12800\nM 164,184,132\nR 180,246,16384\nM 164,188,140\n"
"R 182,246,16896\nW 4\nM 168,192,128\nR 184,247,12288\nM 168,196,136\nR 186,247,12800\nM 172,192,132\nR 188,247,16384\nM 172,196,140\nR 190,247,16896\n"
"s_cbranch_vccz 139f\ns_waitcnt vmcnt(4)\ns_barrier\n139:\ns_add_u32 m0, s60, 24576\ns_add_u32 s61, s67, 0\n"
"buffer_load_dwordx4 v249, s[44:47], s61 offen lds\ns_nop 2\nv_mul_f32 v144, v144, v160\nv_mul_f32 v145, v145, v161\nv_mul_f32 v146, v146, v162\n"
"v_mul_f32 v147, v147, v163\nv_mul_f32 v148, v148, v164\nv_mul_f32 v149, v149, v165\nv_mul_f32 v150, v150, v166\ns_add_u32 m0, s60, 32768\n"
"s_add_u32 s61, s67, 589824\nbuffer_load_dwordx4 v249, s[44:47], s61 offen lds\nv_mul_f32 v151, v151, v167\nv_cvt_pk_bf16_f32 v200, v144, v145\n"
"v_cvt_pk_bf16_f32 v201, v146, v147\nv_cvt_pk_bf16_f32 v204, v148, v149\nv_cvt_pk_bf16_f32 v205, v150, v151\nE 240\nE 241\nE 242\n"
"s_add_u32 m0, s60, 40960\ns_add_u32 s61, s67, 1179648\nbuffer_load_dwordx4 v249, s[44:47], s61 offen lds\nE 243\nv_add_f32 v240, 1.0, v240\n"
"v_add_f32 v241, 1.0, v241\nv_add_f32 v242, 1.0, v242\nv_add_f32 v243, 1.0, v243\nv_rcp_f32 v240, v240\nv_rcp_f32 v241, v241\nv_rcp_f32 v242, v242\n"
"s_cbranch_vccnz 140f\ns_waitcnt vmcnt(7)\ns_barrier\n140:\nW 4\nZ 144,176,0\nR 192,246,20480\nM 144,180,8\nR 194,246,20992\nZ 148,176,4\n"
"R 196,246,24576\nM 148,180,12\nR 198,246,25088\nW 4\nZ 160,184,0\nR 176,247,20480\nM 160,188,8\nR 178,247,20992\nZ 164,184,4\nR 180,247,24576\n"
"M 164,188,12\nR 182,247,25088\nW 4\nM 144,192,16\nR 184,246,28672\nM 144,196,24\nR 186,246,29184\nM 148,192,20\nR 188,246,32768\nM 148,196,28\n"
"R 190,246,33280\nW 4\nM 160,176,16\nR 192,247,28672\nM 160,180,24\nR 194,247,29184\nM 164,176,20\nR 196,247,32768\nM 164,180,28\nR 198,247,33280\nW 4\n"
"M 144,184,32\nR 176,244,0\nM 144,188,40\nR 178,244,512\nM 148,184,36\nR 180,244,4096\nM 148,188,44\nR 182,244,4608\nW 4\nM 160,192,32\nR 184,245,0\n"
"M 160,196,40\nR 186,245,512\nM 164,192,36\nR 188,245,4096\nM 164,196,44\nR 190,245,4608\ns_cbranch_vccz 141f\ns_waitcnt vmcnt(4)\ns_barrier\n141:\n"
"s_add_u32 m0, s60, 49152\ns_add_u32 s61, s67, 1769472\nbuffer_load_dwordx4 v249, s[44:47], s61 offen lds\nv_rcp_f32 v243, v243\n"
"v_mul_f32 v152, v240, v152\nv_mul_f32 v153, v241, v153\nv_mul_f32 v154, v242, v154\nv_mul_f32 v155, v243, v155\nv_mul_f32 v240, v156, v156\n"
"v_mul_f32 v241, v157, v157\ns_add_u32 m0, s60, 57344\ns_add_u32 s61, s67, 2359296\nbuffer_load_dwordx4 v249, s[44:47], s61 offen lds\n"
"v_mul_f32 v242, v158, v158\nv_mul_f32 v243, v159, v159\nv_fma_f32 v240, v240, s70, v248\nv_fma_f32 v241, v241, s70, v248\n"
"v_fma_f32 v242, v242, s70, v248\nv_fma_f32 v243, v243, s70, v248\nv_mul_f32 v240, v240, v156\nv_mul_f32 v241, v241, v157\ns_add_u32 m0, s60, 65536\n"
"s_add_u32 s61, s67, 2949120\nbuffer_load_dwordx4 v249, s[44:47], s61 offen lds\nv_mul_f32 v242, v242, v158\nv_mul_f32 v243, v243, v159\nE 240\nE 241\n"
"E 242\nE 243\nv_add_f32 v240, 1.0, v240\nv_add_f32 v241, 1.0, v241\ns_cbranch_vccnz 142f\ns_waitcnt vmcnt(7)\ns_barrier\n142:\nW 4\nM 144,176,48\n"
"R 192,244,8192\nM 144,180,56\nR 194,244,8704\nM 148,176,52\nR 196,244,12288\nM 148,180,60\nR 198,244,12800\nW 4\nM 160,184,48\nR 176,245,8192\n"
"M 160,188,56\nR 178,245,8704\nM 164,184,52\nR 180,245,12288\nM 164,188,60\nR 182,245,12800\nW 4\nM 144,192,64\nR 184,244,16384\nM 144,196,72\n"
"R 186,244,16896\nM 148,192,68\nR 188,244,20480\nM 148,196,76\nR 190,244,20992\nW 4\nM 160,176,64\nR 192,245,16384\nM 160,180,72\nR 194,245,16896\n"
"M 164,176,68\nR 196,245,20480\nM 164,180,76\nR 198,245,20992\nW 4\nM 144,184,80\nR 176,246,36864\nM 144,188,88\nR 178,246,37376\nM 148,184,84\n"
"R 180,246,40960\nM 148,188,92\nR 182,246,41472\nW 4\nM 160,192,80\nR 184,247,36864\nM 160,196,88\nR 186,247,37376\nM 164,192,84\nR 188,247,40960\n"
"M 164,196,92\nR 190,247,41472\ns_cbranch_vccz 143f\ns_waitcnt vmcnt(4)\ns_barrier\n143:\ns_add_u32 m0, s60, 73728\ns_add_u32 s61, s67, 3538944\n"
"buffer_load_dwordx4 v249, s[44:47], s61 offen lds\nv_add_f32 v242, 1.0, v242\nv_add_f32 v243, 1.0, v243\nv_rcp_f32 v240, v240\nv_rcp_f32 v241, v241\n"
"v_rcp_f32 v242, v242\nv_rcp_f32 v243, v243\nv_mul_f32 v156, v240, v156\ns_add_u32 m0, s60, 81920\ns_add_u32 s61, s67, 4128768\n"
"buffer_load_dwordx4 v249, s[44:47], s61 offen lds\nv_mul_f32 v157, v241, v157\nv_mul_f32 v158, v242, v158\nv_mul_f32 v159, v243, v159\n"
"v_mul_f32 v152, v152, v168\nv_mul_f32 v153, v153, v169\nv_mul_f32 v154, v154, v170\nv_mul_f32 v155, v155, v171\ns_add_u32 m0, s60, 90112\n"
"s_add_u32 s61, s67, 4718592\nbuffer_load_dwordx4 v249, s[44:47], s61 offen lds\nv_mul_f32 v156, v156, v172\nv_mul_f32 v157, v157, v173\n"
"v_mul_f32 v158, v158, v174\nv_mul_f32 v159, v159, v175\nv_cvt_pk_bf16_f32 v202, v152, v153\nv_cvt_pk_bf16_f32 v203, v154, v155\n"
"v_cvt_pk_bf16_f32 v206, v156, v157\nv_cvt_pk_bf16_f32 v207, v158, v159\ns_cbranch_vccnz 144f\ns_waitcnt vmcnt(7)\ns_barrier\n144:\nW 4\nM 144,176,96\n"
"R 192,246,45056\nM 144,180,104\nR 194,246,45568\nM 148,176,100\nR 196,246,49152\nM 148,180,108\nR 198,246,49664\nW 4\nM 160,184,96\nR 176,247,45056\n"
"M 160,188,104\nR 178,247,45568\nM 164,184,100\nR 180,247,49152\nM 164,188,108\nR 182,247,49664\nW 4\nM 144,192,112\nR 184,246,53248\nM 144,196,120\n"
"R 186,246,53760\nM 148,192,116\nR 188,246,57344\nM 148,196,124\nR 190,246,57856\nW 4\nM 160,176,112\nR 192,247,53248\nM 160,180,120\nR 194,247,53760\n"
"M 164,176,116\nR 196,247,57344\nM 164,180,124\nR 198,247,57856\nW 4\nM 144,184,128\nR 176,244,24576\nM 144,188,136\nR 178,244,25088\nM 148,184,132\n"
"R 180,244,28672\nM 148,188,140\nR 182,244,29184\nW 4\nM 160,192,128\nR 184,245,24576\nM 160,196,136\nR 186,245,25088\nM 164,192,132\nR 188,245,28672\n"
"M 164,196,140\nR 190,245,29184\ns_cbranch_vccz 145f\ns_waitcnt vmcnt(4)\ns_barrier\n145:\ns_add_u32 s67, s76, 5\ns_cmp_ge_u32 s67, 9\n"
"s_cselect_b32 s61, 9, 0\ns_sub_u32 s67, s67, s61\ns_mul_i32 s94, s67, 73728\ns_lshl_b32 s67, s67, 7\ns_add_u32 s67, s67, s62\ns_add_u32 m0, s60, 0\n"
"s_add_u32 s61, s67, 0\nbuffer_load_dwordx4 v249, s[40:43], s61 offen lds\nv_mul_f32 v240, v144, v144\nv_mul_f32 v241, v145, v145\n"
"v_mul_f32 v242, v146, v146\nv_mul_f32 v243, v147, v147\nv_fma_f32 v240, v240, s70, v248\nv_fma_f32 v241, v241, s70, v248\n"
"v_fma_f32 v242, v242, s70, v248\ns_add_u32 m0, s60, 8192\ns_add_u32 s61, s67, 589824\nbuffer_load_dwordx4 v249, s[40:43], s61 offen lds\n"
"v_fma_f32 v243, v243, s70, v248\nv_mul_f32 v240, v240, v144\nv_mul_f32 v241, v241, v145\nv_mul_f32 v242, v242, v146\nv_mul_f32 v243, v243, v147\nE 240\n"
"E 241\nE 242\ns_add_u32 m0, s60, 16384\ns_add_u32 s61, s67, 1179648\nbuffer_load_dwordx4 v249, s[40:43], s61 offen lds\nE 243\n"
"v_add_f32 v240, 1.0, v240\nv_add_f32 v241, 1.0, v241\nv_add_f32 v242, 1.0, v242\nv_add_f32 v243, 1.0, v243\nv_rcp_f32 v240, v240\nv_rcp_f32 v241, v241\n"
"v_rcp_f32 v242, v242\ns_cbranch_vccnz 146f\ns_waitcnt vmcnt(7)\ns_barrier\n146:\nW 4\nZ 152,176,0\nR 192,244,32768\nM 152,180,8\nR 194,244,33280\n"
"Z 156,176,4\nR 196,244,36864\nM 156,180,12\nR 198,244,37376\nW 4\nZ 168,184,0\nR 176,245,32768\nM 168,188,8\nR 178,245,33280\nZ 172,184,4\n"
"R 180,245,36864\nM 172,188,12\nR 182,245,37376\nW 4\nM 152,192,16\nR 184,244,40960\nM 152,196,24\nR 186,244,41472\nM 156,192,20\nR 188,244,45056\n"
"M 156,196,28\nR 190,244,45568\nW 4\nM 168,176,16\nR 192,245,40960\nM 168,180,24\nR 194,245,41472\nM 172,176,20\nR 196,245,45056\nM 172,180,28\n"
"R 198,245,45568\nW 4\nM 152,184,32\nR 176,244,49152\nM 152,188,40\nR 178,244,49664\nM 156,184,36\nR 180,244,53248\nM 156,188,44\nR 182,244,53760\nW 4\n"
"M 168,192,32\nR 184,245,49152\nM 168,196,40\nR 186,245,49664\nM 172,192,36\nR 188,245,53248\nM 172,196,44\nR 190,245,53760\ns_cbranch_vccz 147f\n"
"s_waitcnt vmcnt(4)\ns_barrier\n147:\ns_add_u32 m0, s60, 98304\ns_add_u32 s61, s67, 1769472\nbuffer_load_dwordx4 v249, s[40:43], s61 offen lds\n"
"v_rcp_f32 v243, v243\nv_mul_f32 v144, v240, v144\nv_mul_f32 v145, v241, v145\nv_mul_f32 v146, v242, v146\nv_mul_f32 v147, v243, v147\n"
"v_mul_f32 v240, v148, v148\nv_mul_f32 v241, v149, v149\ns_add_u32 m0, s60, 106496\ns_add_u32 s61, s67, 2359296\n"
"buffer_load_dwordx4 v249, s[40:43], s61 offen lds\nv_mul_f32 v242, v150, v150\nv_mul_f32 v243, v151, v151\nv_fma_f32 v240, v240, s70, v248\n"
"v_fma_f32 v241, v241, s70, v248\nv_fma_f32 v242, v242, s70, v248\nv_fma_f32 v243, v243, s70, v248\nv_mul_f32 v240, v240, v148\n"
"v_mul_f32 v241, v241, v149\ns_add_u32 m0, s60, 114688\ns_add_u32 s61, s67, 2949120\nbuffer_load_dwordx4 v249, s[40:43], s61 offen lds\n"
"v_mul_f32 v242, v242, v150\nv_mul_f32 v243, v243, v151\nE 240\nE 241\nE 242\nE 243\nv_add_f32 v240, 1.0, v240\nv_add_f32 v241, 1.0, v241\n"
"s_cbranch_vccnz 148f\ns_waitcnt vmcnt(7)\ns_barrier\n148:\nW 4\nM 152,176,48\nR 192,244,57344\nM 152,180,56\nR 194,244,57856\nM 156,176,52\n"
"R 196,244,61440\nM 156,180,60\nR 198,244,61952\nW 4\nM 168,184,48\nR 176,245,57344\nM 168,188,56\nR 178,245,57856\nM 172,184,52\nR 180,245,61440\n"
"M 172,188,60\nR 182,245,61952\nW 4\nM 152,192,64\nR 184,246,4096\nM 152,196,72\nR 186,246,4608\nM 156,192,68\nR 188,246,8192\nM 156,196,76\n"
"R 190,246,8704\nW 4\nM 168,176,64\nR 192,247,4096\nM 168,180,72\nR 194,247,4608\nM 172,176,68\nR 196,247,8192\nM 172,180,76\nR 198,247,8704\nW 4\n"
"M 152,184,80\nR 176,246,12288\nM 152,188,88\nR 178,246,12800\nM 156,184,84\nR 180,246,16384\nM 156,188,92\nR 182,246,16896\nW 4\nM 168,192,80\n"
"R 184,247,12288\nM 168,196,88\nR 186,247,12800\nM 172,192,84\nR 188,247,16384\nM 172,196,92\nR 190,247,16896\ns_cbranch_vccz 149f\ns_waitcnt vmcnt(4)\n"
"s_barrier\n149:\ns_add_u32 m0, s60, 24576\ns_add_u32 s61, s67, 3538944\nbuffer_load_dwordx4 v249, s[40:43], s61 offen lds\nv_add_f32 v242, 1.0, v242\n"
"v_add_f32 v243, 1.0, v243\nv_rcp_f32 v240, v240\nv_rcp_f32 v241, v241\nv_rcp_f32 v242, v242\nv_rcp_f32 v243, v243\nv_mul_f32 v148, v240, v148\n"
"s_add_u32 m0, s60, 32768\ns_add_u32 s61, s67, 4128768\nbuffer_load_dwordx4 v249, s[40:43], s61 offen lds\nv_mul_f32 v149, v241, v149\n"
"v_mul_f32 v150, v242, v150\nv_mul_f32 v151, v243, v151\nv_mul_f32 v240, v160, v160\nv_mul_f32 v241, v161, v161\nv_mul_f32 v242, v162, v162\n"
"v_mul_f32 v243, v163, v163\ns_add_u32 m0, s60, 40960\ns_add_u32 s61, s67, 4718592\nbuffer_load_dwordx4 v249, s[40:43], s61 offen lds\n"
"v_fma_f32 v240, v240, s70, v248\nv_fma_f32 v241, v241, s70, v248\nv_fma_f32 v242, v242, s70, v248\nv_fma_f32 v243, v243, s70, v248\n"
"v_mul_f32 v240, v240, v160\nv_mul_f32 v241, v241, v161\nv_mul_f32 v242, v242, v162\nv_mul_f32 v243, v243, v163\ns_cbranch_vccnz 150f\n"
"s_waitcnt vmcnt(7)\ns_barrier\n150:\nW 4\nM 152,176,96\nR 192,246,20480\nM 152,180,104\nR 194,246,20992\nM 156,176,100\nR 196,246,24576\nM 156,180,108\n"
"R 198,246,25088\nW 4\nM 168,184,96\nR 176,247,20480\nM 168,188,104\nR 178,247,20992\nM 172,184,100\nR 180,247,24576\nM 172,188,108\nR 182,247,25088\n"
"W 4\nM 152,192,112\nR 184,246,28672\nM 152,196,120\nR 186,246,29184\nM 156,192,116\nR 188,246,32768\nM 156,196,124\nR 190,246,33280\nW 4\n"
"M 168,176,112\nR 192,247,28672\nM 168,180,120\nR 194,247,29184\nM 172,176,116\nR 196,247,32768\nM 172,180,124\nR 198,247,33280\nW 4\nM 152,184,128\n"
"R 176,244,0\nM 152,188,136\nR 178,244,512\nM 156,184,132\nR 180,244,4096\nM 156,188,140\nR 182,244,4608\nW 4\nM 168,192,128\nR 184,245,0\n"
"M 168,196,136\nR 186,245,512\nM 172,192,132\nR 188,245,4096\nM 172,196,140\nR 190,245,4608\ns_cbranch_vccz 151f\ns_waitcnt vmcnt(4)\ns_barrier\n151:\n"
"s_add_u32 m0, s60, 49152\ns_add_u32 s61, s67, 0\nbuffer_load_dwordx4 v249, s[44:47], s61 offen lds\ns_nop 2\nv_mul_f32 v144, v144, v152\n"
"v_mul_f32 v145, v145, v153\nv_mul_f32 v146, v146, v154\nv_mul_f32 v147, v147, v155\nv_mul_f32 v148, v148, v156\nv_mul_f32 v149, v149, v157\n"
"v_mul_f32 v150, v150, v158\ns_add_u32 m0, s60, 57344\ns_add_u32 s61, s67, 589824\nbuffer_load_dwordx4 v249, s[44:47], s61 offen lds\n"
"v_mul_f32 v151, v151, v159\nv_cvt_pk_bf16_f32 v208, v144, v145\nv_cvt_pk_bf16_f32 v209, v146, v147\nv_cvt_pk_bf16_f32 v212, v148, v149\n"
"v_cvt_pk_bf16_f32 v213, v150, v151\nE 240\nE 241\nE 242\ns_add_u32 m0, s60, 65536\ns_add_u32 s61, s67, 1179648\n"
"buffer_load_dwordx4 v249, s[44:47], s61 offen lds\nE 243\nv_add_f32 v240, 1.0, v240\nv_add_f32 v241, 1.0, v241\nv_add_f32 v242, 1.0, v242\n"
"v_add_f32 v243, 1.0, v243\nv_rcp_f32 v240, v240\nv_rcp_f32 v241, v241\nv_rcp_f32 v242, v242\ns_cbranch_vccnz 152f\ns_waitcnt vmcnt(7)\ns_barrier\n152:\n"
"W 4\nZ 144,176,0\nR 192,244,8192\nM 144,180,8\nR 194,244,8704\nZ 148,176,4\nR 196,244,12288\nM 148,180,12\nR 198,244,12800\nW 4\nZ 152,184,0\n"
"R 176,245,8192\nM 152,188,8\nR 178,245,8704\nZ 156,184,4\nR 180,245,12288\nM 156,188,12\nR 182,245,12800\nW 4\nM 144,192,16\nR 184,244,16384\n"
"M 144,196,24\nR 186,244,16896\nM 148,192,20\nR 188,244,20480\nM 148,196,28\nR 190,244,20992\nW 4\nM 152,176,16\nR 192,245,16384\nM 152,180,24\n"
"R 194,245,16896\nM 156,176,20\nR 196,245,20480\nM 156,180,28\nR 198,245,20992\nW 4\nM 144,184,32\nR 176,246,36864\nM 144,188,40\nR 178,246,37376\n"
"M 148,184,36\nR 180,246,40960\nM 148,188,44\nR 182,246,41472\nW 4\nM 152,192,32\nR 184,247,36864\nM 152,196,40\nR 186,247,37376\nM 156,192,36\n"
"R 188,247,40960\nM 156,196,44\nR 190,247,41472\ns_cbranch_vccz 153f\ns_waitcnt vmcnt(4)\ns_barrier\n153:\ns_add_u32 m0, s60, 73728\n"
"s_add_u32 s61, s67, 1769472\nbuffer_load_dwordx4 v249, s[44:47], s61 offen lds\nv_rcp_f32 v243, v243\nv_mul_f32 v160, v240, v160\n"
"v_mul_f32 v161, v241, v161\nv_mul_f32 v162, v242, v162\nv_mul_f32 v163, v243, v163\nv_mul_f32 v240, v164, v164\nv_mul_f32 v241, v165, v165\n"
"s_add_u32 m0, s60, 81920\ns_add_u32 s61, s67, 2359296\nbuffer_load_dwordx4 v249, s[44:47], s61 offen lds\nv_mul_f32 v242, v166, v166\n"
"v_mul_f32 v243, v167, v167\nv_fma_f32 v240, v240, s70, v248\nv_fma_f32 v241, v241, s70, v248\nv_fma_f32 v242, v242, s70, v248\n"
"v_fma_f32 v243, v243, s70, v248\nv_mul_f32 v240, v240, v164\nv_mul_f32 v241, v241, v165\ns_add_u32 m0, s60, 90112\ns_add_u32 s61, s67, 2949120\n"
"buffer_load_dwordx4 v249, s[44:47], s61 offen lds\nv_mul_f32 v242, v242, v166\nv_mul_f32 v243, v243, v167\nE 240\nE 241\nE 242\nE 243\n"
"v_add_f32 v240, 1.0, v240\nv_add_f32 v241, 1.0, v241\ns_cbranch_vccnz 154f\ns_waitcnt vmcnt(7)\ns_barrier\n154:\nW 4\nM 144,176,48\nR 192,246,45056\n"
"M 144,180,56\nR 194,246,45568\nM 148,176,52\nR 196,246,49152\nM 148,180,60\nR 198,246,49664\nW 4\nM 152,184,48\nR 176,247,45056\nM 152,188,56\n"
"R 178,247,45568\nM 156,184,52\nR 180,247,49152\nM 156,188,60\nR 182,247,49664\nW 4\nM 144,192,64\nR 184,246,53248\nM 144,196,72\nR 186,246,53760\n"
"M 148,192,68\nR 188,246,57344\nM 148,196,76\nR 190,246,57856\nW 4\nM 152,176,64\nR 192,247,53248\nM 152,180,72\nR 194,247,53760\nM 156,176,68\n"
"R 196,247,57344\nM 156,180,76\nR 198,247,57856\nW 4\nM 144,184,80\nR 176,244,24576\nM 144,188,88\nR 178,244,25088\nM 148,184,84\nR 180,244,28672\n"
"M 148,188,92\nR 182,244,29184\nW 4\nM 152,192,80\nR 184,245,24576\nM 152,196,88\nR 186,245,25088\nM 156,192,84\nR 188,245,28672\nM 156,196,92\n"
"R 190,245,29184\ns_cbranch_vccz 155f\ns_waitcnt vmcnt(4)\ns_barrier\n155:\ns_add_u32 m0, s60, 0\ns_add_u32 s61, s67, 3538944\n"
"buffer_load_dwordx4 v249, s[44:47], s61 offen lds\nv_add_f32 v242, 1.0, v242\nv_add_f32 v243, 1.0, v243\nv_rcp_f32 v240, v240\nv_rcp_f32 v241, v241\n"
"v_rcp_f32 v242, v242\nv_rcp_f32 v243, v243\nv_mul_f32 v164, v240, v164\ns_add_u32 m0, s60, 8192\ns_add_u32 s61, s67, 4128768\n"
"buffer_load_dwordx4 v249, s[44:47], s61 offen lds\nv_mul_f32 v165, v241, v165\nv_mul_f32 v166, v242, v166\nv_mul_f32 v167, v243, v167\n"
"v_mul_f32 v160, v160, v168\nv_mul_f32 v161, v161, v169\nv_mul_f32 v162, v162, v170\nv_mul_f32 v163, v163, v171\ns_add_u32 m0, s60, 16384\n"
"s_add_u32 s61, s67, 4718592\nbuffer_load_dwordx4 v249, s[44:47], s61 offen lds\nv_mul_f32 v164, v164, v172\nv_mul_f32 v165, v165, v173\n"
"v_mul_f32 v166, v166, v174\nv_mul_f32 v167, v167, v175\nv_cvt_pk_bf16_f32 v210, v160, v161\nv_cvt_pk_bf16_f32 v211, v162, v163\n"
"v_cvt_pk_bf16_f32 v214, v164, v165\nv_cvt_pk_bf16_f32 v215, v166, v167\ns_cbranch_vccnz 156f\ns_waitcnt vmcnt(7)\ns_barrier\n156:\nW 4\nM 144,176,96\n"
"R 192,244,32768\nM 144,180,104\nR 194,244,33280\nM 148,176,100\nR 196,244,36864\nM 148,180,108\nR 198,244,37376\nW 4\nM 152,184,96\nR 176,245,32768\n"
"M 152,188,104\nR 178,245,33280\nM 156,184,100\nR 180,245,36864\nM 156,188,108\nR 182,245,37376\nW 4\nM 144,192,112\nR 184,244,40960\nM 144,196,120\n"
"R 186,244,41472\nM 148,192,116\nR 188,244,45056\nM 148,196,124\nR 190,244,45568\nW 4\nM 152,176,112\nR 192,245,40960\nM 152,180,120\nR 194,245,41472\n"
"M 156,176,116\nR 196,245,45056\nM 156,180,124\nR 198,245,45568\nW 4\nM 144,184,128\nR 176,244,49152\nM 144,188,136\nR 178,244,49664\nM 148,184,132\n"
"R 180,244,53248\nM 148,188,140\nR 182,244,53760\nW 4\nM 152,192,128\nR 184,245,49152\nM 152,196,136\nR 186,245,49664\nM 156,192,132\nR 188,245,53248\n"
"M 156,196,140\nR 190,245,53760\ns_cbranch_vccz 157f\ns_waitcnt vmcnt(4)\ns_barrier\n157:\ns_add_u32 s67, s76, 6\ns_cmp_ge_u32 s67, 9\n"
"s_cselect_b32 s61, 9, 0\ns_sub_u32 s67, s67, s61\ns_mul_i32 s95, s67, 73728\ns_lshl_b32 s67, s67, 7\ns_add_u32 s67, s67, s62\ns_add_u32 m0, s60, 98304\n"
"s_add_u32 s61, s67, 0\nbuffer_load_dwordx4 v249, s[40:43], s61 offen lds\nv_mul_f32 v240, v144, v144\nv_mul_f32 v241, v145, v145\n"
"v_mul_f32 v242, v146, v146\nv_mul_f32 v243, v147, v147\nv_fma_f32 v240, v240, s70, v248\nv_fma_f32 v241, v241, s70, v248\n"
"v_fma_f32 v242, v242, s70, v248\ns_add_u32 m0, s60, 106496\ns_add_u32 s61, s67, 589824\nbuffer_load_dwordx4 v249, s[40:43], s61 offen lds\n"
"v_fma_f32 v243, v243, s70, v248\nv_mul_f32 v240, v240, v144\nv_mul_f32 v241, v241, v145\nv_mul_f32 v242, v242, v146\nv_mul_f32 v243, v243, v147\nE 240\n"
"E 241\nE 242\ns_add_u32 m0, s60, 114688\ns_add_u32 s61, s67, 1179648\nbuffer_load_dwordx4 v249, s[40:43], s61 offen lds\nE 243\n"
"v_add_f32 v240, 1.0, v240\nv_add_f32 v241, 1.0, v241\nv_add_f32 v242, 1.0, v242\nv_add_f32 v243, 1.0, v243\nv_rcp_f32 v240, v240\nv_rcp_f32 v241, v241\n"
"v_rcp_f32 v242, v242\ns_cbranch_vccnz 158f\ns_waitcnt vmcnt(7)\ns_barrier\n158:\nW 4\nZ 160,176,0\nR 192,244,57344\nM 160,180,8\nR 194,244,57856\n"
"Z 164,176,4\nR 196,244,61440\nM 164,180,12\nR 198,244,61952\nW 4\nZ 168,184,0\nR 176,245,57344\nM 168,188,8\nR 178,245,57856\nZ 172,184,4\n"
"R 180,245,61440\nM 172,188,12\nR 182,245,61952\nW 4\nM 160,192,16\nR 184,246,4096\nM 160,196,24\nR 186,246,4608\nM 164,192,20\nR 188,246,8192\n"
"M 164,196,28\nR 190,246,8704\nW 4\nM 168,176,16\nR 192,247,4096\nM 168,180,24\nR 194,247,4608\nM 172,176,20\nR 196,247,8192\nM 172,180,28\n"
"R 198,247,8704\nW 4\nM 160,184,32\nR 176,246,12288\nM 160,188,40\nR 178,246,12800\nM 164,184,36\nR 180,246,16384\nM 164,188,44\nR 182,246,16896\nW 4\n"
"M 168,192,32\nR 184,247,12288\nM 168,196,40\nR 186,247,12800\nM 172,192,36\nR 188,247,16384\nM 172,196,44\nR 190,247,16896\ns_cbranch_vccz 159f\n"
"s_waitcnt vmcnt(4)\ns_barrier\n159:\ns_add_u32 m0, s60, 24576\ns_add_u32 s61, s67, 1769472\nbuffer_load_dwordx4 v249, s[40:43], s61 offen lds\n"
"v_rcp_f32 v243, v243\nv_mul_f32 v144, v240, v144\nv_mul_f32 v145, v241, v145\nv_mul_f32 v146, v242, v146\nv_mul_f32 v147, v243, v147\n"
"v_mul_f32 v240, v148, v148\nv_mul_f32 v241, v149, v149\ns_add_u32 m0, s60, 32768\ns_add_u32 s61, s67, 2359296\n"
"buffer_load_dwordx4 v249, s[40:43], s61 offen lds\nv_mul_f32 v242, v150, v150\nv_mul_f32 v243, v151, v151\nv_fma_f32 v240, v240, s70, v248\n"
"v_fma_f32 v241, v241, s70, v248\nv_fma_f32 v242, v242, s70, v248\nv_fma_f32 v243, v243, s70, v248\nv_mul_f32 v240, v240, v148\n"
"v_mul_f32 v241, v241, v149\ns_add_u32 m0, s60, 40960\ns_add_u32 s61, s67, 2949120\nbuffer_load_dwordx4 v249, s[40:43], s61 offen lds\n"
"v_mul_f32 v242, v242, v150\nv_mul_f32 v243, v243, v151\nE 240\nE 241\nE 242\nE 243\nv_add_f32 v240, 1.0, v240\nv_add_f32 v241, 1.0, v241\n"
"s_cbranch_vccnz 160f\ns_waitcnt vmcnt(7)\ns_barrier\n160:\nW 4\nM 160,176,48\nR 192,246,20480\nM 160,180,56\nR 194,246,20992\nM 164,176,52\n"
"R 196,246,24576\nM 164,180,60\nR 198,246,25088\nW 4\nM 168,184,48\nR 176,247,20480\nM 168,188,56\nR 178,247,20992\nM 172,184,52\nR 180,247,24576\n"
"M 172,188,60\nR 182,247,25088\nW 4\nM 160,192,64\nR 184,246,28672\nM 160,196,72\nR 186,246,29184\nM 164,192,68\nR 188,246,32768\nM 164,196,76\n"
"R 190,246,33280\nW 4\nM 168,176,64\nR 192,247,28672\nM 168,180,72\nR 194,247,29184\nM 172,176,68\nR 196,247,32768\nM 172,180,76\nR 198,247,33280\nW 4\n"
"M 160,184,80\nR 176,244,0\nM 160,188,88\nR 178,244,512\nM 164,184,84\nR 180,244,4096\nM 164,188,92\nR 182,244,4608\nW 4\nM 168,192,80\nR 184,245,0\n"
"M 168,196,88\nR 186,245,512\nM 172,192,84\nR 188,245,4096\nM 172,196,92\nR 190,245,4608\ns_cbranch_vccz 161f\ns_waitcnt vmcnt(4)\ns_barrier\n161:\n"
"s_add_u32 m0, s60, 49152\ns_add_u32 s61, s67, 3538944\nbuffer_load_dwordx4 v249, s[40:43], s61 offen lds\nv_add_f32 v242, 1.0, v242\n"
"v_add_f32 v243, 1.0, v243\nv_rcp_f32 v240, v240\nv_rcp_f32 v241, v241\nv_rcp_f32 v242, v242\nv_rcp_f32 v243, v243\nv_mul_f32 v148, v240, v148\n"
"s_add_u32 m0, s60, 57344\ns_add_u32 s61, s67, 4128768\nbuffer_load_dwordx4 v249, s[40:43], s61 offen lds\nv_mul_f32 v149, v241, v149\n"
"v_mul_f32 v150, v242, v150\nv_mul_f32 v151, v243, v151\nv_mul_f32 v240, v152, v152\nv_mul_f32 v241, v153, v153\nv_mul_f32 v242, v154, v154\n"
"v_mul_f32 v243, v155, v155\ns_add_u32 m0, s60, 65536\ns_add_u32 s61, s67, 4718592\nbuffer_load_dwordx4 v249, s[40:43], s61 offen lds\n"
"v_fma_f32 v240, v240, s70, v248\nv_fma_f32 v241, v241, s70, v248\nv_fma_f32 v242, v242, s70, v248\nv_fma_f32 v243, v243, s70, v248\n"
"v_mul_f32 v240, v240, v152\nv_mul_f32 v241, v241, v153\nv_mul_f32 v242, v242, v154\nv_mul_f32 v243, v243, v155\ns_cbranch_vccnz 162f\n"
"s_waitcnt vmcnt(7)\ns_barrier\n162:\nW 4\nM 160,176,96\nR 192,244,8192\nM 160,180,104\nR 194,244,8704\nM 164,176,100\nR 196,244,12288\nM 164,180,108\n"
"R 198,244,12800\nW 4\nM 168,184,96\nR 176,245,8192\nM 168,188,104\nR 178,245,8704\nM 172,184,100\nR 180,245,12288\nM 172,188,108\nR 182,245,12800\nW 4\n"
"M 160,192,112\nR 184,244,16384\nM 160,196,120\nR 186,244,16896\nM 164,192,116\nR 188,244,20480\nM 164,196,124\nR 190,244,20992\nW 4\nM 168,176,112\n"
"R 192,245,16384\nM 168,180,120\nR 194,245,16896\nM 172,176,116\nR 196,245,20480\nM 172,180,124\nR 198,245,20992\nW 4\nM 160,184,128\nR 176,246,36864\n"
"M 160,188,136\nR 178,246,37376\nM 164,184,132\nR 180,246,40960\nM 164,188,140\nR 182,246,41472\nW 4\nM 168,192,128\nR 184,247,36864\nM 168,196,136\n"
"R 186,247,37376\nM 172,192,132\nR 188,247,40960\nM 172,196,140\nR 190,247,41472\ns_cbranch_vccz 163f\ns_waitcnt vmcnt(4)\ns_barrier\n163:\n"
"s_add_u32 m0, s60, 73728\ns_add_u32 s61, s67, 0\nbuffer_load_dwordx4 v249, s[44:47], s61 offen lds\ns_nop 2\nv_mul_f32 v144, v144, v160\n"
"v_mul_f32 v145, v145, v161\nv_mul_f32 v146, v146, v162\nv_mul_f32 v147, v147, v163\nv_mul_f32 v148, v148, v164\nv_mul_f32 v149, v149, v165\n"
"v_mul_f32 v150, v150, v166\ns_add_u32 m0, s60, 81920\ns_add_u32 s61, s67, 589824\nbuffer_load_dwordx4 v249, s[44:47], s61 offen lds\n"
"v_mul_f32 v151, v151, v167\nv_cvt_pk_bf16_f32 v216, v144, v145\nv_cvt_pk_bf16_f32 v217, v146, v147\nv_cvt_pk_bf16_f32 v220, v148, v149\n"
"v_cvt_pk_bf16_f32 v221, v150, v151\nE 240\nE 241\nE 242\ns_add_u32 m0, s60, 90112\ns_add_u32 s61, s67, 1179648\n"
"buffer_load_dwordx4 v249, s[44:47], s61 offen lds\nE 243\nv_add_f32 v240, 1.0, v240\nv_add_f32 v241, 1.0, v241\nv_add_f32 v242, 1.0, v242\n"
"v_add_f32 v243, 1.0, v243\nv_rcp_f32 v240, v240\nv_rcp_f32 v241, v241\nv_rcp_f32 v242, v242\ns_cbranch_vccnz 164f\ns_waitcnt vmcnt(7)\ns_barrier\n164:\n"
"W 4\nZ 144,176,0\nR 192,246,45056\nM 144,180,8\nR 194,246,45568\nZ 148,176,4\nR 196,246,49152\nM 148,180,12\nR 198,246,49664\nW 4\nZ 160,184,0\n"
"R 176,247,45056\nM 160,188,8\nR 178,247,45568\nZ 164,184,4\nR 180,247,49152\nM 164,188,12\nR 182,247,49664\nW 4\nM 144,192,16\nR 184,246,53248\n"
"M 144,196,24\nR 186,246,53760\nM 148,192,20\nR 188,246,57344\nM 148,196,28\nR 190,246,57856\nW 4\nM 160,176,16\nR 192,247,53248\nM 160,180,24\n"
"R 194,247,53760\nM 164,176,20\nR 196,247,57344\nM 164,180,28\nR 198,247,57856\nW 4\nM 144,184,32\nR 176,244,24576\nM 144,188,40\nR 178,244,25088\n"
"M 148,184,36\nR 180,244,28672\nM 148,188,44\nR 182,244,29184\nW 4\nM 160,192,32\nR 184,245,24576\nM 160,196,40\nR 186,245,25088\nM 164,192,36\n"
"R 188,245,28672\nM 164,196,44\nR 190,245,29184\ns_cbranch_vccz 165f\ns_waitcnt vmcnt(4)\ns_barrier\n165:\ns_add_u32 m0, s60, 0\n"
"s_add_u32 s61, s67, 1769472\nbuffer_load_dwordx4 v249, s[44:47], s61 offen lds\nv_rcp_f32 v243, v243\nv_mul_f32 v152, v240, v152\n"
"v_mul_f32 v153, v241, v153\nv_mul_f32 v154, v242, v154\nv_mul_f32 v155, v243, v155\nv_mul_f32 v240, v156, v156\nv_mul_f32 v241, v157, v157\n"
"s_add_u32 m0, s60, 8192\ns_add_u32 s61, s67, 2359296\nbuffer_load_dwordx4 v249, s[44:47], s61 offen lds\nv_mul_f32 v242, v158, v158\n"
"v_mul_f32 v243, v159, v159\nv_fma_f32 v240, v240, s70, v248\nv_fma_f32 v241, v241, s70, v248\nv_fma_f32 v242, v242, s70, v248\n"
"v_fma_f32 v243, v243, s70, v248\nv_mul_f32 v240, v240, v156\nv_mul_f32 v241, v241, v157\ns_add_u32 m0, s60, 16384\ns_add_u32 s61, s67, 2949120\n"
"buffer_load_dwordx4 v249, s[44:47], s61 offen lds\nv_mul_f32 v242, v242, v158\nv_mul_f32 v243, v243, v159\nE 240\nE 241\nE 242\nE 243\n"
"v_add_f32 v240, 1.0, v240\nv_add_f32 v241, 1.0, v241\ns_cbranch_vccnz 166f\ns_waitcnt vmcnt(7)\ns_barrier\n166:\nW 4\nM 144,176,48\nR 192,244,32768\n"
"M 144,180,56\nR 194,244,33280\nM 148,176,52\nR 196,244,36864\nM 148,180,60\nR 198,244,37376\nW 4\nM 160,184,48\nR 176,245,32768\nM 160,188,56\n"
"R 178,245,33280\nM 164,184,52\nR 180,245,36864\nM 164,188,60\nR 182,245,37376\nW 4\nM 144,192,64\nR 184,244,40960\nM 144,196,72\nR 186,244,41472\n"
"M 148,192,68\nR 188,244,45056\nM 148,196,76\nR 190,244,45568\nW 4\nM 160,176,64\nR 192,245,40960\nM 160,180,72\nR 194,245,41472\nM 164,176,68\n"
"R 196,245,45056\nM 164,180,76\nR 198,245,45568\nW 4\nM 144,184,80\nR 176,244,49152\nM 144,188,88\nR 178,244,49664\nM 148,184,84\nR 180,244,53248\n"
"M 148,188,92\nR 182,244,53760\nW 4\nM 160,192,80\nR 184,245,49152\nM 160,196,88\nR 186,245,49664\nM 164,192,84\nR 188,245,53248\nM 164,196,92\n"
"R 190,245,53760\ns_cbranch_vccz 167f\ns_waitcnt vmcnt(4)\ns_barrier\n167:\ns_add_u32 m0, s60, 98304\ns_add_u32 s61, s67, 3538944\n"
"buffer_load_dwordx4 v249, s[44:47], s61 offen lds\nv_add_f32 v242, 1.0, v242\nv_add_f32 v243, 1.0, v243\nv_rcp_f32 v240, v240\nv_rcp_f32 v241, v241\n"
"v_rcp_f32 v242, v242\nv_rcp_f32 v243, v243\nv_mul_f32 v156, v240, v156\ns_add_u32 m0, s60, 106496\ns_add_u32 s61, s67, 4128768\n"
"buffer_load_dwordx4 v249, s[44:47], s61 offen lds\nv_mul_f32 v157, v241, v157\nv_mul_f32 v158, v242, v158\nv_mul_f32 v159, v243, v159\n"
"v_mul_f32 v152, v152, v168\nv_mul_f32 v153, v153, v169\nv_mul_f32 v154, v154, v170\nv_mul_f32 v155, v155, v171\ns_add_u32 m0, s60, 114688\n"
"s_add_u32 s61, s67, 4718592\nbuffer_load_dwordx4 v249, s[44:47], s61 offen lds\nv_mul_f32 v156, v156, v172\nv_mul_f32 v157, v157, v173\n"
"v_mul_f32 v158, v158, v174\nv_mul_f32 v159, v159, v175\nv_cvt_pk_bf16_f32 v218, v152, v153\nv_cvt_pk_bf16_f32 v219, v154, v155\n"
"v_cvt_pk_bf16_f32 v222, v156, v157\nv_cvt_pk_bf16_f32 v223, v158, v159\ns_cbranch_vccnz 168f\ns_waitcnt vmcnt(7)\ns_barrier\n168:\nW 4\nM 144,176,96\n"
"R 192,244,57344\nM 144,180,104\nR 194,244,57856\nM 148,176,100\nR 196,244,61440\nM 148,180,108\nR 198,244,61952\nW 4\nM 160,184,96\nR 176,245,57344\n"
"M 160,188,104\nR 178,245,57856\nM 164,184,100\nR 180,245,61440\nM 164,188,108\nR 182,245,61952\nW 4\nM 144,192,112\nR 184,246,4096\nM 144,196,120\n"
"R 186,246,4608\nM 148,192,116\nR 188,246,8192\nM 148,196,124\nR 190,246,8704\nW 4\nM 160,176,112\nR 192,247,4096\nM 160,180,120\nR 194,247,4608\n"
"M 164,176,116\nR 196,247,8192\nM 164,180,124\nR 198,247,8704\nW 4\nM 144,184,128\nR 176,246,12288\nM 144,188,136\nR 178,246,12800\nM 148,184,132\n"
"R 180,246,16384\nM 148,188,140\nR 182,246,16896\nW 4\nM 160,192,128\nR 184,247,12288\nM 160,196,136\nR 186,247,12800\nM 164,192,132\nR 188,247,16384\n"
"M 164,196,140\nR 190,247,16896\ns_cbranch_vccz 169f\ns_waitcnt vmcnt(4)\ns_barrier\n169:\ns_add_u32 s67, s76, 7\ns_cmp_ge_u32 s67, 9\n"
"s_cselect_b32 s61, 9, 0\ns_sub_u32 s67, s67, s61\ns_mul_i32 s96, s67, 73728\ns_lshl_b32 s67, s67, 7\ns_add_u32 s67, s67, s62\ns_add_u32 m0, s60, 24576\n"
"s_add_u32 s61, s67, 0\nbuffer_load_dwordx4 v249, s[40:43], s61 offen lds\nv_mul_f32 v240, v144, v144\nv_mul_f32 v241, v145, v145\n"
"v_mul_f32 v242, v146, v146\nv_mul_f32 v243, v147, v147\nv_fma_f32 v240, v240, s70, v248\nv_fma_f32 v241, v241, s70, v248\n"
"v_fma_f32 v242, v242, s70, v248\ns_add_u32 m0, s60, 32768\ns_add_u32 s61, s67, 589824\nbuffer_load_dwordx4 v249, s[40:43], s61 offen lds\n"
"v_fma_f32 v243, v243, s70, v248\nv_mul_f32 v240, v240, v144\nv_mul_f32 v241, v241, v145\nv_mul_f32 v242, v242, v146\nv_mul_f32 v243, v243, v147\nE 240\n"
"E 241\nE 242\ns_add_u32 m0, s60, 40960\ns_add_u32 s61, s67, 1179648\nbuffer_load_dwordx4 v249, s[40:43], s61 offen lds\nE 243\n"
"v_add_f32 v240, 1.0, v240\nv_add_f32 v241, 1.0, v241\nv_add_f32 v242, 1.0, v242\nv_add_f32 v243, 1.0, v243\nv_rcp_f32 v240, v240\nv_rcp_f32 v241, v241\n"
"v_rcp_f32 v242, v242\ns_cbranch_vccnz 170f\ns_waitcnt vmcnt(7)\ns_barrier\n170:\nW 4\nZ 152,176,0\nR 192,246,20480\nM 152,180,8\nR 194,246,20992\n"
"Z 156,176,4\nR 196,246,24576\nM 156,180,12\nR 198,246,25088\nW 4\nZ 168,184,0\nR 176,247,20480\nM 168,188,8\nR 178,247,20992\nZ 172,184,4\n"
"R 180,247,24576\nM 172,188,12\nR 182,247,25088\nW 4\nM 152,192,16\nR 184,246,28672\nM 152,196,24\nR 186,246,29184\nM 156,192,20\nR 188,246,32768\n"
"M 156,196,28\nR 190,246,33280\nW 4\nM 168,176,16\nR 192,247,28672\nM 168,180,24\nR 194,247,29184\nM 172,176,20\nR 196,247,32768\nM 172,180,28\n"
"R 198,247,33280\nW 4\nM 152,184,32\nR 176,244,0\nM 152,188,40\nR 178,244,512\nM 156,184,36\nR 180,244,4096\nM 156,188,44\nR 182,244,4608\nW 4\n"
"M 168,192,32\nR 184,245,0\nM 168,196,40\nR 186,245,512\nM 172,192,36\nR 188,245,4096\nM 172,196,44\nR 190,245,4608\ns_cbranch_vccz 171f\n"
"s_waitcnt vmcnt(4)\ns_barrier\n171:\ns_add_u32 m0, s60, 49152\ns_add_u32 s61, s67, 1769472\nbuffer_load_dwordx4 v249, s[40:43], s61 offen lds\n"
"v_rcp_f32 v243, v243\nv_mul_f32 v144, v240, v144\nv_mul_f32 v145, v241, v145\nv_mul_f32 v146, v242, v146\nv_mul_f32 v147, v243, v147\n"
"v_mul_f32 v240, v148, v148\nv_mul_f32 v241, v149, v149\ns_add_u32 m0, s60, 57344\ns_add_u32 s61, s67, 2359296\n"
"buffer_load_dwordx4 v249, s[40:43], s61 offen lds\nv_mul_f32 v242, v150, v150\nv_mul_f32 v243, v151, v151\nv_fma_f32 v240, v240, s70, v248\n"
"v_fma_f32 v241, v241, s70, v248\nv_fma_f32 v242, v242, s70, v248\nv_fma_f32 v243, v243, s70, v248\nv_mul_f32 v240, v240, v148\n"
"v_mul_f32 v241, v241, v149\ns_add_u32 m0, s60, 65536\ns_add_u32 s61, s67, 2949120\nbuffer_load_dwordx4 v249, s[40:43], s61 offen lds\n"
"v_mul_f32 v242, v242, v150\nv_mul_f32 v243, v243, v151\nE 240\nE 241\nE 242\nE 243\nv_add_f32 v240, 1.0, v240\nv_add_f32 v241, 1.0, v241\n"
"s_cbranch_vccnz 172f\ns_waitcnt vmcnt(7)\ns_barrier\n172:\nW 4\nM 152,176,48\nR 192,244,8192\nM 152,180,56\nR 194,244,8704\nM 156,176,52\n"
"R 196,244,12288\nM 156,180,60\nR 198,244,12800\nW 4\nM 168,184,48\nR 176,245,8192\nM 168,188,56\nR 178,245,8704\nM 172,184,52\nR 180,245,12288\n"
"M 172,188,60\nR 182,245,12800\nW 4\nM 152,192,64\nR 184,244,16384\nM 152,196,72\nR 186,244,16896\nM 156,192,68\nR 188,244,20480\nM 156,196,76\n"
"R 190,244,20992\nW 4\nM 168,176,64\nR 192,245,16384\nM 168,180,72\nR 194,245,16896\nM 172,176,68\nR 196,245,20480\nM 172,180,76\nR 198,245,20992\nW 4\n"
"M 152,184,80\nR 176,246,36864\nM 152,188,88\nR 178,246,37376\nM 156,184,84\nR 180,246,40960\nM 156,188,92\nR 182,246,41472\nW 4\nM 168,192,80\n"
"R 184,247,36864\nM 168,196,88\nR 186,247,37376\nM 172,192,84\nR 188,247,40960\nM 172,196,92\nR 190,247,41472\ns_cbranch_vccz 173f\ns_waitcnt vmcnt(4)\n"
"s_barrier\n173:\ns_add_u32 m0, s60, 73728\ns_add_u32 s61, s67, 3538944\nbuffer_load_dwordx4 v249, s[40:43], s61 offen lds\nv_add_f32 v242, 1.0, v242\n"
"v_add_f32 v243, 1.0, v243\nv_rcp_f32 v240, v240\nv_rcp_f32 v241, v241\nv_rcp_f32 v242, v242\nv_rcp_f32 v243, v243\nv_mul_f32 v148, v240, v148\n"
"s_add_u32 m0, s60, 81920\ns_add_u32 s61, s67, 4128768\nbuffer_load_dwordx4 v249, s[40:43], s61 offen lds\nv_mul_f32 v149, v241, v149\n"
"v_mul_f32 v150, v242, v150\nv_mul_f32 v151, v243, v151\nv_mul_f32 v240, v160, v160\nv_mul_f32 v241, v161, v161\nv_mul_f32 v242, v162, v162\n"
"v_mul_f32 v243, v163, v163\ns_add_u32 m0, s60, 90112\ns_add_u32 s61, s67, 4718592\nbuffer_load_dwordx4 v249, s[40:43], s61 offen lds\n"
"v_fma_f32 v240, v240, s70, v248\nv_fma_f32 v241, v241, s70, v248\nv_fma_f32 v242, v242, s70, v248\nv_fma_f32 v243, v243, s70, v248\n"
"v_mul_f32 v240, v240, v160\nv_mul_f32 v241, v241, v161\nv_mul_f32 v242, v242, v162\nv_mul_f32 v243, v243, v163\ns_cbranch_vccnz 174f\n"
"s_waitcnt vmcnt(7)\ns_barrier\n174:\nW 4\nM 152,176,96\nR 192,246,45056\nM 152,180,104\nR 194,246,45568\nM 156,176,100\nR 196,246,49152\nM 156,180,108\n"
"R 198,246,49664\nW 4\nM 168,184,96\nR 176,247,45056\nM 168,188,104\nR 178,247,45568\nM 172,184,100\nR 180,247,49152\nM 172,188,108\nR 182,247,49664\n"
"W 4\nM 152,192,112\nR 184,246,53248\nM 152,196,120\nR 186,246,53760\nM 156,192,116\nR 188,246,57344\nM 156,196,124\nR 190,246,57856\nW 4\n"
"M 168,176,112\nR 192,247,53248\nM 168,180,120\nR 194,247,53760\nM 172,176,116\nR 196,247,57344\nM 172,180,124\nR 198,247,57856\nW 4\nM 152,184,128\n"
"R 176,244,24576\nM 152,188,136\nR 178,244,25088\nM 156,184,132\nR 180,244,28672\nM 156,188,140\nR 182,244,29184\nW 4\nM 168,192,128\nR 184,245,24576\n"
"M 168,196,136\nR 186,245,25088\nM 172,192,132\nR 188,245,28672\nM 172,196,140\nR 190,245,29184\ns_cbranch_vccz 175f\ns_waitcnt vmcnt(4)\ns_barrier\n"
"175:\ns_add_u32 m0, s60, 0\ns_add_u32 s61, s67, 0\nbuffer_load_dwordx4 v249, s[44:47], s61 offen lds\ns_nop 2\nv_mul_f32 v144, v144, v152\n"
"v_mul_f32 v145, v145, v153\nv_mul_f32 v146, v146, v154\nv_mul_f32 v147, v147, v155\nv_mul_f32 v148, v148, v156\nv_mul_f32 v149, v149, v157\n"
"v_mul_f32 v150, v150, v158\ns_add_u32 m0, s60, 8192\ns_add_u32 s61, s67, 589824\nbuffer_load_dwordx4 v249, s[44:47], s61 offen lds\n"
"v_mul_f32 v151, v151, v159\nv_cvt_pk_bf16_f32 v224, v144, v145\nv_cvt_pk_bf16_f32 v225, v146, v147\nv_cvt_pk_bf16_f32 v228, v148, v149\n"
"v_cvt_pk_bf16_f32 v229, v150, v151\nE 240\nE 241\nE 242\ns_add_u32 m0, s60, 16384\ns_add_u32 s61, s67, 1179648\n"
"buffer_load_dwordx4 v249, s[44:47], s61 offen lds\nE 243\nv_add_f32 v240, 1.0, v240\nv_add_f32 v241, 1.0, v241\nv_add_f32 v242, 1.0, v242\n"
"v_add_f32 v243, 1.0, v243\nv_rcp_f32 v240, v240\nv_rcp_f32 v241, v241\nv_rcp_f32 v242, v242\ns_cbranch_vccnz 176f\ns_waitcnt vmcnt(7)\ns_barrier\n176:\n"
"W 4\nZ 144,176,0\nR 192,244,32768\nM 144,180,8\nR 194,244,33280\nZ 148,176,4\nR 196,244,36864\nM 148,180,12\nR 198,244,37376\nW 4\nZ 152,184,0\n"
"R 176,245,32768\nM 152,188,8\nR 178,245,33280\nZ 156,184,4\nR 180,245,36864\nM 156,188,12\nR 182,245,37376\nW 4\nM 144,192,16\nR 184,244,40960\n"
"M 144,196,24\nR 186,244,41472\nM 148,192,20\nR 188,244,45056\nM 148,196,28\nR 190,244,45568\nW 4\nM 152,176,16\nR 192,245,40960\nM 152,180,24\n"
"R 194,245,41472\nM 156,176,20\nR 196,245,45056\nM 156,180,28\nR 198,245,45568\nW 4\nM 144,184,32\nR 176,244,49152\nM 144,188,40\nR 178,244,49664\n"
"M 148,184,36\nR 180,244,53248\nM 148,188,44\nR 182,244,53760\nW 4\nM 152,192,32\nR 184,245,49152\nM 152,196,40\nR 186,245,49664\nM 156,192,36\n"
"R 188,245,53248\nM 156,196,44\nR 190,245,53760\ns_cbranch_vccz 177f\ns_waitcnt vmcnt(4)\ns_barrier\n177:\ns_add_u32 m0, s60, 98304\n"
"s_add_u32 s61, s67, 1769472\nbuffer_load_dwordx4 v249, s[44:47], s61 offen lds\nv_rcp_f32 v243, v243\nv_mul_f32 v160, v240, v160\n"
"v_mul_f32 v161, v241, v161\nv_mul_f32 v162, v242, v162\nv_mul_f32 v163, v243, v163\nv_mul_f32 v240, v164, v164\nv_mul_f32 v241, v165, v165\n"
"s_add_u32 m0, s60, 106496\ns_add_u32 s61, s67, 2359296\nbuffer_load_dwordx4 v249, s[44:47], s61 offen lds\nv_mul_f32 v242, v166, v166\n"
"v_mul_f32 v243, v167, v167\nv_fma_f32 v240, v240, s70, v248\nv_fma_f32 v241, v241, s70, v248\nv_fma_f32 v242, v242, s70, v248\n"
"v_fma_f32 v243, v243, s70, v248\nv_mul_f32 v240, v240, v164\nv_mul_f32 v241, v241, v165\ns_add_u32 m0, s60, 114688\ns_add_u32 s61, s67, 2949120\n"
"buffer_load_dwordx4 v249, s[44:47], s61 offen lds\nv_mul_f32 v242, v242, v166\nv_mul_f32 v243, v243, v167\nE 240\nE 241\nE 242\nE 243\n"
"v_add_f32 v240, 1.0, v240\nv_add_f32 v241, 1.0, v241\ns_cbranch_vccnz 178f\ns_waitcnt vmcnt(7)\ns_barrier\n178:\nW 4\nM 144,176,48\nR 192,244,57344\n"
"M 144,180,56\nR 194,244,57856\nM 148,176,52\nR 196,244,61440\nM 148,180,60\nR 198,244,61952\nW 4\nM 152,184,48\nR 176,245,57344\nM 152,188,56\n"
"R 178,245,57856\nM 156,184,52\nR 180,245,61440\nM 156,188,60\nR 182,245,61952\nW 4\nM 144,192,64\nR 184,246,4096\nM 144,196,72\nR 186,246,4608\n"
"M 148,192,68\nR 188,246,8192\nM 148,196,76\nR 190,246,8704\nW 4\nM 152,176,64\nR 192,247,4096\nM 152,180,72\nR 194,247,4608\nM 156,176,68\n"
"R 196,247,8192\nM 156,180,76\nR 198,247,8704\nW 4\nM 144,184,80\nR 176,246,12288\nM 144,188,88\nR 178,246,12800\nM 148,184,84\nR 180,246,16384\n"
"M 148,188,92\nR 182,246,16896\nW 4\nM 152,192,80\nR 184,247,12288\nM 152,196,88\nR 186,247,12800\nM 156,192,84\nR 188,247,16384\nM 156,196,92\n"
"R 190,247,16896\ns_cbranch_vccz 179f\ns_waitcnt vmcnt(4)\ns_barrier\n179:\ns_add_u32 m0, s60, 24576\ns_add_u32 s61, s67, 3538944\n"
"buffer_load_dwordx4 v249, s[44:47], s61 offen lds\nv_add_f32 v242, 1.0, v242\nv_add_f32 v243, 1.0, v243\nv_rcp_f32 v240, v240\nv_rcp_f32 v241, v241\n"
"v_rcp_f32 v242, v242\nv_rcp_f32 v243, v243\nv_mul_f32 v164, v240, v164\ns_add_u32 m0, s60, 32768\ns_add_u32 s61, s67, 4128768\n"
"buffer_load_dwordx4 v249, s[44:47], s61 offen lds\nv_mul_f32 v165, v241, v165\nv_mul_f32 v166, v242, v166\nv_mul_f32 v167, v243, v167\n"
"v_mul_f32 v160, v160, v168\nv_mul_f32 v161, v161, v169\nv_mul_f32 v162, v162, v170\nv_mul_f32 v163, v163, v171\ns_add_u32 m0, s60, 40960\n"
"s_add_u32 s61, s67, 4718592\nbuffer_load_dwordx4 v249, s[44:47], s61 offen lds\nv_mul_f32 v164, v164, v172\nv_mul_f32 v165, v165, v173\n"
"v_mul_f32 v166, v166, v174\nv_mul_f32 v167, v167, v175\nv_cvt_pk_bf16_f32 v226, v160, v161\nv_cvt_pk_bf16_f32 v227, v162, v163\n"
"v_cvt_pk_bf16_f32 v230, v164, v165\nv_cvt_pk_bf16_f32 v231, v166, v167\ns_cbranch_vccnz 180f\ns_waitcnt vmcnt(7)\ns_barrier\n180:\nW 4\nM 144,176,96\n"
"R 192,246,20480\nM 144,180,104\nR 194,246,20992\nM 148,176,100\nR 196,246,24576\nM 148,180,108\nR 198,246,25088\nW 4\nM 152,184,96\nR 176,247,20480\n"
"M 152,188,104\nR 178,247,20992\nM 156,184,100\nR 180,247,24576\nM 156,188,108\nR 182,247,25088\nW 4\nM 144,192,112\nR 184,246,28672\nM 144,196,120\n"
"R 186,246,29184\nM 148,192,116\nR 188,246,32768\nM 148,196,124\nR 190,246,33280\nW 4\nM 152,176,112\nR 192,247,28672\nM 152,180,120\nR 194,247,29184\n"
"M 156,176,116\nR 196,247,32768\nM 156,180,124\nR 198,247,33280\nW 4\nM 144,184,128\nR 176,244,0\nM 144,188,136\nR 178,244,512\nM 148,184,132\n"
"R 180,244,4096\nM 148,188,140\nR 182,244,4608\nW 4\nM 152,192,128\nR 184,245,0\nM 152,196,136\nR 186,245,512\nM 156,192,132\nR 188,245,4096\n"
"M 156,196,140\nR 190,245,4608\ns_cbranch_vccz 181f\ns_waitcnt vmcnt(4)\ns_barrier\n181:\ns_add_u32 s67, s76, 8\ns_cmp_ge_u32 s67, 9\n"
"s_cselect_b32 s61, 9, 0\ns_sub_u32 s67, s67, s61\ns_mul_i32 s97, s67, 73728\ns_lshl_b32 s67, s67, 7\ns_add_u32 s67, s67, s62\ns_add_u32 m0, s60, 49152\n"
"s_add_u32 s61, s67, 0\nbuffer_load_dwordx4 v249, s[40:43], s61 offen lds\nv_mul_f32 v240, v144, v144\nv_mul_f32 v241, v145, v145\n"
"v_mul_f32 v242, v146, v146\nv_mul_f32 v243, v147, v147\nv_fma_f32 v240, v240, s70, v248\nv_fma_f32 v241, v241, s70, v248\n"
"v_fma_f32 v242, v242, s70, v248\ns_add_u32 m0, s60, 57344\ns_add_u32 s61, s67, 589824\nbuffer_load_dwordx4 v249, s[40:43], s61 offen lds\n"
"v_fma_f32 v243, v243, s70, v248\nv_mul_f32 v240, v240, v144\nv_mul_f32 v241, v241, v145\nv_mul_f32 v242, v242, v146\nv_mul_f32 v243, v243, v147\nE 240\n"
"E 241\nE 242\ns_add_u32 m0, s60, 65536\ns_add_u32 s61, s67, 1179648\nbuffer_load_dwordx4 v249, s[40:43], s61 offen lds\nE 243\n"
"v_add_f32 v240, 1.0, v240\nv_add_f32 v241, 1.0, v241\nv_add_f32 v242, 1.0, v242\nv_add_f32 v243, 1.0, v243\nv_rcp_f32 v240, v240\nv_rcp_f32 v241, v241\n"
"v_rcp_f32 v242, v242\ns_cbranch_vccnz 182f\ns_waitcnt vmcnt(7)\ns_barrier\n182:\nW 4\nZ 160,176,0\nR 192,244,8192\nM 160,180,8\nR 194,244,8704\n"
"Z 164,176,4\nR 196,244,12288\nM 164,180,12\nR 198,244,12800\nW 4\nZ 168,184,0\nR 176,245,8192\nM 168,188,8\nR 178,245,8704\nZ 172,184,4\n"
"R 180,245,12288\nM 172,188,12\nR 182,245,12800\nW 4\nM 160,192,16\nR 184,244,16384\nM 160,196,24\nR 186,244,16896\nM 164,192,20\nR 188,244,20480\n"
"M 164,196,28\nR 190,244,20992\nW 4\nM 168,176,16\nR 192,245,16384\nM 168,180,24\nR 194,245,16896\nM 172,176,20\nR 196,245,20480\nM 172,180,28\n"
"R 198,245,20992\nW 4\nM 160,184,32\nR 176,246,36864\nM 160,188,40\nR 178,246,37376\nM 164,184,36\nR 180,246,40960\nM 164,188,44\nR 182,246,41472\nW 4\n"
"M 168,192,32\nR 184,247,36864\nM 168,196,40\nR 186,247,37376\nM 172,192,36\nR 188,247,40960\nM 172,196,44\nR 190,247,41472\ns_cbranch_vccz 183f\n"
"s_waitcnt vmcnt(4)\ns_barrier\n183:\ns_add_u32 m0, s60, 73728\ns_add_u32 s61, s67, 1769472\nbuffer_load_dwordx4 v249, s[40:43], s61 offen lds\n"
"v_rcp_f32 v243, v243\nv_mul_f32 v144, v240, v144\nv_mul_f32 v145, v241, v145\nv_mul_f32 v146, v242, v146\nv_mul_f32 v147, v243, v147\n"
"v_mul_f32 v240, v148, v148\nv_mul_f32 v241, v149, v149\ns_add_u32 m0, s60, 81920\ns_add_u32 s61, s67, 2359296\n"
"buffer_load_dwordx4 v249, s[40:43], s61 offen lds\nv_mul_f32 v242, v150, v150\nv_mul_f32 v243, v151, v151\nv_fma_f32 v240, v240, s70, v248\n"
"v_fma_f32 v241, v241, s70, v248\nv_fma_f32 v242, v242, s70, v248\nv_fma_f32 v243, v243, s70, v248\nv_mul_f32 v240, v240, v148\n"
"v_mul_f32 v241, v241, v149\ns_add_u32 m0, s60, 90112\ns_add_u32 s61, s67, 2949120\nbuffer_load_dwordx4 v249, s[40:43], s61 offen lds\n"
"v_mul_f32 v242, v242, v150\nv_mul_f32 v243, v243, v151\nE 240\nE 241\nE 242\nE 243\nv_add_f32 v240, 1.0, v240\nv_add_f32 v241, 1.0, v241\n"
"s_cbranch_vccnz 184f\ns_waitcnt vmcnt(7)\ns_barrier\n184:\nW 4\nM 160,176,48\nR 192,246,45056\nM 160,180,56\nR 194,246,45568\nM 164,176,52\n"
"R 196,246,49152\nM 164,180,60\nR 198,246,49664\nW 4\nM 168,184,48\nR 176,247,45056\nM 168,188,56\nR 178,247,45568\nM 172,184,52\nR 180,247,49152\n"
"M 172,188,60\nR 182,247,49664\nW 4\nM 160,192,64\nR 184,246,53248\nM 160,196,72\nR 186,246,53760\nM 164,192,68\nR 188,246,57344\nM 164,196,76\n"
"R 190,246,57856\nW 4\nM 168,176,64\nR 192,247,53248\nM 168,180,72\nR 194,247,53760\nM 172,176,68\nR 196,247,57344\nM 172,180,76\nR 198,247,57856\nW 4\n"
"M 160,184,80\nR 176,244,24576\nM 160,188,88\nR 178,244,25088\nM 164,184,84\nR 180,244,28672\nM 164,188,92\nR 182,244,29184\nW 4\nM 168,192,80\n"
"R 184,245,24576\nM 168,196,88\nR 186,245,25088\nM 172,192,84\nR 188,245,28672\nM 172,196,92\nR 190,245,29184\ns_cbranch_vccz 185f\ns_waitcnt vmcnt(4)\n"
"s_barrier\n185:\ns_add_u32 m0, s60, 0\ns_add_u32 s61, s67, 3538944\nbuffer_load_dwordx4 v249, s[40:43], s61 offen lds\nv_add_f32 v242, 1.0, v242\n"
"v_add_f32 v243, 1.0, v243\nv_rcp_f32 v240, v240\nv_rcp_f32 v241, v241\nv_rcp_f32 v242, v242\nv_rcp_f32 v243, v243\nv_mul_f32 v148, v240, v148\n"
"s_add_u32 m0, s60, 8192\ns_add_u32 s61, s67, 4128768\nbuffer_load_dwordx4 v249, s[40:43], s61 offen lds\nv_mul_f32 v149, v241, v149\n"
"v_mul_f32 v150, v242, v150\nv_mul_f32 v151, v243, v151\nv_mul_f32 v240, v152, v152\nv_mul_f32 v241, v153, v153\nv_mul_f32 v242, v154, v154\n"
"v_mul_f32 v243, v155, v155\ns_add_u32 m0, s60, 16384\ns_add_u32 s61, s67, 4718592\nbuffer_load_dwordx4 v249, s[40:43], s61 offen lds\n"
"v_fma_f32 v240, v240, s70, v248\nv_fma_f32 v241, v241, s70, v248\nv_fma_f32 v242, v242, s70, v248\nv_fma_f32 v243, v243, s70, v248\n"
"v_mul_f32 v240, v240, v152\nv_mul_f32 v241, v241, v153\nv_mul_f32 v242, v242, v154\nv_mul_f32 v243, v243, v155\ns_cbranch_vccnz 186f\n"
"s_waitcnt vmcnt(7)\ns_barrier\n186:\nW 4\nM 160,176,96\nR 192,244,32768\nM 160,180,104\nR 194,244,33280\nM 164,176,100\nR 196,244,36864\nM 164,180,108\n"
"R 198,244,37376\nW 4\nM 168,184,96\nR 176,245,32768\nM 168,188,104\nR 178,245,33280\nM 172,184,100\nR 180,245,36864\nM 172,188,108\nR 182,245,37376\n"
"W 4\nM 160,192,112\nR 184,244,40960\nM 160,196,120\nR 186,244,41472\nM 164,192,116\nR 188,244,45056\nM 164,196,124\nR 190,244,45568\nW 4\n"
"M 168,176,112\nR 192,245,40960\nM 168,180,120\nR 194,245,41472\nM 172,176,116\nR 196,245,45056\nM 172,180,124\nR 198,245,45568\nW 4\nM 160,184,128\n"
"R 176,244,49152\nM 160,188,136\nR 178,244,49664\nM 164,184,132\nR 180,244,53248\nM 164,188,140\nR 182,244,53760\nW 4\nM 168,192,128\nR 184,245,49152\n"
"M 168,196,136\nR 186,245,49664\nM 172,192,132\nR 188,245,53248\nM 172,196,140\nR 190,245,53760\ns_cbranch_vccz 187f\ns_waitcnt vmcnt(4)\ns_barrier\n"
"187:\ns_nop 5\nv_mul_f32 v144, v144, v160\nv_mul_f32 v145, v145, v161\nv_mul_f32 v146, v146, v162\nv_mul_f32 v147, v147, v163\n"
"v_mul_f32 v148, v148, v164\nv_mul_f32 v149, v149, v165\nv_mul_f32 v150, v150, v166\nv_mul_f32 v151, v151, v167\nv_cvt_pk_bf16_f32 v232, v144, v145\n"
"v_cvt_pk_bf16_f32 v233, v146, v147\nv_cvt_pk_bf16_f32 v236, v148, v149\nv_cvt_pk_bf16_f32 v237, v150, v151\nE 240\nE 241\nE 242\nE 243\n"
"v_add_f32 v240, 1.0, v240\nv_add_f32 v241, 1.0, v241\nv_add_f32 v242, 1.0, v242\nv_add_f32 v243, 1.0, v243\nv_rcp_f32 v240, v240\nv_rcp_f32 v241, v241\n"
"v_rcp_f32 v242, v242\ns_cbranch_vccnz 188f\ns_waitcnt vmcnt(4)\ns_barrier\n188:\nW 4\nZ 144,176,0\nR 192,244,57344\nM 144,180,8\nR 194,244,57856\n"
"Z 148,176,4\nR 196,244,61440\nM 148,180,12\nR 198,244,61952\nW 4\nZ 160,184,0\nR 176,245,57344\nM 160,188,8\nR 178,245,57856\nZ 164,184,4\n"
"R 180,245,61440\nM 164,188,12\nR 182,245,61952\nW 4\nM 144,192,16\nR 184,246,4096\nM 144,196,24\nR 186,246,4608\nM 148,192,20\nR 188,246,8192\n"
"M 148,196,28\nR 190,246,8704\nW 4\nM 160,176,16\nR 192,247,4096\nM 160,180,24\nR 194,247,4608\nM 164,176,20\nR 196,247,8192\nM 164,180,28\n"
"R 198,247,8704\nW 4\nM 144,184,32\nR 176,246,12288\nM 144,188,40\nR 178,246,12800\nM 148,184,36\nR 180,246,16384\nM 148,188,44\nR 182,246,16896\nW 4\n"
"M 160,192,32\nR 184,247,12288\nM 160,196,40\nR 186,247,12800\nM 164,192,36\nR 188,247,16384\nM 164,196,44\nR 190,247,16896\ns_cbranch_vccz 189f\n"
"s_waitcnt vmcnt(1)\ns_barrier\nds_write_b128 v251, v[200:203] offset:0\nds_write_b128 v251, v[204:207] offset:1024\n"
"ds_write_b128 v251, v[208:211] offset:16384\nds_write_b128 v251, v[212:215] offset:17408\n189:\ns_add_u32 m0, s60, 24576\ns_add_u32 s61, s67, 0\n"
"buffer_load_dwordx4 v249, s[44:47], s61 offen lds\nv_rcp_f32 v243, v243\nv_mul_f32 v152, v240, v152\nv_mul_f32 v153, v241, v153\n"
"v_mul_f32 v154, v242, v154\nv_mul_f32 v155, v243, v155\nv_mul_f32 v240, v156, v156\nv_mul_f32 v241, v157, v157\nv_mul_f32 v242, v158, v158\n"
"s_add_u32 m0, s60, 32768\ns_add_u32 s61, s67, 589824\nbuffer_load_dwordx4 v249, s[44:47], s61 offen lds\nv_mul_f32 v243, v159, v159\n"
"v_fma_f32 v240, v240, s70, v248\nv_fma_f32 v241, v241, s70, v248\nv_fma_f32 v242, v242, s70, v248\nv_fma_f32 v243, v243, s70, v248\n"
"v_mul_f32 v240, v240, v156\nv_mul_f32 v241, v241, v157\nv_mul_f32 v242, v242, v158\ns_add_u32 m0, s60, 40960\ns_add_u32 s61, s67, 1179648\n"
"buffer_load_dwordx4 v249, s[44:47], s61 offen lds\nv_mul_f32 v243, v243, v159\nE 240\nE 241\nE 242\nE 243\nv_add_f32 v240, 1.0, v240\n"
"v_add_f32 v241, 1.0, v241\nbuffer_load_dword v254, off, s[52:55], s69 sc1\ns_cbranch_vccnz 190f\ns_waitcnt vmcnt(5)\ns_barrier\n"
"ds_write_b128 v251, v[200:203] offset:0\nds_write_b128 v251, v[204:207] offset:1024\nds_write_b128 v251, v[208:211] offset:16384\n"
"ds_write_b128 v251, v[212:215] offset:17408\n190:\nW 4\nM 144,176,48\nR 192,246,20480\nM 144,180,56\nR 194,246,20992\nM 148,176,52\nR 196,246,24576\n"
"M 148,180,60\nR 198,246,25088\nW 4\nM 160,184,48\nR 176,247,20480\nM 160,188,56\nR 178,247,20992\nM 164,184,52\nR 180,247,24576\nM 164,188,60\n"
"R 182,247,25088\nW 4\nM 144,192,64\nR 184,246,28672\nM 144,196,72\nR 186,246,29184\nM 148,192,68\nR 188,246,32768\nM 148,196,76\nR 190,246,33280\nW 4\n"
"M 160,176,64\nR 192,247,28672\nM 160,180,72\nR 194,247,29184\nM 164,176,68\nR 196,247,32768\nM 164,180,76\nR 198,247,33280\nW 4\nM 144,184,80\n"
"R 176,244,0\nM 144,188,88\nR 178,244,512\nM 148,184,84\nR 180,244,4096\nM 148,188,92\nR 182,244,4608\nW 4\nM 160,192,80\nR 184,245,0\nM 160,196,88\n"
"R 186,245,512\nM 164,192,84\nR 188,245,4096\nM 164,196,92\nR 190,245,4608\ns_cbranch_vccz 191f\ns_waitcnt vmcnt(2)\ns_barrier\n191:\n"
"s_add_u32 m0, s60, 49152\ns_add_u32 s61, s67, 1769472\nbuffer_load_dwordx4 v249, s[44:47], s61 offen lds\nv_add_f32 v242, 1.0, v242\n"
"v_add_f32 v243, 1.0, v243\nv_rcp_f32 v240, v240\nv_rcp_f32 v241, v241\nv_rcp_f32 v242, v242\nv_rcp_f32 v243, v243\nv_mul_f32 v156, v240, v156\n"
"s_add_u32 m0, s60, 57344\ns_add_u32 s61, s67, 2359296\nbuffer_load_dwordx4 v249, s[44:47], s61 offen lds\nv_mul_f32 v157, v241, v157\n"
"v_mul_f32 v158, v242, v158\nv_mul_f32 v159, v243, v159\nv_mul_f32 v152, v152, v168\nv_mul_f32 v153, v153, v169\nv_mul_f32 v154, v154, v170\n"
"v_mul_f32 v155, v155, v171\ns_add_u32 m0, s60, 65536\ns_add_u32 s61, s67, 2949120\nbuffer_load_dwordx4 v249, s[44:47], s61 offen lds\n"
"v_mul_f32 v156, v156, v172\nv_mul_f32 v157, v157, v173\nv_mul_f32 v158, v158, v174\nv_mul_f32 v159, v159, v175\nv_cvt_pk_bf16_f32 v234, v152, v153\n"
"v_cvt_pk_bf16_f32 v235, v154, v155\nv_cvt_pk_bf16_f32 v238, v156, v157\nv_cvt_pk_bf16_f32 v239, v158, v159\ns_cbranch_vccnz 192f\ns_waitcnt vmcnt(5)\n"
"s_barrier\n192:\nW 4\nM 144,176,96\nR 192,244,8192\nM 144,180,104\nR 194,244,8704\nM 148,176,100\nR 196,244,12288\nM 148,180,108\nR 198,244,12800\nW 4\n"
"M 160,184,96\nR 176,245,8192\nM 160,188,104\nR 178,245,8704\nM 164,184,100\nR 180,245,12288\nM 164,188,108\nR 182,245,12800\nW 4\nM 144,192,112\n"
"R 184,244,16384\nM 144,196,120\nR 186,244,16896\nM 148,192,116\nR 188,244,20480\nM 148,196,124\nR 190,244,20992\nW 4\nM 160,176,112\nR 192,245,16384\n"
"M 160,180,120\nR 194,245,16896\nM 164,176,116\nR 196,245,20480\nM 164,180,124\nR 198,245,20992\nW 4\nM 144,184,128\nR 176,244,24576\nM 144,188,136\n"
"R 178,244,25088\nM 148,184,132\nR 180,244,28672\nM 148,188,140\nR 182,244,29184\nW 4\nM 160,192,128\nR 184,245,24576\nM 160,196,136\nR 186,245,25088\n"
"M 164,192,132\nR 188,245,28672\nM 164,196,140\nR 190,245,29184\ns_cbranch_vccz 193f\ns_waitcnt vmcnt(1)\ns_barrier\n193:\ns_add_u32 m0, s60, 73728\n"
"s_add_u32 s61, s67, 3538944\nbuffer_load_dwordx4 v249, s[44:47], s61 offen lds\ns_nop 2\nv_mul_f32 v240, v144, v144\nv_mul_f32 v241, v145, v145\n"
"v_mul_f32 v242, v146, v146\nv_mul_f32 v243, v147, v147\nv_fma_f32 v240, v240, s70, v248\nv_fma_f32 v241, v241, s70, v248\n"
"v_fma_f32 v242, v242, s70, v248\ns_add_u32 m0, s60, 81920\ns_add_u32 s61, s67, 4128768\nbuffer_load_dwordx4 v249, s[44:47], s61 offen lds\n"
"v_fma_f32 v243, v243, s70, v248\nv_mul_f32 v240, v240, v144\nv_mul_f32 v241, v241, v145\nv_mul_f32 v242, v242, v146\nv_mul_f32 v243, v243, v147\nE 240\n"
"E 241\nE 242\ns_add_u32 m0, s60, 90112\ns_add_u32 s61, s67, 4718592\nbuffer_load_dwordx4 v249, s[44:47], s61 offen lds\nE 243\n"
"v_add_f32 v240, 1.0, v240\nv_add_f32 v241, 1.0, v241\nv_add_f32 v242, 1.0, v242\nv_add_f32 v243, 1.0, v243\nv_rcp_f32 v240, v240\nv_rcp_f32 v241, v241\n"
"v_rcp_f32 v242, v242\ns_cbranch_vccnz 194f\ns_waitcnt vmcnt(4)\ns_barrier\n194:\nW 4\nZ 152,176,0\nR 192,244,32768\nM 152,180,8\nR 194,244,33280\n"
"Z 156,176,4\nR 196,244,36864\nM 156,180,12\nR 198,244,37376\nW 4\nZ 168,184,0\nR 176,245,32768\nM 168,188,8\nR 178,245,33280\nZ 172,184,4\n"
"R 180,245,36864\nM 172,188,12\nR 182,245,37376\nW 4\nM 152,192,16\nR 184,244,40960\nM 152,196,24\nR 186,244,41472\nM 156,192,20\nR 188,244,45056\n"
"M 156,196,28\nR 190,244,45568\nW 4\nM 168,176,16\nR 192,245,40960\nM 168,180,24\nR 194,245,41472\nM 172,176,20\nR 196,245,45056\nM 172,180,28\n"
"R 198,245,45568\nW 4\nM 152,184,32\nR 176,244,49152\nM 152,188,40\nR 178,244,49664\nM 156,184,36\nR 180,244,53248\nM 156,188,44\nR 182,244,53760\nW 4\n"
"M 168,192,32\nR 184,245,49152\nM 168,196,40\nR 186,245,49664\nM 172,192,36\nR 188,245,53248\nM 172,196,44\nR 190,245,53760\ns_cbranch_vccz 195f\n"
"s_waitcnt vmcnt(1)\ns_barrier\n195:\ns_mov_b64 s[48:49], s[86:87]\ns_mov_b32 s50, 5308416\ns_mul_i32 s98, s76, 73728\ns_add_u32 s64, s63, 1024\n"
"s_add_u32 m0, s60, 0\ns_add_u32 s61, s64, s90\nbuffer_load_dwordx4 v250, s[48:51], s61 offen lds\ns_cmp_ge_u32 s71, 4\ns_cselect_b32 s74, -1, 1\n"
"s_lshl_b32 s74, s74, 13\nv_add_u32 v249, s74, v251\nv_rcp_f32 v243, v243\nv_mul_f32 v144, v240, v144\nv_mul_f32 v145, v241, v145\n"
"v_mul_f32 v146, v242, v146\nv_mul_f32 v147, v243, v147\nv_mul_f32 v240, v148, v148\nv_mul_f32 v241, v149, v149\nv_mul_f32 v242, v150, v150\n"
"v_mul_f32 v243, v151, v151\nv_fma_f32 v240, v240, s70, v248\nv_fma_f32 v241, v241, s70, v248\ns_add_u32 m0, s60, 8192\ns_add_u32 s61, s64, s91\n"
"buffer_load_dwordx4 v250, s[48:51], s61 offen lds\nv_fma_f32 v242, v242, s70, v248\nv_fma_f32 v243, v243, s70, v248\nv_mul_f32 v240, v240, v148\n"
"v_mul_f32 v241, v241, v149\nv_mul_f32 v242, v242, v150\nv_mul_f32 v243, v243, v151\nE 240\nE 241\nE 242\nE 243\nv_add_f32 v240, 1.0, v240\n"
"v_add_f32 v241, 1.0, v241\ns_add_u32 m0, s60, 16384\ns_add_u32 s61, s64, s92\nbuffer_load_dwordx4 v250, s[48:51], s61 offen lds\n"
"ds_read_b128 v[0:3], v251 offset:32768\nds_read_b128 v[4:7], v251 offset:33792\nds_read_b128 v[16:19], v249 offset:32768\n"
"ds_read_b128 v[20:23], v249 offset:33792\nds_read_b128 v[8:11], v251 offset:49152\nds_read_b128 v[12:15], v251 offset:50176\n"
"ds_read_b128 v[24:27], v249 offset:49152\nds_read_b128 v[28:31], v249 offset:50176\nds_read_b128 v[32:35], v249 offset:0\n"
"ds_read_b128 v[36:39], v249 offset:1024\nds_read_b128 v[40:43], v249 offset:16384\nds_read_b128 v[44:47], v249 offset:17408\ns_cbranch_vccnz 196f\n"
"s_waitcnt vmcnt(4)\ns_barrier\n196:\nW 15\nM 152,176,48\nR 192,244,57344\nM 152,180,56\nR 194,244,57856\nM 156,176,52\nR 196,244,61440\nM 156,180,60\n"
"R 198,244,61952\nW 15\nM 168,184,48\nR 176,245,57344\nM 168,188,56\nR 178,245,57856\nM 172,184,52\nR 180,245,61440\nM 172,188,60\nR 182,245,61952\nW 4\n"
"M 152,192,64\nR 184,246,4096\nM 152,196,72\nR 186,246,4608\nM 156,192,68\nR 188,246,8192\nM 156,196,76\nR 190,246,8704\nW 4\nM 168,176,64\n"
"R 192,247,4096\nM 168,180,72\nR 194,247,4608\nM 172,176,68\nR 196,247,8192\nM 172,180,76\nR 198,247,8704\nW 4\nM 152,184,80\nR 176,246,12288\n"
"M 152,188,88\nR 178,246,12800\nM 156,184,84\nR 180,246,16384\nM 156,188,92\nR 182,246,16896\nW 4\nM 168,192,80\nR 184,247,12288\nM 168,196,88\n"
"R 186,247,12800\nM 172,192,84\nR 188,247,16384\nM 172,196,92\nR 190,247,16896\ns_cbranch_vccz 197f\ns_waitcnt vmcnt(1)\ns_barrier\n197:\n"
"s_add_u32 s74, s66, 0\ns_and_b32 s74, s74, 7\ns_lshl_b32 s74, s74, 7\ns_add_u32 s62, s63, s74\ns_add_u32 m0, s60, 24576\ns_add_u32 s61, s62, s90\n"
"buffer_load_dwordx4 v250, s[48:51], s61 offen lds\nv_add_f32 v242, 1.0, v242\nv_add_f32 v243, 1.0, v243\nv_rcp_f32 v240, v240\nv_rcp_f32 v241, v241\n"
"v_rcp_f32 v242, v242\nv_rcp_f32 v243, v243\nv_mul_f32 v148, v240, v148\nv_mul_f32 v149, v241, v149\nv_mul_f32 v150, v242, v150\n"
"v_mul_f32 v151, v243, v151\ns_add_u32 m0, s60, 32768\ns_add_u32 s61, s62, s91\nbuffer_load_dwordx4 v250, s[48:51], s61 offen lds\n"
"v_mul_f32 v240, v160, v160\nv_mul_f32 v241, v161, v161\nv_mul_f32 v242, v162, v162\nv_mul_f32 v243, v163, v163\nv_fma_f32 v240, v240, s70, v248\n"
"v_fma_f32 v241, v241, s70, v248\nv_fma_f32 v242, v242, s70, v248\nv_fma_f32 v243, v243, s70, v248\nv_mul_f32 v240, v240, v160\n"
"v_mul_f32 v241, v241, v161\ns_add_u32 m0, s60, 40960\ns_add_u32 s61, s62, s92\nbuffer_load_dwordx4 v250, s[48:51], s61 offen lds\n"
"v_mul_f32 v242, v242, v162\nv_mul_f32 v243, v243, v163\nv_xor_b32 v48, 0, v252\nv_xor_b32 v49, 32, v252\nv_xor_b32 v50, 4096, v252\n"
"v_xor_b32 v51, 4128, v252\nv_add_u32 v52, 61440, v48\nv_add_u32 v53, 61440, v49\nv_add_u32 v54, 61440, v50\nv_add_u32 v55, 61440, v51\n"
"s_waitcnt vmcnt(12)\nv_readfirstlane_b32 s74, v254\ns_sub_u32 s68, s68, 98304\ns_nop 0\ns_cmp_eq_u32 s74, s72\ns_cbranch_scc1 61f\n"
"s_mov_b32 s75, 0x40000\n60:\ns_sleep 2\nbuffer_load_dword v254, off, s[52:55], s69 sc1\ns_waitcnt vmcnt(0)\nv_readfirstlane_b32 s74, v254\n"
"s_sub_u32 s75, s75, 1\ns_cmp_eq_u32 s75, 0\ns_cbranch_scc1 61f\ns_cmp_lg_u32 s74, s72\ns_cbranch_scc1 60b\n61:\n"
"buffer_load_dwordx4 v[56:59], v251, s[52:55], s68 offen sc1\nbuffer_load_dwordx4 v[60:63], v251, s[52:55], s68 offen offset:1024 sc1\n"
"buffer_load_dwordx4 v[64:67], v249, s[52:55], s68 offen sc1\nbuffer_load_dwordx4 v[68:71], v249, s[52:55], s68 offen offset:1024 sc1\n"
"s_cbranch_vccnz 198f\ns_waitcnt vmcnt(8)\ns_barrier\n198:\nW 4\nM 152,176,96\nR 192,246,20480\nM 152,180,104\nR 194,246,20992\nM 156,176,100\n"
"R 196,246,24576\nM 156,180,108\nR 198,246,25088\nW 4\nM 168,184,96\nR 176,247,20480\nM 168,188,104\nR 178,247,20992\nM 172,184,100\nR 180,247,24576\n"
"M 172,188,108\nR 182,247,25088\nW 4\nM 152,192,112\nR 184,246,28672\nM 152,196,120\nR 186,246,29184\nM 156,192,116\nR 188,246,32768\nM 156,196,124\n"
"R 190,246,33280\nW 4\nM 168,176,112\nR 192,247,28672\nM 168,180,120\nR 194,247,29184\nM 172,176,116\nR 196,247,32768\nM 172,180,124\nR 198,247,33280\n"
"W 4\nM 152,184,128\nR 176,48,0\nM 152,188,136\nR 178,48,2048\nM 156,184,132\nR 180,50,0\nM 156,188,140\nR 182,50,2048\nW 4\nM 168,192,128\nR 184,49,0\n"
"M 168,196,136\nR 186,49,2048\nM 172,192,132\nR 188,51,0\nM 172,196,140\nR 190,51,2048\ns_cbranch_vccz 199f\ns_waitcnt vmcnt(5)\ns_barrier\n"
"ds_write_b128 v251, v[216:219] offset:0\nds_write_b128 v251, v[220:223] offset:1024\nds_write_b128 v251, v[224:227] offset:16384\n"
"ds_write_b128 v251, v[228:231] offset:17408\nds_write_b128 v251, v[232:235] offset:32768\nds_write_b128 v251, v[236:239] offset:33792\n199:\n"
"s_add_u32 m0, s60, 49152\ns_add_u32 s61, s64, s93\nbuffer_load_dwordx4 v250, s[48:51], s61 offen lds\ns_nop 2\nv_mul_f32 v144, v144, v152\n"
"v_mul_f32 v145, v145, v153\nv_mul_f32 v146, v146, v154\nv_mul_f32 v147, v147, v155\nv_mul_f32 v148, v148, v156\nv_mul_f32 v149, v149, v157\n"
"v_mul_f32 v150, v150, v158\ns_add_u32 m0, s60, 57344\ns_add_u32 s61, s64, s98\nbuffer_load_dwordx4 v250, s[48:51], s61 offen lds\n"
"v_mul_f32 v151, v151, v159\nv_cvt_pk_bf16_f32 v72, v144, v145\nv_cvt_pk_bf16_f32 v73, v146, v147\nv_cvt_pk_bf16_f32 v76, v148, v149\n"
"v_cvt_pk_bf16_f32 v77, v150, v151\nE 240\nE 241\nE 242\ns_add_u32 m0, s60, 65536\ns_add_u32 s61, s64, s94\n"
"buffer_load_dwordx4 v250, s[48:51], s61 offen lds\nE 243\nv_add_f32 v240, 1.0, v240\nv_add_f32 v241, 1.0, v241\nv_add_f32 v242, 1.0, v242\n"
"v_add_f32 v243, 1.0, v243\nv_rcp_f32 v240, v240\nv_rcp_f32 v241, v241\nv_rcp_f32 v242, v242\ns_cbranch_vccnz 200f\ns_waitcnt vmcnt(8)\ns_barrier\n"
"ds_write_b128 v251, v[216:219] offset:0\nds_write_b128 v251, v[220:223] offset:1024\nds_write_b128 v251, v[224:227] offset:16384\n"
"ds_write_b128 v251, v[228:231] offset:17408\nds_write_b128 v251, v[232:235] offset:32768\nds_write_b128 v251, v[236:239] offset:33792\n200:\nW 4\n"
"Z 112,176,0\nR 192,48,8192\nM 112,180,16\nR 194,48,10240\nZ 116,176,4\nR 196,50,8192\nM 116,180,20\nR 198,50,10240\nW 4\nZ 120,184,0\nR 176,49,8192\n"
"M 120,188,16\nR 178,49,10240\nZ 124,184,4\nR 180,51,8192\nM 124,188,20\nR 182,51,10240\nW 4\nM 112,192,8\nR 184,48,16384\nM 112,196,24\nR 186,48,18432\n"
"M 116,192,12\nR 188,50,16384\nM 116,196,28\nR 190,50,18432\nW 4\nM 120,176,8\nR 192,49,16384\nM 120,180,24\nR 194,49,18432\nM 124,176,12\n"
"R 196,51,16384\nM 124,180,28\nR 198,51,18432\nW 4\nM 112,184,200\nR 176,48,24576\nM 112,188,32\nR 178,48,26624\nM 116,184,204\nR 180,50,24576\n"
"M 116,188,36\nR 182,50,26624\nW 4\nM 120,192,200\nR 184,49,24576\nM 120,196,32\nR 186,49,26624\nM 124,192,204\nR 188,51,24576\nM 124,196,36\n"
"R 190,51,26624\ns_cbranch_vccz 201f\ns_waitcnt vmcnt(1)\ns_barrier\n201:\ns_add_u32 m0, s60, 73728\ns_add_u32 s61, s62, s93\n"
"buffer_load_dwordx4 v250, s[48:51], s61 offen lds\nv_rcp_f32 v243, v243\nv_mul_f32 v160, v240, v160\nv_mul_f32 v161, v241, v161\n"
"v_mul_f32 v162, v242, v162\nv_mul_f32 v163, v243, v163\nv_mul_f32 v240, v164, v164\nv_mul_f32 v241, v165, v165\ns_add_u32 m0, s60, 81920\n"
"s_add_u32 s61, s62, s98\nbuffer_load_dwordx4 v250, s[48:51], s61 offen lds\nv_mul_f32 v242, v166, v166\nv_mul_f32 v243, v167, v167\n"
"v_fma_f32 v240, v240, s70, v248\nv_fma_f32 v241, v241, s70, v248\nv_fma_f32 v242, v242, s70, v248\nv_fma_f32 v243, v243, s70, v248\n"
"v_mul_f32 v240, v240, v164\ns_add_u32 m0, s60, 90112\ns_add_u32 s61, s62, s94\nbuffer_load_dwordx4 v250, s[48:51], s61 offen lds\n"
"v_mul_f32 v241, v241, v165\nv_mul_f32 v242, v242, v166\nv_mul_f32 v243, v243, v167\nE 240\nE 241\nE 242\nE 243\nv_add_f32 v240, 1.0, v240\n"
"s_cbranch_vccnz 202f\ns_waitcnt vmcnt(4)\ns_barrier\n202:\nW 4\nZ 128,176,0\nR 192,48,32768\nM 128,180,16\nR 194,48,34816\nZ 132,176,4\nR 196,50,32768\n"
"M 132,180,20\nR 198,50,34816\nW 4\nZ 136,184,0\nR 176,49,32768\nM 136,188,16\nR 178,49,34816\nZ 140,184,4\nR 180,51,32768\nM 140,188,20\n"
"R 182,51,34816\nW 4\nM 128,192,8\nR 184,48,40960\nM 128,196,24\nR 186,48,43008\nM 132,192,12\nR 188,50,40960\nM 132,196,28\nR 190,50,43008\nW 4\n"
"M 136,176,8\nR 192,49,40960\nM 136,180,24\nR 194,49,43008\nM 140,176,12\nR 196,51,40960\nM 140,180,28\nR 198,51,43008\nW 4\nM 128,184,200\n"
"R 176,48,49152\nM 128,188,32\nR 178,48,51200\nM 132,184,204\nR 180,50,49152\nM 132,188,36\nR 182,50,51200\nW 4\nM 136,192,200\nR 184,49,49152\n"
"M 136,196,32\nR 186,49,51200\nM 140,192,204\nR 188,51,49152\nM 140,196,36\nR 190,51,51200\ns_cbranch_vccz 203f\ns_waitcnt vmcnt(1)\ns_barrier\n203:\n"
"s_add_u32 m0, s60, 0\ns_add_u32 s61, s64, s95\nbuffer_load_dwordx4 v250, s[48:51], s61 offen lds\nds_read_b128 v[80:83], v249 offset:0\n"
"ds_read_b128 v[84:87], v249 offset:1024\nv_add_f32 v241, 1.0, v241\nv_add_f32 v242, 1.0, v242\nv_add_f32 v243, 1.0, v243\nv_rcp_f32 v240, v240\n"
"v_rcp_f32 v241, v241\nv_rcp_f32 v242, v242\nv_rcp_f32 v243, v243\ns_add_u32 m0, s60, 8192\ns_add_u32 s61, s64, s96\n"
"buffer_load_dwordx4 v250, s[48:51], s61 offen lds\nv_mul_f32 v164, v240, v164\nv_mul_f32 v165, v241, v165\nv_mul_f32 v166, v242, v166\n"
"v_mul_f32 v167, v243, v167\nv_mul_f32 v160, v160, v168\nv_mul_f32 v161, v161, v169\nv_mul_f32 v162, v162, v170\nv_mul_f32 v163, v163, v171\n"
"v_mul_f32 v164, v164, v172\ns_add_u32 m0, s60, 16384\ns_add_u32 s61, s64, s97\nbuffer_load_dwordx4 v250, s[48:51], s61 offen lds\n"
"v_mul_f32 v165, v165, v173\nv_mul_f32 v166, v166, v174\nv_mul_f32 v167, v167, v175\nv_cvt_pk_bf16_f32 v74, v160, v161\n"
"v_cvt_pk_bf16_f32 v75, v162, v163\nv_cvt_pk_bf16_f32 v78, v164, v165\nv_cvt_pk_bf16_f32 v79, v166, v167\ns_cbranch_vccz 204f\n"
"ds_write_b128 v251, v[72:75] offset:49152\nds_write_b128 v251, v[76:79] offset:50176\n204:\ns_waitcnt vmcnt(9)\ns_cbranch_vccnz 205f\n"
"s_waitcnt vmcnt(4)\ns_barrier\nds_write_b128 v251, v[72:75] offset:49152\nds_write_b128 v251, v[76:79] offset:50176\n205:\nW 6\nM 112,176,208\n"
"R 192,48,57344\nM 112,180,40\nR 194,48,59392\nM 116,176,212\nR 196,50,57344\nM 116,180,44\nR 198,50,59392\nW 6\nM 120,184,208\nR 176,49,57344\n"
"M 120,188,40\nR 178,49,59392\nM 124,184,212\nR 180,51,57344\nM 124,188,44\nR 182,51,59392\nW 4\nM 112,192,56\nR 184,52,4096\nM 112,196,64\n"
"R 186,52,6144\nM 116,192,60\nR 188,54,4096\nM 116,196,68\nR 190,54,6144\nW 4\nM 120,176,56\nR 192,53,4096\nM 120,180,64\nR 194,53,6144\nM 124,176,60\n"
"R 196,55,4096\nM 124,180,68\nR 198,55,6144\nW 4\nM 112,184,216\nR 176,52,12288\nM 112,188,80\nR 178,52,14336\nM 116,184,220\nR 180,54,12288\n"
"M 116,188,84\nR 182,54,14336\nW 4\nM 120,192,216\nR 184,53,12288\nM 120,196,80\nR 186,53,14336\nM 124,192,220\nR 188,55,12288\nM 124,196,84\n"
"R 190,55,14336\ns_cbranch_vccz 206f\ns_waitcnt vmcnt(1)\ns_barrier\n206:\ns_add_u32 m0, s60, 24576\ns_add_u32 s61, s62, s95\n"
"buffer_load_dwordx4 v250, s[48:51], s61 offen lds\nds_read_b128 v[88:91], v249 offset:16384\nds_read_b128 v[92:95], v249 offset:17408\n"
"s_add_u32 m0, s60, 32768\ns_add_u32 s61, s62, s96\nbuffer_load_dwordx4 v250, s[48:51], s61 offen lds\nds_read_b128 v[96:99], v249 offset:32768\n"
"ds_read_b128 v[100:103], v249 offset:33792\ns_add_u32 m0, s60, 40960\ns_add_u32 s61, s62, s97\nbuffer_load_dwordx4 v250, s[48:51], s61 offen lds\n"
"v_and_b32 v248, 0xf00, v253\nv_add_u32 v248, v248, v253\ns_cbranch_vccnz 207f\ns_waitcnt vmcnt(4)\ns_barrier\n207:\nW 8\nM 128,176,208\nR 192,52,20480\n"
"M 128,180,40\nR 194,52,22528\nM 132,176,212\nR 196,54,20480\nM 132,180,44\nR 198,54,22528\nW 8\nM 136,184,208\nR 176,53,20480\nM 136,188,40\n"
"R 178,53,22528\nM 140,184,212\nR 180,55,20480\nM 140,188,44\nR 182,55,22528\nW 4\nM 128,192,56\nR 184,52,28672\nM 128,196,64\nR 186,52,30720\n"
"M 132,192,60\nR 188,54,28672\nM 132,196,68\nR 190,54,30720\nW 4\nM 136,176,56\nR 192,53,28672\nM 136,180,64\nR 194,53,30720\nM 140,176,60\n"
"R 196,55,28672\nM 140,180,68\nR 198,55,30720\nW 4\nM 128,184,216\nR 176,48,0\nM 128,188,80\nR 178,48,2048\nM 132,184,220\nR 180,50,0\nM 132,188,84\n"
"R 182,50,2048\nW 4\nM 136,192,216\nR 184,49,0\nM 136,196,80\nR 186,49,2048\nM 140,192,220\nR 188,51,0\nM 140,196,84\nR 190,51,2048\n"
"s_cbranch_vccz 208f\ns_waitcnt vmcnt(1)\ns_barrier\n208:\ns_add_u32 s74, s66, 1\ns_and_b32 s74, s74, 7\ns_lshl_b32 s74, s74, 7\n"
"s_add_u32 s64, s63, s74\ns_add_u32 m0, s60, 49152\ns_add_u32 s61, s64, s90\nbuffer_load_dwordx4 v250, s[48:51], s61 offen lds\n"
"s_add_u32 m0, s60, 57344\ns_add_u32 s61, s64, s91\nbuffer_load_dwordx4 v250, s[48:51], s61 offen lds\nds_read_b128 v[104:107], v249 offset:49152\n"
"s_add_u32 m0, s60, 65536\ns_add_u32 s61, s64, s92\nbuffer_load_dwordx4 v250, s[48:51], s61 offen lds\nds_read_b128 v[108:111], v249 offset:50176\n"
"s_cbranch_vccnz 209f\ns_waitcnt vmcnt(4)\ns_barrier\n209:\nW 6\nM 112,176,224\nR 192,48,8192\nM 112,180,88\nR 194,48,10240\nM 116,176,228\n"
"R 196,50,8192\nM 116,180,92\nR 198,50,10240\nW 6\nM 120,184,224\nR 176,49,8192\nM 120,188,88\nR 178,49,10240\nM 124,184,228\nR 180,51,8192\n"
"M 124,188,92\nR 182,51,10240\nW 4\nM 112,192,232\nR 184,48,16384\nM 112,196,96\nR 186,48,18432\nM 116,192,236\nR 188,50,16384\nM 116,196,100\n"
"R 190,50,18432\nW 4\nM 120,176,232\nR 192,49,16384\nM 120,180,96\nR 194,49,18432\nM 124,176,236\nR 196,51,16384\nM 124,180,100\nR 198,51,18432\nW 4\n"
"M 112,184,72\nR 176,48,24576\nM 112,188,104\nR 178,48,26624\nM 116,184,76\nR 180,50,24576\nM 116,188,108\nR 182,50,26624\nW 4\nM 120,192,72\n"
"R 184,49,24576\nM 120,196,104\nR 186,49,26624\nM 124,192,76\nR 188,51,24576\nM 124,196,108\nR 190,51,26624\ns_cbranch_vccz 210f\ns_waitcnt vmcnt(1)\n"
"s_barrier\n210:\ns_add_u32 m0, s60, 98304\ns_add_u32 s61, s64, s93\nbuffer_load_dwordx4 v250, s[48:51], s61 offen lds\ns_nop 2\n"
"v_cvt_pk_bf16_f32 v144, v112, v113\ns_add_u32 m0, s60, 106496\ns_add_u32 s61, s64, s98\nbuffer_load_dwordx4 v250, s[48:51], s61 offen lds\n"
"v_cvt_pk_bf16_f32 v145, v114, v115\nv_cvt_pk_bf16_f32 v146, v116, v117\ns_add_u32 m0, s60, 114688\ns_add_u32 s61, s64, s94\n"
"buffer_load_dwordx4 v250, s[48:51], s61 offen lds\nv_cvt_pk_bf16_f32 v147, v118, v119\ns_add_u32 m0, s60, 73728\ns_add_u32 s61, s64, s95\n"
"buffer_load_dwordx4 v250, s[48:51], s61 offen lds\nv_cvt_pk_bf16_f32 v148, v120, v121\nv_cvt_pk_bf16_f32 v149, v122, v123\ns_add_u32 m0, s60, 81920\n"
"s_add_u32 s61, s64, s96\nbuffer_load_dwordx4 v250, s[48:51], s61 offen lds\nv_cvt_pk_bf16_f32 v150, v124, v125\ns_add_u32 m0, s60, 90112\n"
"s_add_u32 s61, s64, s97\nbuffer_load_dwordx4 v250, s[48:51], s61 offen lds\nv_cvt_pk_bf16_f32 v151, v126, v127\ns_add_u32 s74, s65, 131072\n"
"s_add_u32 s75, s74, 4096\nbuffer_store_dwordx4 v[144:147], v253, s[52:55], s74 offen sc1\n"
"buffer_store_dwordx4 v[148:151], v253, s[52:55], s75 offen sc1\ns_cbranch_vccnz 211f\ns_waitcnt vmcnt(9)\ns_barrier\n211:\nW 4\nM 128,176,224\n"
"R 192,48,32768\nM 128,180,88\nR 194,48,34816\nM 132,176,228\nR 196,50,32768\nM 132,180,92\nR 198,50,34816\nW 4\nM 136,184,224\nR 176,49,32768\n"
"M 136,188,88\nR 178,49,34816\nM 140,184,228\nR 180,51,32768\nM 140,188,92\nR 182,51,34816\nW 4\nM 128,192,232\nR 184,48,40960\nM 128,196,96\n"
"R 186,48,43008\nM 132,192,236\nR 188,50,40960\nM 132,196,100\nR 190,50,43008\nW 4\nM 136,176,232\nR 192,49,40960\nM 136,180,96\nR 194,49,43008\n"
"M 140,176,236\nR 196,51,40960\nM 140,180,100\nR 198,51,43008\nW 4\nM 128,184,72\nR 176,48,49152\nM 128,188,104\nR 178,48,51200\nM 132,184,76\n"
"R 180,50,49152\nM 132,188,108\nR 182,50,51200\nW 4\nM 136,192,72\nR 184,49,49152\nM 136,196,104\nR 186,49,51200\nM 140,192,76\nR 188,51,49152\n"
"M 140,196,108\nR 190,51,51200\ns_cbranch_vccz 212f\ns_waitcnt vmcnt(6)\ns_barrier\n212:\ns_add_u32 s74, s66, 2\ns_and_b32 s74, s74, 7\n"
"s_lshl_b32 s74, s74, 7\ns_add_u32 s62, s63, s74\ns_add_u32 m0, s60, 0\ns_add_u32 s61, s62, s90\nbuffer_load_dwordx4 v250, s[48:51], s61 offen lds\n"
"v_cvt_pk_bf16_f32 v144, v128, v129\nv_cvt_pk_bf16_f32 v145, v130, v131\nv_cvt_pk_bf16_f32 v146, v136, v137\ns_add_u32 m0, s60, 8192\n"
"s_add_u32 s61, s62, s91\nbuffer_load_dwordx4 v250, s[48:51], s61 offen lds\nv_cvt_pk_bf16_f32 v147, v138, v139\nv_cvt_pk_bf16_f32 v148, v132, v133\n"
"v_cvt_pk_bf16_f32 v149, v134, v135\ns_add_u32 m0, s60, 16384\ns_add_u32 s61, s62, s92\nbuffer_load_dwordx4 v250, s[48:51], s61 offen lds\n"
"v_cvt_pk_bf16_f32 v150, v140, v141\nv_cvt_pk_bf16_f32 v151, v142, v143\ns_add_u32 s74, s66, 0\ns_and_b32 s74, s74, 7\ns_lshl_b32 s74, s74, 14\n"
"s_add_u32 s74, s74, s65\nbuffer_store_dwordx4 v[144:147], v248, s[52:55], s74 offen sc1\n"
"buffer_store_dwordx4 v[148:151], v248, s[52:55], s74 offen offset:256 sc1\ns_cbranch_vccnz 213f\ns_waitcnt vmcnt(11)\ns_barrier\n213:\nW 4\n"
"Z 112,176,0\nR 192,48,57344\nM 112,180,16\nR 194,48,59392\nZ 116,176,4\nR 196,50,57344\nM 116,180,20\nR 198,50,59392\nW 4\nZ 120,184,0\nR 176,49,57344\n"
"M 120,188,16\nR 178,49,59392\nZ 124,184,4\nR 180,51,57344\nM 124,188,20\nR 182,51,59392\nW 4\nM 112,192,8\nR 184,52,4096\nM 112,196,24\nR 186,52,6144\n"
"M 116,192,12\nR 188,54,4096\nM 116,196,28\nR 190,54,6144\nW 4\nM 120,176,8\nR 192,53,4096\nM 120,180,24\nR 194,53,6144\nM 124,176,12\nR 196,55,4096\n"
"M 124,180,28\nR 198,55,6144\nW 4\nM 112,184,200\nR 176,52,36864\nM 112,188,32\nR 178,52,38912\nM 116,184,204\nR 180,54,36864\nM 116,188,36\n"
"R 182,54,38912\nW 4\nM 120,192,200\nR 184,53,36864\nM 120,196,32\nR 186,53,38912\nM 124,192,204\nR 188,55,36864\nM 124,196,36\nR 190,55,38912\n"
"s_cbranch_vccz 214f\ns_waitcnt vmcnt(8)\ns_barrier\n214:\ns_add_u32 m0, s60, 24576\ns_add_u32 s61, s62, s93\n"
"buffer_load_dwordx4 v250, s[48:51], s61 offen lds\ns_add_u32 m0, s60, 32768\ns_add_u32 s61, s62, s98\n"
"buffer_load_dwordx4 v250, s[48:51], s61 offen lds\ns_add_u32 m0, s60, 40960\ns_add_u32 s61, s62, s94\n"
"buffer_load_dwordx4 v250, s[48:51], s61 offen lds\ns_cbranch_vccnz 215f\ns_waitcnt vmcnt(11)\ns_barrier\n215:\nW 4\nM 112,176,208\nR 192,52,45056\n"
"M 112,180,40\nR 194,52,47104\nM 116,176,212\nR 196,54,45056\nM 116,180,44\nR 198,54,47104\nW 4\nM 120,184,208\nR 176,53,45056\nM 120,188,40\n"
"R 178,53,47104\nM 124,184,212\nR 180,55,45056\nM 124,188,44\nR 182,55,47104\nW 4\nM 112,192,56\nR 184,52,53248\nM 112,196,64\nR 186,52,55296\n"
"M 116,192,60\nR 188,54,53248\nM 116,196,68\nR 190,54,55296\nW 4\nM 120,176,56\nR 192,53,53248\nM 120,180,64\nR 194,53,55296\nM 124,176,60\n"
"R 196,55,53248\nM 124,180,68\nR 198,55,55296\nW 4\nM 112,184,216\nR 176,52,12288\nM 112,188,80\nR 178,52,14336\nM 116,184,220\nR 180,54,12288\n"
"M 116,188,84\nR 182,54,14336\nW 4\nM 120,192,216\nR 184,53,12288\nM 120,196,80\nR 186,53,14336\nM 124,192,220\nR 188,55,12288\nM 124,196,84\n"
"R 190,55,14336\ns_cbranch_vccz 216f\ns_waitcnt vmcnt(3)\ns_barrier\ns_sub_u32 s75, s73, 57344\ns_cmp_lg_u32 s71, 4\ns_cbranch_scc1 41f\n"
"s_mov_b64 exec, 1\nv_mov_b32 v251, s72\nbuffer_store_dword v251, off, s[52:55], s75 sc1\ns_mov_b64 exec, -1\n41:\n216:\ns_add_u32 m0, s60, 49152\n"
"s_add_u32 s61, s62, s95\nbuffer_load_dwordx4 v250, s[48:51], s61 offen lds\ns_add_u32 m0, s60, 57344\ns_add_u32 s61, s62, s96\n"
"buffer_load_dwordx4 v250, s[48:51], s61 offen lds\ns_add_u32 m0, s60, 65536\ns_add_u32 s61, s62, s97\n"
"buffer_load_dwordx4 v250, s[48:51], s61 offen lds\ns_cbranch_vccnz 217f\ns_waitcnt vmcnt(6)\ns_barrier\n217:\nW 4\nM 112,176,224\nR 192,52,20480\n"
"M 112,180,88\nR 194,52,22528\nM 116,176,228\nR 196,54,20480\nM 116,180,92\nR 198,54,22528\nW 4\nM 120,184,224\nR 176,53,20480\nM 120,188,88\n"
"R 178,53,22528\nM 124,184,228\nR 180,55,20480\nM 124,188,92\nR 182,55,22528\nW 4\nM 112,192,232\nR 184,52,28672\nM 112,196,96\nR 186,52,30720\n"
"M 116,192,236\nR 188,54,28672\nM 116,196,100\nR 190,54,30720\nW 4\nM 120,176,232\nR 192,53,28672\nM 120,180,96\nR 194,53,30720\nM 124,176,236\n"
"R 196,55,28672\nM 124,180,100\nR 198,55,30720\nW 4\nM 112,184,72\nR 176,48,0\nM 112,188,104\nR 178,48,2048\nM 116,184,76\nR 180,50,0\nM 116,188,108\n"
"R 182,50,2048\nW 4\nM 120,192,72\nR 184,49,0\nM 120,196,104\nR 186,49,2048\nM 124,192,76\nR 188,51,0\nM 124,196,108\nR 190,51,2048\n"
"s_cbranch_vccz 218f\ns_waitcnt vmcnt(4)\ns_barrier\n218:\ns_add_u32 s74, s66, 3\ns_and_b32 s74, s74, 7\ns_lshl_b32 s74, s74, 7\n"
"s_add_u32 s64, s63, s74\ns_add_u32 m0, s60, 98304\ns_add_u32 s61, s64, s90\nbuffer_load_dwordx4 v250, s[48:51], s61 offen lds\n"
"v_cvt_pk_bf16_f32 v144, v112, v113\nv_cvt_pk_bf16_f32 v145, v114, v115\nv_cvt_pk_bf16_f32 v146, v120, v121\ns_add_u32 m0, s60, 106496\n"
"s_add_u32 s61, s64, s91\nbuffer_load_dwordx4 v250, s[48:51], s61 offen lds\nv_cvt_pk_bf16_f32 v147, v122, v123\nv_cvt_pk_bf16_f32 v148, v116, v117\n"
"v_cvt_pk_bf16_f32 v149, v118, v119\ns_add_u32 m0, s60, 114688\ns_add_u32 s61, s64, s92\nbuffer_load_dwordx4 v250, s[48:51], s61 offen lds\n"
"v_cvt_pk_bf16_f32 v150, v124, v125\nv_cvt_pk_bf16_f32 v151, v126, v127\ns_add_u32 s74, s66, 1\ns_and_b32 s74, s74, 7\ns_lshl_b32 s74, s74, 14\n"
"s_add_u32 s74, s74, s65\nbuffer_store_dwordx4 v[144:147], v248, s[52:55], s74 offen sc1\n"
"buffer_store_dwordx4 v[148:151], v248, s[52:55], s74 offen offset:256 sc1\ns_cbranch_vccnz 219f\ns_waitcnt vmcnt(9)\ns_barrier\n219:\nW 4\nZ 128,176,0\n"
"R 192,48,8192\nM 128,180,16\nR 194,48,10240\nZ 132,176,4\nR 196,50,8192\nM 132,180,20\nR 198,50,10240\nW 4\nZ 136,184,0\nR 176,49,8192\nM 136,188,16\n"
"R 178,49,10240\nZ 140,184,4\nR 180,51,8192\nM 140,188,20\nR 182,51,10240\nW 4\nM 128,192,8\nR 184,48,16384\nM 128,196,24\nR 186,48,18432\nM 132,192,12\n"
"R 188,50,16384\nM 132,196,28\nR 190,50,18432\nW 4\nM 136,176,8\nR 192,49,16384\nM 136,180,24\nR 194,49,18432\nM 140,176,12\nR 196,51,16384\n"
"M 140,180,28\nR 198,51,18432\nW 4\nM 128,184,200\nR 176,48,24576\nM 128,188,32\nR 178,48,26624\nM 132,184,204\nR 180,50,24576\nM 132,188,36\n"
"R 182,50,26624\nW 4\nM 136,192,200\nR 184,49,24576\nM 136,196,32\nR 186,49,26624\nM 140,192,204\nR 188,51,24576\nM 140,196,36\nR 190,51,26624\n"
"s_cbranch_vccz 220f\ns_waitcnt vmcnt(6)\ns_barrier\n220:\ns_add_u32 m0, s60, 73728\ns_add_u32 s61, s64, s93\n"
"buffer_load_dwordx4 v250, s[48:51], s61 offen lds\ns_add_u32 m0, s60, 81920\ns_add_u32 s61, s64, s98\n"
"buffer_load_dwordx4 v250, s[48:51], s61 offen lds\ns_add_u32 m0, s60, 90112\ns_add_u32 s61, s64, s94\n"
"buffer_load_dwordx4 v250, s[48:51], s61 offen lds\ns_and_b32 s44, s84, 1\ns_lshl_b32 s44, s44, 3\ns_bfe_u32 s74, s84, 0x30004\ns_or_b32 s44, s44, s74\n"
"s_and_b32 s67, s84, 6\ns_lshr_b32 s74, s84, 7\ns_add_u32 s67, s67, s74\ns_mul_i32 s68, s44, 1179648\ns_add_u32 s68, s68, 12648448\n"
"s_add_u32 s74, s66, 7\ns_and_b32 s74, s74, 7\ns_lshl_b32 s74, s74, 14\ns_add_u32 s68, s68, s74\ns_lshl_b32 s69, s44, 9\ns_add_u32 s69, s69, 65536\n"
"s_cbranch_vccnz 221f\ns_waitcnt vmcnt(9)\ns_barrier\n221:\nW 4\nM 128,176,208\nR 192,48,32768\nM 128,180,40\nR 194,48,34816\nM 132,176,212\n"
"R 196,50,32768\nM 132,180,44\nR 198,50,34816\nW 4\nM 136,184,208\nR 176,49,32768\nM 136,188,40\nR 178,49,34816\nM 140,184,212\nR 180,51,32768\n"
"M 140,188,44\nR 182,51,34816\nW 4\nM 128,192,56\nR 184,48,40960\nM 128,196,64\nR 186,48,43008\nM 132,192,60\nR 188,50,40960\nM 132,196,68\n"
"R 190,50,43008\nW 4\nM 136,176,56\nR 192,49,40960\nM 136,180,64\nR 194,49,43008\nM 140,176,60\nR 196,51,40960\nM 140,180,68\nR 198,51,43008\nW 4\n"
"M 128,184,216\nR 176,48,49152\nM 128,188,80\nR 178,48,51200\nM 132,184,220\nR 180,50,49152\nM 132,188,84\nR 182,50,51200\nW 4\nM 136,192,216\n"
"R 184,49,49152\nM 136,196,80\nR 186,49,51200\nM 140,192,220\nR 188,51,49152\nM 140,196,84\nR 190,51,51200\ns_cbranch_vccz 222f\ns_waitcnt vmcnt(3)\n"
"s_barrier\ns_sub_u32 s75, s73, 49152\ns_cmp_lg_u32 s71, 4\ns_cbranch_scc1 42f\ns_mov_b64 exec, 1\nv_mov_b32 v251, s72\n"
"buffer_store_dword v251, off, s[52:55], s75 sc1\ns_mov_b64 exec, -1\n42:\n222:\ns_add_u32 m0, s60, 0\ns_add_u32 s61, s64, s95\n"
"buffer_load_dwordx4 v250, s[48:51], s61 offen lds\ns_add_u32 m0, s60, 8192\ns_add_u32 s61, s64, s96\nbuffer_load_dwordx4 v250, s[48:51], s61 offen lds\n"
"s_add_u32 m0, s60, 16384\ns_add_u32 s61, s64, s97\nbuffer_load_dwordx4 v250, s[48:51], s61 offen lds\ns_sub_u32 s44, s67, 1\ns_and_b32 s44, s44, 7\n"
"s_lshl_b32 s70, s44, 6\ns_add_u32 s70, s70, s69\ns_add_u32 s70, s70, 0\nbuffer_load_dword v254, off, s[52:55], s70 sc1\ns_cbranch_vccnz 223f\n"
"s_waitcnt vmcnt(7)\ns_barrier\n223:\nW 4\nM 128,176,224\nR 192,48,57344\nM 128,180,88\nR 194,48,59392\nM 132,176,228\nR 196,50,57344\nM 132,180,92\n"
"R 198,50,59392\nW 4\nM 136,184,224\nR 176,49,57344\nM 136,188,88\nR 178,49,59392\nM 140,184,228\nR 180,51,57344\nM 140,188,92\nR 182,51,59392\nW 4\n"
"M 128,192,232\nR 184,52,4096\nM 128,196,96\nR 186,52,6144\nM 132,192,236\nR 188,54,4096\nM 132,196,100\nR 190,54,6144\nW 4\nM 136,176,232\n"
"R 192,53,4096\nM 136,180,96\nR 194,53,6144\nM 140,176,236\nR 196,55,4096\nM 140,180,100\nR 198,55,6144\nW 4\nM 128,184,72\nR 176,52,36864\n"
"M 128,188,104\nR 178,52,38912\nM 132,184,76\nR 180,54,36864\nM 132,188,108\nR 182,54,38912\nW 4\nM 136,192,72\nR 184,53,36864\nM 136,196,104\n"
"R 186,53,38912\nM 140,192,76\nR 188,55,36864\nM 140,196,108\nR 190,55,38912\ns_cbranch_vccz 224f\ns_waitcnt vmcnt(5)\ns_barrier\n224:\n"
"s_add_u32 s74, s66, 4\ns_and_b32 s74, s74, 7\ns_lshl_b32 s74, s74, 7\ns_add_u32 s62, s63, s74\ns_add_u32 m0, s60, 24576\ns_add_u32 s61, s62, s90\n"
"buffer_load_dwordx4 v250, s[48:51], s61 offen lds\nv_cvt_pk_bf16_f32 v144, v128, v129\nv_cvt_pk_bf16_f32 v145, v130, v131\n"
"v_cvt_pk_bf16_f32 v146, v136, v137\ns_add_u32 m0, s60, 32768\ns_add_u32 s61, s62, s91\nbuffer_load_dwordx4 v250, s[48:51], s61 offen lds\n"
"v_cvt_pk_bf16_f32 v147, v138, v139\nv_cvt_pk_bf16_f32 v148, v132, v133\nv_cvt_pk_bf16_f32 v149, v134, v135\ns_add_u32 m0, s60, 40960\n"
"s_add_u32 s61, s62, s92\nbuffer_load_dwordx4 v250, s[48:51], s61 offen lds\nv_cvt_pk_bf16_f32 v150, v140, v141\nv_cvt_pk_bf16_f32 v151, v142, v143\n"
"s_add_u32 s74, s66, 2\ns_and_b32 s74, s74, 7\ns_lshl_b32 s74, s74, 14\ns_add_u32 s74, s74, s65\n"
"buffer_store_dwordx4 v[144:147], v248, s[52:55], s74 offen sc1\nbuffer_store_dwordx4 v[148:151], v248, s[52:55], s74 offen offset:256 sc1\n"
"s_cbranch_vccnz 225f\ns_waitcnt vmcnt(10)\ns_barrier\n225:\nW 4\nZ 112,176,0\nR 192,52,45056\nM 112,180,16\nR 194,52,47104\nZ 116,176,4\n"
"R 196,54,45056\nM 116,180,20\nR 198,54,47104\nW 4\nZ 120,184,0\nR 176,53,45056\nM 120,188,16\nR 178,53,47104\nZ 124,184,4\nR 180,55,45056\n"
"M 124,188,20\nR 182,55,47104\nW 4\nM 112,192,8\nR 184,52,53248\nM 112,196,24\nR 186,52,55296\nM 116,192,12\nR 188,54,53248\nM 116,196,28\n"
"R 190,54,55296\nW 4\nM 120,176,8\nR 192,53,53248\nM 120,180,24\nR 194,53,55296\nM 124,176,12\nR 196,55,53248\nM 124,180,28\nR 198,55,55296\nW 4\n"
"M 112,184,200\nR 176,52,12288\nM 112,188,32\nR 178,52,14336\nM 116,184,204\nR 180,54,12288\nM 116,188,36\nR 182,54,14336\nW 4\nM 120,192,200\n"
"R 184,53,12288\nM 120,196,32\nR 186,53,14336\nM 124,192,204\nR 188,55,12288\nM 124,196,36\nR 190,55,14336\ns_cbranch_vccz 226f\ns_waitcnt vmcnt(7)\n"
"s_barrier\n226:\ns_add_u32 m0, s60, 49152\ns_add_u32 s61, s62, s93\nbuffer_load_dwordx4 v250, s[48:51], s61 offen lds\ns_add_u32 m0, s60, 57344\n"
"s_add_u32 s61, s62, s98\nbuffer_load_dwordx4 v250, s[48:51], s61 offen lds\ns_add_u32 m0, s60, 65536\ns_add_u32 s61, s62, s94\n"
"buffer_load_dwordx4 v250, s[48:51], s61 offen lds\ns_waitcnt vmcnt(8)\nv_readfirstlane_b32 s74, v254\ns_mul_i32 s44, s44, 147456\n"
"s_add_u32 s44, s44, s68\ns_cmp_eq_u32 s74, s72\ns_cbranch_scc1 51f\ns_mov_b32 s75, 0x40000\n31:\ns_sleep 1\n"
"buffer_load_dword v254, off, s[52:55], s70 sc1\ns_waitcnt vmcnt(0)\nv_readfirstlane_b32 s74, v254\ns_sub_u32 s75, s75, 1\ns_cmp_eq_u32 s75, 0\n"
"s_cbranch_scc1 51f\ns_cmp_lg_u32 s74, s72\ns_cbranch_scc1 31b\n51:\nbuffer_load_dwordx4 v[168:171], v248, s[52:55], s44 offen sc1\n"
"buffer_load_dwordx4 v[172:175], v248, s[52:55], s44 offen offset:256 sc1\ns_cbranch_vccnz 227f\ns_waitcnt vmcnt(12)\ns_barrier\n227:\nW 4\n"
"M 112,176,208\nR 192,52,20480\nM 112,180,40\nR 194,52,22528\nM 116,176,212\nR 196,54,20480\nM 116,180,44\nR 198,54,22528\nW 4\nM 120,184,208\n"
"R 176,53,20480\nM 120,188,40\nR 178,53,22528\nM 124,184,212\nR 180,55,20480\nM 124,188,44\nR 182,55,22528\nW 4\nM 112,192,56\nR 184,52,28672\n"
"M 112,196,64\nR 186,52,30720\nM 116,192,60\nR 188,54,28672\nM 116,196,68\nR 190,54,30720\nW 4\nM 120,176,56\nR 192,53,28672\nM 120,180,64\n"
"R 194,53,30720\nM 124,176,60\nR 196,55,28672\nM 124,180,68\nR 198,55,30720\nW 4\nM 112,184,216\nR 176,48,0\nM 112,188,80\nR 178,48,2048\nM 116,184,220\n"
"R 180,50,0\nM 116,188,84\nR 182,50,2048\nW 4\nM 120,192,216\nR 184,49,0\nM 120,196,80\nR 186,49,2048\nM 124,192,220\nR 188,51,0\nM 124,196,84\n"
"R 190,51,2048\ns_cbranch_vccz 228f\ns_waitcnt vmcnt(5)\ns_barrier\ns_sub_u32 s75, s73, 40960\ns_cmp_lg_u32 s71, 4\ns_cbranch_scc1 43f\n"
"s_mov_b64 exec, 1\nv_mov_b32 v251, s72\nbuffer_store_dword v251, off, s[52:55], s75 sc1\ns_mov_b64 exec, -1\n43:\n228:\ns_add_u32 m0, s60, 98304\n"
"s_add_u32 s61, s62, s95\nbuffer_load_dwordx4 v250, s[48:51], s61 offen lds\ns_add_u32 m0, s60, 106496\ns_add_u32 s61, s62, s96\n"
"buffer_load_dwordx4 v250, s[48:51], s61 offen lds\ns_add_u32 m0, s60, 114688\ns_add_u32 s61, s62, s97\n"
"buffer_load_dwordx4 v250, s[48:51], s61 offen lds\ns_sub_u32 s44, s67, 2\ns_and_b32 s44, s44, 7\ns_lshl_b32 s70, s44, 6\ns_add_u32 s70, s70, s69\n"
"s_add_u32 s70, s70, 8192\nbuffer_load_dword v254, off, s[52:55], s70 sc1\ns_cbranch_vccnz 229f\ns_waitcnt vmcnt(9)\ns_barrier\n229:\nW 4\n"
"M 112,176,224\nR 192,48,8192\nM 112,180,88\nR 194,48,10240\nM 116,176,228\nR 196,50,8192\nM 116,180,92\nR 198,50,10240\nW 4\nM 120,184,224\n"
"R 176,49,8192\nM 120,188,88\nR 178,49,10240\nM 124,184,228\nR 180,51,8192\nM 124,188,92\nR 182,51,10240\nW 4\nM 112,192,232\nR 184,48,16384\n"
"M 112,196,96\nR 186,48,18432\nM 116,192,236\nR 188,50,16384\nM 116,196,100\nR 190,50,18432\nW 4\nM 120,176,232\nR 192,49,16384\nM 120,180,96\n"
"R 194,49,18432\nM 124,176,236\nR 196,51,16384\nM 124,180,100\nR 198,51,18432\nW 4\nM 112,184,72\nR 176,48,24576\nM 112,188,104\nR 178,48,26624\n"
"M 116,184,76\nR 180,50,24576\nM 116,188,108\nR 182,50,26624\nW 4\nM 120,192,72\nR 184,49,24576\nM 120,196,104\nR 186,49,26624\nM 124,192,76\n"
"R 188,51,24576\nM 124,196,108\nR 190,51,26624\ns_cbranch_vccz 230f\ns_waitcnt vmcnt(7)\ns_barrier\n230:\ns_add_u32 s74, s66, 5\ns_and_b32 s74, s74, 7\n"
"s_lshl_b32 s74, s74, 7\ns_add_u32 s64, s63, s74\ns_add_u32 m0, s60, 73728\ns_add_u32 s61, s64, s90\nbuffer_load_dwordx4 v250, s[48:51], s61 offen lds\n"
"v_cvt_pk_bf16_f32 v144, v112, v113\nv_cvt_pk_bf16_f32 v145, v114, v115\nv_cvt_pk_bf16_f32 v146, v120, v121\ns_add_u32 m0, s60, 81920\n"
"s_add_u32 s61, s64, s91\nbuffer_load_dwordx4 v250, s[48:51], s61 offen lds\nv_cvt_pk_bf16_f32 v147, v122, v123\nv_cvt_pk_bf16_f32 v148, v116, v117\n"
"v_cvt_pk_bf16_f32 v149, v118, v119\ns_add_u32 m0, s60, 90112\ns_add_u32 s61, s64, s92\nbuffer_load_dwordx4 v250, s[48:51], s61 offen lds\n"
"v_cvt_pk_bf16_f32 v150, v124, v125\nv_cvt_pk_bf16_f32 v151, v126, v127\ns_add_u32 s74, s66, 3\ns_and_b32 s74, s74, 7\ns_lshl_b32 s74, s74, 14\n"
"s_add_u32 s74, s74, s65\nbuffer_store_dwordx4 v[144:147], v248, s[52:55], s74 offen sc1\n"
"buffer_store_dwordx4 v[148:151], v248, s[52:55], s74 offen offset:256 sc1\ns_cbranch_vccnz 231f\ns_waitcnt vmcnt(12)\ns_barrier\n231:\nW 4\n"
"Z 128,176,0\nR 192,48,32768\nM 128,180,16\nR 194,48,34816\nZ 132,176,4\nR 196,50,32768\nM 132,180,20\nR 198,50,34816\nW 4\nZ 136,184,0\nR 176,49,32768\n"
"M 136,188,16\nR 178,49,34816\nZ 140,184,4\nR 180,51,32768\nM 140,188,20\nR 182,51,34816\nW 4\nM 128,192,8\nR 184,48,40960\nM 128,196,24\n"
"R 186,48,43008\nM 132,192,12\nR 188,50,40960\nM 132,196,28\nR 190,50,43008\nW 4\nM 136,176,8\nR 192,49,40960\nM 136,180,24\nR 194,49,43008\n"
"M 140,176,12\nR 196,51,40960\nM 140,180,28\nR 198,51,43008\nW 4\nM 128,184,200\nR 176,48,49152\nM 128,188,32\nR 178,48,51200\nM 132,184,204\n"
"R 180,50,49152\nM 132,188,36\nR 182,50,51200\nW 4\nM 136,192,200\nR 184,49,49152\nM 136,196,32\nR 186,49,51200\nM 140,192,204\nR 188,51,49152\n"
"M 140,196,36\nR 190,51,51200\ns_cbranch_vccz 232f\ns_waitcnt vmcnt(7)\ns_barrier\n232:\ns_add_u32 m0, s60, 0\ns_add_u32 s61, s64, s93\n"
"buffer_load_dwordx4 v250, s[48:51], s61 offen lds\ns_waitcnt vmcnt(10)\nv_lshlrev_b32 v152, 16, v168\nv_and_b32 v153, 0xffff0000, v168\n"
"v_lshlrev_b32 v154, 16, v169\nv_and_b32 v155, 0xffff0000, v169\nv_lshlrev_b32 v160, 16, v170\ns_add_u32 m0, s60, 8192\ns_add_u32 s61, s64, s98\n"
"buffer_load_dwordx4 v250, s[48:51], s61 offen lds\nv_and_b32 v161, 0xffff0000, v170\nv_lshlrev_b32 v162, 16, v171\nv_and_b32 v163, 0xffff0000, v171\n"
"v_lshlrev_b32 v156, 16, v172\nv_and_b32 v157, 0xffff0000, v172\nv_lshlrev_b32 v158, 16, v173\nv_and_b32 v159, 0xffff0000, v173\n"
"s_add_u32 m0, s60, 16384\ns_add_u32 s61, s64, s94\nbuffer_load_dwordx4 v250, s[48:51], s61 offen lds\nv_lshlrev_b32 v164, 16, v174\n"
"v_and_b32 v165, 0xffff0000, v174\nv_lshlrev_b32 v166, 16, v175\nv_and_b32 v167, 0xffff0000, v175\ns_waitcnt vmcnt(8)\nv_readfirstlane_b32 s74, v254\n"
"s_mul_i32 s44, s44, 147456\ns_add_u32 s44, s44, s68\ns_cmp_eq_u32 s74, s72\ns_cbranch_scc1 52f\ns_mov_b32 s75, 0x40000\n32:\ns_sleep 1\n"
"buffer_load_dword v254, off, s[52:55], s70 sc1\ns_waitcnt vmcnt(0)\nv_readfirstlane_b32 s74, v254\ns_sub_u32 s75, s75, 1\ns_cmp_eq_u32 s75, 0\n"
"s_cbranch_scc1 52f\ns_cmp_lg_u32 s74, s72\ns_cbranch_scc1 32b\n52:\nbuffer_load_dwordx4 v[168:171], v248, s[52:55], s44 offen sc1\n"
"buffer_load_dwordx4 v[172:175], v248, s[52:55], s44 offen offset:256 sc1\ns_cbranch_vccnz 233f\ns_waitcnt vmcnt(12)\ns_barrier\n233:\nW 4\n"
"M 128,176,208\nR 192,48,57344\nM 128,180,40\nR 194,48,59392\nM 132,176,212\nR 196,50,57344\nM 132,180,44\nR 198,50,59392\nW 4\nM 136,184,208\n"
"R 176,49,57344\nM 136,188,40\nR 178,49,59392\nM 140,184,212\nR 180,51,57344\nM 140,188,44\nR 182,51,59392\nW 4\nM 128,192,56\nR 184,52,4096\n"
"M 128,196,64\nR 186,52,6144\nM 132,192,60\nR 188,54,4096\nM 132,196,68\nR 190,54,6144\nW 4\nM 136,176,56\nR 192,53,4096\nM 136,180,64\nR 194,53,6144\n"
"M 140,176,60\nR 196,55,4096\nM 140,180,68\nR 198,55,6144\nW 4\nM 128,184,216\nR 176,52,36864\nM 128,188,80\nR 178,52,38912\nM 132,184,220\n"
"R 180,54,36864\nM 132,188,84\nR 182,54,38912\nW 4\nM 136,192,216\nR 184,53,36864\nM 136,196,80\nR 186,53,38912\nM 140,192,220\nR 188,55,36864\n"
"M 140,196,84\nR 190,55,38912\ns_cbranch_vccz 234f\ns_waitcnt vmcnt(5)\ns_barrier\ns_sub_u32 s75, s73, 32768\ns_cmp_lg_u32 s71, 4\ns_cbranch_scc1 44f\n"
"s_mov_b64 exec, 1\nv_mov_b32 v251, s72\nbuffer_store_dword v251, off, s[52:55], s75 sc1\ns_mov_b64 exec, -1\n44:\n234:\ns_add_u32 m0, s60, 24576\n"
"s_add_u32 s61, s64, s95\nbuffer_load_dwordx4 v250, s[48:51], s61 offen lds\ns_add_u32 m0, s60, 32768\ns_add_u32 s61, s64, s96\n"
"buffer_load_dwordx4 v250, s[48:51], s61 offen lds\ns_add_u32 m0, s60, 40960\ns_add_u32 s61, s64, s97\n"
"buffer_load_dwordx4 v250, s[48:51], s61 offen lds\ns_sub_u32 s44, s67, 3\ns_and_b32 s44, s44, 7\ns_lshl_b32 s70, s44, 6\ns_add_u32 s70, s70, s69\n"
"s_add_u32 s70, s70, 16384\nbuffer_load_dword v254, off, s[52:55], s70 sc1\ns_cbranch_vccnz 235f\ns_waitcnt vmcnt(9)\ns_barrier\n235:\nW 4\n"
"M 128,176,224\nR 192,52,45056\nM 128,180,88\nR 194,52,47104\nM 132,176,228\nR 196,54,45056\nM 132,180,92\nR 198,54,47104\nW 4\nM 136,184,224\n"
"R 176,53,45056\nM 136,188,88\nR 178,53,47104\nM 140,184,228\nR 180,55,45056\nM 140,188,92\nR 182,55,47104\nW 4\nM 128,192,232\nR 184,52,53248\n"
"M 128,196,96\nR 186,52,55296\nM 132,192,236\nR 188,54,53248\nM 132,196,100\nR 190,54,55296\nW 4\nM 136,176,232\nR 192,53,53248\nM 136,180,96\n"
"R 194,53,55296\nM 140,176,236\nR 196,55,53248\nM 140,180,100\nR 198,55,55296\nW 4\nM 128,184,72\nR 176,52,12288\nM 128,188,104\nR 178,52,14336\n"
"M 132,184,76\nR 180,54,12288\nM 132,188,108\nR 182,54,14336\nW 4\nM 136,192,72\nR 184,53,12288\nM 136,196,104\nR 186,53,14336\nM 140,192,76\n"
"R 188,55,12288\nM 140,196,108\nR 190,55,14336\ns_cbranch_vccz 236f\ns_waitcnt vmcnt(7)\ns_barrier\n236:\ns_add_u32 s74, s66, 6\ns_and_b32 s74, s74, 7\n"
"s_lshl_b32 s74, s74, 7\ns_add_u32 s62, s63, s74\ns_add_u32 m0, s60, 49152\ns_add_u32 s61, s62, s90\nbuffer_load_dwordx4 v250, s[48:51], s61 offen lds\n"
"v_cvt_pk_bf16_f32 v144, v128, v129\nv_cvt_pk_bf16_f32 v145, v130, v131\nv_cvt_pk_bf16_f32 v146, v136, v137\ns_add_u32 m0, s60, 57344\n"
"s_add_u32 s61, s62, s91\nbuffer_load_dwordx4 v250, s[48:51], s61 offen lds\nv_cvt_pk_bf16_f32 v147, v138, v139\nv_cvt_pk_bf16_f32 v148, v132, v133\n"
"v_cvt_pk_bf16_f32 v149, v134, v135\ns_add_u32 m0, s60, 65536\ns_add_u32 s61, s62, s92\nbuffer_load_dwordx4 v250, s[48:51], s61 offen lds\n"
"v_cvt_pk_bf16_f32 v150, v140, v141\nv_cvt_pk_bf16_f32 v151, v142, v143\ns_add_u32 s74, s66, 4\ns_and_b32 s74, s74, 7\ns_lshl_b32 s74, s74, 14\n"
"s_add_u32 s74, s74, s65\nbuffer_store_dwordx4 v[144:147], v248, s[52:55], s74 offen sc1\n"
"buffer_store_dwordx4 v[148:151], v248, s[52:55], s74 offen offset:256 sc1\ns_cbranch_vccnz 237f\ns_waitcnt vmcnt(12)\ns_barrier\n237:\nW 4\n"
"Z 112,176,0\nR 192,52,20480\nM 112,180,16\nR 194,52,22528\nZ 116,176,4\nR 196,54,20480\nM 116,180,20\nR 198,54,22528\nW 4\nZ 120,184,0\nR 176,53,20480\n"
"M 120,188,16\nR 178,53,22528\nZ 124,184,4\nR 180,55,20480\nM 124,188,20\nR 182,55,22528\nW 4\nM 112,192,8\nR 184,52,28672\nM 112,196,24\n"
"R 186,52,30720\nM 116,192,12\nR 188,54,28672\nM 116,196,28\nR 190,54,30720\nW 4\nM 120,176,8\nR 192,53,28672\nM 120,180,24\nR 194,53,30720\n"
"M 124,176,12\nR 196,55,28672\nM 124,180,28\nR 198,55,30720\nW 4\nM 112,184,200\nR 176,48,0\nM 112,188,32\nR 178,48,2048\nM 116,184,204\nR 180,50,0\n"
"M 116,188,36\nR 182,50,2048\nW 4\nM 120,192,200\nR 184,49,0\nM 120,196,32\nR 186,49,2048\nM 124,192,204\nR 188,51,0\nM 124,196,36\nR 190,51,2048\n"
"s_cbranch_vccz 238f\ns_waitcnt vmcnt(7)\ns_barrier\n238:\ns_add_u32 m0, s60, 98304\ns_add_u32 s61, s62, s93\n"
"buffer_load_dwordx4 v250, s[48:51], s61 offen lds\ns_waitcnt vmcnt(10)\nv_lshlrev_b32 v240, 16, v168\nv_and_b32 v144, 0xffff0000, v168\n"
"v_lshlrev_b32 v241, 16, v169\nv_and_b32 v145, 0xffff0000, v169\nv_lshlrev_b32 v242, 16, v170\ns_add_u32 m0, s60, 106496\ns_add_u32 s61, s62, s98\n"
"buffer_load_dwordx4 v250, s[48:51], s61 offen lds\nv_and_b32 v146, 0xffff0000, v170\nv_lshlrev_b32 v243, 16, v171\nv_and_b32 v147, 0xffff0000, v171\n"
"v_lshlrev_b32 v244, 16, v172\nv_and_b32 v148, 0xffff0000, v172\nv_lshlrev_b32 v245, 16, v173\nv_and_b32 v149, 0xffff0000, v173\n"
"s_add_u32 m0, s60, 114688\ns_add_u32 s61, s62, s94\nbuffer_load_dwordx4 v250, s[48:51], s61 offen lds\nv_lshlrev_b32 v246, 16, v174\n"
"v_and_b32 v150, 0xffff0000, v174\nv_lshlrev_b32 v247, 16, v175\nv_and_b32 v151, 0xffff0000, v175\ns_waitcnt vmcnt(8)\nv_readfirstlane_b32 s74, v254\n"
"s_mul_i32 s44, s44, 147456\ns_add_u32 s44, s44, s68\ns_cmp_eq_u32 s74, s72\ns_cbranch_scc1 53f\ns_mov_b32 s75, 0x40000\n33:\ns_sleep 1\n"
"buffer_load_dword v254, off, s[52:55], s70 sc1\ns_waitcnt vmcnt(0)\nv_readfirstlane_b32 s74, v254\ns_sub_u32 s75, s75, 1\ns_cmp_eq_u32 s75, 0\n"
"s_cbranch_scc1 53f\ns_cmp_lg_u32 s74, s72\ns_cbranch_scc1 33b\n53:\nbuffer_load_dwordx4 v[168:171], v248, s[52:55], s44 offen sc1\n"
"buffer_load_dwordx4 v[172:175], v248, s[52:55], s44 offen offset:256 sc1\ns_cbranch_vccnz 239f\ns_waitcnt vmcnt(12)\ns_barrier\n239:\nW 4\n"
"M 112,176,208\nR 192,48,8192\nM 112,180,40\nR 194,48,10240\nM 116,176,212\nR 196,50,8192\nM 116,180,44\nR 198,50,10240\nW 4\nM 120,184,208\n"
"R 176,49,8192\nM 120,188,40\nR 178,49,10240\nM 124,184,212\nR 180,51,8192\nM 124,188,44\nR 182,51,10240\nW 4\nM 112,192,56\nR 184,48,16384\n"
"M 112,196,64\nR 186,48,18432\nM 116,192,60\nR 188,50,16384\nM 116,196,68\nR 190,50,18432\nW 4\nM 120,176,56\nR 192,49,16384\nM 120,180,64\n"
"R 194,49,18432\nM 124,176,60\nR 196,51,16384\nM 124,180,68\nR 198,51,18432\nW 4\nM 112,184,216\nR 176,48,24576\nM 112,188,80\nR 178,48,26624\n"
"M 116,184,220\nR 180,50,24576\nM 116,188,84\nR 182,50,26624\nW 4\nM 120,192,216\nR 184,49,24576\nM 120,196,80\nR 186,49,26624\nM 124,192,220\n"
"R 188,51,24576\nM 124,196,84\nR 190,51,26624\ns_cbranch_vccz 240f\ns_waitcnt vmcnt(5)\ns_barrier\ns_sub_u32 s75, s73, 24576\ns_cmp_lg_u32 s71, 4\n"
"s_cbranch_scc1 45f\ns_mov_b64 exec, 1\nv_mov_b32 v251, s72\nbuffer_store_dword v251, off, s[52:55], s75 sc1\ns_mov_b64 exec, -1\n45:\n240:\n"
"s_add_u32 m0, s60, 73728\ns_add_u32 s61, s62, s95\nbuffer_load_dwordx4 v250, s[48:51], s61 offen lds\nv_add_f32 v152, v152, v240\n"
"v_add_f32 v153, v153, v144\nv_add_f32 v154, v154, v241\nv_add_f32 v155, v155, v145\nv_add_f32 v160, v160, v242\nv_add_f32 v161, v161, v146\n"
"s_add_u32 m0, s60, 81920\ns_add_u32 s61, s62, s96\nbuffer_load_dwordx4 v250, s[48:51], s61 offen lds\nv_add_f32 v162, v162, v243\n"
"v_add_f32 v163, v163, v147\nv_add_f32 v156, v156, v244\nv_add_f32 v157, v157, v148\nv_add_f32 v158, v158, v245\nv_add_f32 v159, v159, v149\n"
"s_add_u32 m0, s60, 90112\ns_add_u32 s61, s62, s97\nbuffer_load_dwordx4 v250, s[48:51], s61 offen lds\nv_add_f32 v164, v164, v246\n"
"v_add_f32 v165, v165, v150\nv_add_f32 v166, v166, v247\nv_add_f32 v167, v167, v151\ns_sub_u32 s44, s67, 4\ns_and_b32 s44, s44, 7\n"
"s_lshl_b32 s70, s44, 6\ns_add_u32 s70, s70, s69\ns_add_u32 s70, s70, 24576\nbuffer_load_dword v254, off, s[52:55], s70 sc1\ns_cbranch_vccnz 241f\n"
"s_waitcnt vmcnt(9)\ns_barrier\n241:\nW 4\nM 112,176,224\nR 192,48,32768\nM 112,180,88\nR 194,48,34816\nM 116,176,228\nR 196,50,32768\nM 116,180,92\n"
"R 198,50,34816\nW 4\nM 120,184,224\nR 176,49,32768\nM 120,188,88\nR 178,49,34816\nM 124,184,228\nR 180,51,32768\nM 124,188,92\nR 182,51,34816\nW 4\n"
"M 112,192,232\nR 184,48,40960\nM 112,196,96\nR 186,48,43008\nM 116,192,236\nR 188,50,40960\nM 116,196,100\nR 190,50,43008\nW 4\nM 120,176,232\n"
"R 192,49,40960\nM 120,180,96\nR 194,49,43008\nM 124,176,236\nR 196,51,40960\nM 124,180,100\nR 198,51,43008\nW 4\nM 112,184,72\nR 176,48,49152\n"
"M 112,188,104\nR 178,48,51200\nM 116,184,76\nR 180,50,49152\nM 116,188,108\nR 182,50,51200\nW 4\nM 120,192,72\nR 184,49,49152\nM 120,196,104\n"
"R 186,49,51200\nM 124,192,76\nR 188,51,49152\nM 124,196,108\nR 190,51,51200\ns_cbranch_vccz 242f\ns_waitcnt vmcnt(7)\ns_barrier\n242:\n"
"s_add_u32 s74, s66, 7\ns_and_b32 s74, s74, 7\ns_lshl_b32 s74, s74, 7\ns_add_u32 s64, s63, s74\ns_add_u32 m0, s60, 0\ns_add_u32 s61, s64, s90\n"
"buffer_load_dwordx4 v250, s[48:51], s61 offen lds\nv_cvt_pk_bf16_f32 v144, v112, v113\nv_cvt_pk_bf16_f32 v145, v114, v115\n"
"v_cvt_pk_bf16_f32 v146, v120, v121\ns_add_u32 m0, s60, 8192\ns_add_u32 s61, s64, s91\nbuffer_load_dwordx4 v250, s[48:51], s61 offen lds\n"
"v_cvt_pk_bf16_f32 v147, v122, v123\nv_cvt_pk_bf16_f32 v148, v116, v117\nv_cvt_pk_bf16_f32 v149, v118, v119\ns_add_u32 m0, s60, 16384\n"
"s_add_u32 s61, s64, s92\nbuffer_load_dwordx4 v250, s[48:51], s61 offen lds\nv_cvt_pk_bf16_f32 v150, v124, v125\nv_cvt_pk_bf16_f32 v151, v126, v127\n"
"s_add_u32 s74, s66, 5\ns_and_b32 s74, s74, 7\ns_lshl_b32 s74, s74, 14\ns_add_u32 s74, s74, s65\n"
"buffer_store_dwordx4 v[144:147], v248, s[52:55], s74 offen sc1\nbuffer_store_dwordx4 v[148:151], v248, s[52:55], s74 offen offset:256 sc1\n"
"s_cbranch_vccnz 243f\ns_waitcnt vmcnt(12)\ns_barrier\n243:\nW 4\nZ 128,176,0\nR 192,48,57344\nM 128,180,16\nR 194,48,59392\nZ 132,176,4\n"
"R 196,50,57344\nM 132,180,20\nR 198,50,59392\nW 4\nZ 136,184,0\nR 176,49,57344\nM 136,188,16\nR 178,49,59392\nZ 140,184,4\nR 180,51,57344\n"
"M 140,188,20\nR 182,51,59392\nW 4\nM 128,192,8\nR 184,52,4096\nM 128,196,24\nR 186,52,6144\nM 132,192,12\nR 188,54,4096\nM 132,196,28\nR 190,54,6144\n"
"W 4\nM 136,176,8\nR 192,53,4096\nM 136,180,24\nR 194,53,6144\nM 140,176,12\nR 196,55,4096\nM 140,180,28\nR 198,55,6144\nW 4\nM 128,184,200\n"
"R 176,52,36864\nM 128,188,32\nR 178,52,38912\nM 132,184,204\nR 180,54,36864\nM 132,188,36\nR 182,54,38912\nW 4\nM 136,192,200\nR 184,53,36864\n"
"M 136,196,32\nR 186,53,38912\nM 140,192,204\nR 188,55,36864\nM 140,196,36\nR 190,55,38912\ns_cbranch_vccz 244f\ns_waitcnt vmcnt(7)\ns_barrier\n244:\n"
"s_add_u32 m0, s60, 24576\ns_add_u32 s61, s64, s93\nbuffer_load_dwordx4 v250, s[48:51], s61 offen lds\ns_waitcnt vmcnt(10)\n"
"v_lshlrev_b32 v240, 16, v168\nv_and_b32 v144, 0xffff0000, v168\nv_lshlrev_b32 v241, 16, v169\nv_and_b32 v145, 0xffff0000, v169\n"
"v_lshlrev_b32 v242, 16, v170\ns_add_u32 m0, s60, 32768\ns_add_u32 s61, s64, s98\nbuffer_load_dwordx4 v250, s[48:51], s61 offen lds\n"
"v_and_b32 v146, 0xffff0000, v170\nv_lshlrev_b32 v243, 16, v171\nv_and_b32 v147, 0xffff0000, v171\nv_lshlrev_b32 v244, 16, v172\n"
"v_and_b32 v148, 0xffff0000, v172\nv_lshlrev_b32 v245, 16, v173\nv_and_b32 v149, 0xffff0000, v173\ns_add_u32 m0, s60, 40960\ns_add_u32 s61, s64, s94\n"
"buffer_load_dwordx4 v250, s[48:51], s61 offen lds\nv_lshlrev_b32 v246, 16, v174\nv_and_b32 v150, 0xffff0000, v174\nv_lshlrev_b32 v247, 16, v175\n"
"v_and_b32 v151, 0xffff0000, v175\ns_waitcnt vmcnt(8)\nv_readfirstlane_b32 s74, v254\ns_mul_i32 s44, s44, 147456\ns_add_u32 s44, s44, s68\n"
"s_cmp_eq_u32 s74, s72\ns_cbranch_scc1 54f\ns_mov_b32 s75, 0x40000\n34:\ns_sleep 1\nbuffer_load_dword v254, off, s[52:55], s70 sc1\ns_waitcnt vmcnt(0)\n"
"v_readfirstlane_b32 s74, v254\ns_sub_u32 s75, s75, 1\ns_cmp_eq_u32 s75, 0\ns_cbranch_scc1 54f\ns_cmp_lg_u32 s74, s72\ns_cbranch_scc1 34b\n54:\n"
"buffer_load_dwordx4 v[168:171], v248, s[52:55], s44 offen sc1\nbuffer_load_dwordx4 v[172:175], v248, s[52:55], s44 offen offset:256 sc1\n"
"s_cbranch_vccnz 245f\ns_waitcnt vmcnt(12)\ns_barrier\n245:\nW 4\nM 128,176,208\nR 192,52,45056\nM 128,180,40\nR 194,52,47104\nM 132,176,212\n"
"R 196,54,45056\nM 132,180,44\nR 198,54,47104\nW 4\nM 136,184,208\nR 176,53,45056\nM 136,188,40\nR 178,53,47104\nM 140,184,212\nR 180,55,45056\n"
"M 140,188,44\nR 182,55,47104\nW 4\nM 128,192,56\nR 184,52,53248\nM 128,196,64\nR 186,52,55296\nM 132,192,60\nR 188,54,53248\nM 132,196,68\n"
"R 190,54,55296\nW 4\nM 136,176,56\nR 192,53,53248\nM 136,180,64\nR 194,53,55296\nM 140,176,60\nR 196,55,53248\nM 140,180,68\nR 198,55,55296\nW 4\n"
"M 128,184,216\nR 176,52,12288\nM 128,188,80\nR 178,52,14336\nM 132,184,220\nR 180,54,12288\nM 132,188,84\nR 182,54,14336\nW 4\nM 136,192,216\n"
"R 184,53,12288\nM 136,196,80\nR 186,53,14336\nM 140,192,220\nR 188,55,12288\nM 140,196,84\nR 190,55,14336\ns_cbranch_vccz 246f\ns_waitcnt vmcnt(5)\n"
"s_barrier\ns_sub_u32 s75, s73, 16384\ns_cmp_lg_u32 s71, 4\ns_cbranch_scc1 46f\ns_mov_b64 exec, 1\nv_mov_b32 v251, s72\n"
"buffer_store_dword v251, off, s[52:55], s75 sc1\ns_mov_b64 exec, -1\n46:\n246:\ns_add_u32 m0, s60, 49152\ns_add_u32 s61, s64, s95\n"
"buffer_load_dwordx4 v250, s[48:51], s61 offen lds\nv_add_f32 v152, v152, v240\nv_add_f32 v153, v153, v144\nv_add_f32 v154, v154, v241\n"
"v_add_f32 v155, v155, v145\nv_add_f32 v160, v160, v242\nv_add_f32 v161, v161, v146\ns_add_u32 m0, s60, 57344\ns_add_u32 s61, s64, s96\n"
"buffer_load_dwordx4 v250, s[48:51], s61 offen lds\nv_add_f32 v162, v162, v243\nv_add_f32 v163, v163, v147\nv_add_f32 v156, v156, v244\n"
"v_add_f32 v157, v157, v148\nv_add_f32 v158, v158, v245\nv_add_f32 v159, v159, v149\ns_add_u32 m0, s60, 65536\ns_add_u32 s61, s64, s97\n"
"buffer_load_dwordx4 v250, s[48:51], s61 offen lds\nv_add_f32 v164, v164, v246\nv_add_f32 v165, v165, v150\nv_add_f32 v166, v166, v247\n"
"v_add_f32 v167, v167, v151\ns_sub_u32 s44, s67, 5\ns_and_b32 s44, s44, 7\ns_lshl_b32 s70, s44, 6\ns_add_u32 s70, s70, s69\ns_add_u32 s70, s70, 32768\n"
"buffer_load_dword v254, off, s[52:55], s70 sc1\ns_cbranch_vccnz 247f\ns_waitcnt vmcnt(9)\ns_barrier\n247:\nW 4\nM 128,176,224\nR 192,52,20480\n"
"M 128,180,88\nR 194,52,22528\nM 132,176,228\nR 196,54,20480\nM 132,180,92\nR 198,54,22528\nW 4\nM 136,184,224\nR 176,53,20480\nM 136,188,88\n"
"R 178,53,22528\nM 140,184,228\nR 180,55,20480\nM 140,188,92\nR 182,55,22528\nW 4\nM 128,192,232\nR 184,52,28672\nM 128,196,96\nR 186,52,30720\n"
"M 132,192,236\nR 188,54,28672\nM 132,196,100\nR 190,54,30720\nW 4\nM 136,176,232\nR 192,53,28672\nM 136,180,96\nR 194,53,30720\nM 140,176,236\n"
"R 196,55,28672\nM 140,180,100\nR 198,55,30720\nW 4\nM 128,184,72\nR 176,48,0\nM 128,188,104\nR 178,48,2048\nM 132,184,76\nR 180,50,0\nM 132,188,108\n"
"R 182,50,2048\nW 4\nM 136,192,72\nR 184,49,0\nM 136,196,104\nR 186,49,2048\nM 140,192,76\nR 188,51,0\nM 140,196,108\nR 190,51,2048\n"
"s_cbranch_vccz 248f\ns_waitcnt vmcnt(7)\ns_barrier\n248:\ns_nop 5\nv_cvt_pk_bf16_f32 v144, v128, v129\nv_cvt_pk_bf16_f32 v145, v130, v131\n"
"v_cvt_pk_bf16_f32 v146, v136, v137\nv_cvt_pk_bf16_f32 v147, v138, v139\nv_cvt_pk_bf16_f32 v148, v132, v133\nv_cvt_pk_bf16_f32 v149, v134, v135\n"
"v_cvt_pk_bf16_f32 v150, v140, v141\nv_cvt_pk_bf16_f32 v151, v142, v143\ns_add_u32 s74, s66, 6\ns_and_b32 s74, s74, 7\ns_lshl_b32 s74, s74, 14\n"
"s_add_u32 s74, s74, s65\nbuffer_store_dwordx4 v[144:147], v248, s[52:55], s74 offen sc1\n"
"buffer_store_dwordx4 v[148:151], v248, s[52:55], s74 offen offset:256 sc1\ns_cbranch_vccnz 249f\ns_waitcnt vmcnt(9)\ns_barrier\n249:\nW 4\nM 152,176,0\n"
"R 192,48,8192\nM 152,180,16\nR 194,48,10240\nM 156,176,4\nR 196,50,8192\nM 156,180,20\nR 198,50,10240\nW 4\nM 160,184,0\nR 176,49,8192\nM 160,188,16\n"
"R 178,49,10240\nM 164,184,4\nR 180,51,8192\nM 164,188,20\nR 182,51,10240\nW 4\nM 152,192,8\nR 184,48,16384\nM 152,196,24\nR 186,48,18432\nM 156,192,12\n"
"R 188,50,16384\nM 156,196,28\nR 190,50,18432\nW 4\nM 160,176,8\nR 192,49,16384\nM 160,180,24\nR 194,49,18432\nM 164,176,12\nR 196,51,16384\n"
"M 164,180,28\nR 198,51,18432\nW 4\nM 152,184,200\nR 176,48,24576\nM 152,188,32\nR 178,48,26624\nM 156,184,204\nR 180,50,24576\nM 156,188,36\n"
"R 182,50,26624\nW 4\nM 160,192,200\nR 184,49,24576\nM 160,196,32\nR 186,49,26624\nM 164,192,204\nR 188,51,24576\nM 164,196,36\nR 190,51,26624\n"
"s_cbranch_vccz 250f\ns_waitcnt vmcnt(4)\ns_barrier\n250:\ns_waitcnt vmcnt(6)\nv_lshlrev_b32 v240, 16, v168\nv_and_b32 v144, 0xffff0000, v168\n"
"v_lshlrev_b32 v241, 16, v169\nv_and_b32 v145, 0xffff0000, v169\nv_lshlrev_b32 v242, 16, v170\nv_and_b32 v146, 0xffff0000, v170\n"
"v_lshlrev_b32 v243, 16, v171\nv_and_b32 v147, 0xffff0000, v171\nv_lshlrev_b32 v244, 16, v172\nv_and_b32 v148, 0xffff0000, v172\n"
"v_lshlrev_b32 v245, 16, v173\nv_and_b32 v149, 0xffff0000, v173\nv_lshlrev_b32 v246, 16, v174\nv_and_b32 v150, 0xffff0000, v174\n"
"v_lshlrev_b32 v247, 16, v175\nv_and_b32 v151, 0xffff0000, v175\ns_waitcnt vmcnt(2)\nv_readfirstlane_b32 s74, v254\ns_mul_i32 s44, s44, 147456\n"
"s_add_u32 s44, s44, s68\ns_cmp_eq_u32 s74, s72\ns_cbranch_scc1 55f\ns_mov_b32 s75, 0x40000\n35:\ns_sleep 1\n"
"buffer_load_dword v254, off, s[52:55], s70 sc1\ns_waitcnt vmcnt(0)\nv_readfirstlane_b32 s74, v254\ns_sub_u32 s75, s75, 1\ns_cmp_eq_u32 s75, 0\n"
"s_cbranch_scc1 55f\ns_cmp_lg_u32 s74, s72\ns_cbranch_scc1 35b\n55:\nbuffer_load_dwordx4 v[168:171], v248, s[52:55], s44 offen sc1\n"
"buffer_load_dwordx4 v[172:175], v248, s[52:55], s44 offen offset:256 sc1\ns_cbranch_vccnz 251f\ns_waitcnt vmcnt(6)\ns_barrier\n251:\nW 4\n"
"M 152,176,208\nR 192,48,32768\nM 152,180,40\nR 194,48,34816\nM 156,176,212\nR 196,50,32768\nM 156,180,44\nR 198,50,34816\nW 4\nM 160,184,208\n"
"R 176,49,32768\nM 160,188,40\nR 178,49,34816\nM 164,184,212\nR 180,51,32768\nM 164,188,44\nR 182,51,34816\nW 4\nM 152,192,56\nR 184,48,40960\n"
"M 152,196,64\nR 186,48,43008\nM 156,192,60\nR 188,50,40960\nM 156,196,68\nR 190,50,43008\nW 4\nM 160,176,56\nR 192,49,40960\nM 160,180,64\n"
"R 194,49,43008\nM 164,176,60\nR 196,51,40960\nM 164,180,68\nR 198,51,43008\nW 4\nM 152,184,216\nR 176,48,49152\nM 152,188,80\nR 178,48,51200\n"
"M 156,184,220\nR 180,50,49152\nM 156,188,84\nR 182,50,51200\nW 4\nM 160,192,216\nR 184,49,49152\nM 160,196,80\nR 186,49,51200\nM 164,192,220\n"
"R 188,51,49152\nM 164,196,84\nR 190,51,51200\ns_cbranch_vccz 252f\ns_waitcnt vmcnt(2)\ns_barrier\ns_sub_u32 s75, s73, 8192\ns_cmp_lg_u32 s71, 4\n"
"s_cbranch_scc1 47f\ns_mov_b64 exec, 1\nv_mov_b32 v251, s72\nbuffer_store_dword v251, off, s[52:55], s75 sc1\ns_mov_b64 exec, -1\n47:\n252:\ns_nop 5\n"
"v_add_f32 v152, v152, v240\nv_add_f32 v153, v153, v144\nv_add_f32 v154, v154, v241\nv_add_f32 v155, v155, v145\nv_add_f32 v160, v160, v242\n"
"v_add_f32 v161, v161, v146\nv_add_f32 v162, v162, v243\nv_add_f32 v163, v163, v147\nv_add_f32 v156, v156, v244\nv_add_f32 v157, v157, v148\n"
"v_add_f32 v158, v158, v245\nv_add_f32 v159, v159, v149\nv_add_f32 v164, v164, v246\nv_add_f32 v165, v165, v150\nv_add_f32 v166, v166, v247\n"
"v_add_f32 v167, v167, v151\ns_cbranch_vccnz 253f\ns_waitcnt vmcnt(2)\ns_barrier\n253:\nW 4\nM 152,176,224\nR 192,48,57344\nM 152,180,88\n"
"R 194,48,59392\nM 156,176,228\nR 196,50,57344\nM 156,180,92\nR 198,50,59392\nW 4\nM 160,184,224\nR 176,49,57344\nM 160,188,88\nR 178,49,59392\n"
"M 164,184,228\nR 180,51,57344\nM 164,188,92\nR 182,51,59392\nW 4\nM 152,192,232\nR 184,52,4096\nM 152,196,96\nR 186,52,6144\nM 156,192,236\n"
"R 188,54,4096\nM 156,196,100\nR 190,54,6144\nW 4\nM 160,176,232\nR 192,53,4096\nM 160,180,96\nR 194,53,6144\nM 164,176,236\nR 196,55,4096\n"
"M 164,180,100\nR 198,55,6144\nW 4\nM 152,184,72\nM 152,188,104\nM 156,184,76\nM 156,188,108\nW 0\nM 160,192,72\nM 160,196,104\nM 164,192,76\n"
"M 164,196,108\ns_cbranch_vccz 254f\ns_barrier\n254:\ns_nop 6\ns_waitcnt vmcnt(0)\nv_lshlrev_b32 v240, 16, v168\nv_and_b32 v168, 0xffff0000, v168\n"
"v_lshlrev_b32 v241, 16, v169\nv_and_b32 v169, 0xffff0000, v169\nv_lshlrev_b32 v242, 16, v170\nv_and_b32 v170, 0xffff0000, v170\n"
"v_lshlrev_b32 v243, 16, v171\nv_and_b32 v171, 0xffff0000, v171\nv_lshlrev_b32 v244, 16, v172\nv_and_b32 v172, 0xffff0000, v172\n"
"v_lshlrev_b32 v245, 16, v173\nv_and_b32 v173, 0xffff0000, v173\nv_lshlrev_b32 v246, 16, v174\nv_and_b32 v174, 0xffff0000, v174\n"
"v_lshlrev_b32 v247, 16, v175\nv_and_b32 v175, 0xffff0000, v175\nv_add_f32 v152, v152, v240\nv_add_f32 v153, v153, v168\nv_add_f32 v154, v154, v241\n"
"v_add_f32 v155, v155, v169\nv_add_f32 v160, v160, v242\nv_add_f32 v161, v161, v170\nv_add_f32 v162, v162, v243\nv_add_f32 v163, v163, v171\n"
"v_add_f32 v156, v156, v244\nv_add_f32 v157, v157, v172\nv_add_f32 v158, v158, v245\nv_add_f32 v159, v159, v173\nv_add_f32 v164, v164, v246\n"
"v_add_f32 v165, v165, v174\nv_add_f32 v166, v166, v247\nv_add_f32 v167, v167, v175\nv_cvt_pk_bf16_f32 v144, v152, v153\n"
"v_cvt_pk_bf16_f32 v145, v154, v155\nv_cvt_pk_bf16_f32 v146, v160, v161\nv_cvt_pk_bf16_f32 v147, v162, v163\nv_cvt_pk_bf16_f32 v148, v156, v157\n"
"v_cvt_pk_bf16_f32 v149, v158, v159\nv_cvt_pk_bf16_f32 v150, v164, v165\nv_cvt_pk_bf16_f32 v151, v166, v167\ns_add_u32 s74, s66, 7\n"
"s_and_b32 s74, s74, 7\ns_lshl_b32 s74, s74, 14\ns_add_u32 s74, s74, s65\nbuffer_store_dwordx4 v[144:147], v248, s[52:55], s74 offen sc1\n"
"buffer_store_dwordx4 v[148:151], v248, s[52:55], s74 offen offset:256 sc1\ns_cbranch_vccnz 255f\ns_barrier\n255:\ns_waitcnt vmcnt(0)\ns_barrier\n"
"s_cmp_lg_u32 s71, 0\ns_cbranch_scc1 62f\ns_mov_b64 exec, 1\nv_mov_b32 v254, s72\nbuffer_store_dword v254, off, s[52:55], s73 sc1\ns_mov_b64 exec, -1\n"
"62:\ns_lshl_b32 s74, s84, 3\ns_add_u32 s74, s74, s71\ns_cmp_lg_u32 s74, 1\ns_cbranch_scc1 67f\nv_mbcnt_lo_u32_b32 v0, -1, 0\n"
"v_mbcnt_hi_u32_b32 v0, -1, v0\nv_lshlrev_b32 v0, 6, v0\ns_mov_b32 s75, 0x40000\ns_mov_b32 s40, 4096\ns_mov_b32 s41, 8192\ns_mov_b32 s42, 53248\n"
"s_mov_b32 s43, 57344\n66:\nbuffer_load_dword v1, v0, s[52:55], s40 offen sc1\nbuffer_load_dword v2, v0, s[52:55], s41 offen sc1\n"
"buffer_load_dword v3, v0, s[52:55], s42 offen sc1\nbuffer_load_dword v4, v0, s[52:55], s43 offen sc1\ns_waitcnt vmcnt(0)\nv_xor_b32 v1, s72, v1\n"
"v_xor_b32 v2, s72, v2\nv_xor_b32 v3, s72, v3\nv_xor_b32 v4, s72, v4\nv_or3_b32 v1, v1, v2, v3\nv_or_b32 v1, v1, v4\nv_cmp_ne_u32 s[44:45], 0, v1\n"
"s_sub_u32 s75, s75, 1\ns_cmp_eq_u32 s75, 0\ns_cbranch_scc1 68f\ns_cmp_lg_u64 s[44:45], 0\ns_cbranch_scc1 66b\n68:\ns_mov_b64 exec, 1\n"
"v_mov_b32 v1, s72\nbuffer_store_dword v1, off, s[52:55], 0 sc1\n67:\ns_endpgm\n96:\ns_lshl_b32 s61, s76, 7\ns_add_u32 s62, s62, s61\n"
"s_mov_b32 s93, s66\ns_lshl_b32 s64, s75, 6\ns_add_u32 s64, s64, 2304\ns_add_u32 s61, s63, 384\ns_add_u32 s94, s93, 128\ns_cmp_eq_u32 s94, s61\n"
"s_cselect_b32 s94, s63, s94\ns_add_u32 s95, s94, 128\ns_cmp_eq_u32 s95, s61\ns_cselect_b32 s95, s63, s95\ns_add_u32 s73, s73, 4096\n"
"s_mov_b32 s66, 0x3eae1a93\ns_mov_b32 s80, s74\ns_and_b32 s81, s74, 7\ns_lshl_b32 s81, s81, 3\ns_add_u32 s81, s81, s71\ns_mul_i32 s81, s81, 9\n"
"s_mov_b32 s50, 1990656\ns_mov_b32 s51, 0x20000\ns_cmp_ge_u32 s75, 6\ns_cbranch_scc1 72f\ns_cmp_ge_u32 s75, 3\ns_cbranch_scc1 71f\nR 176,244,0\n"
"R 178,244,512\nR 180,244,4096\nR 182,244,4608\nR 184,245,0\nR 186,245,512\nR 188,245,4096\nR 190,245,4608\ns_cbranch_vccz 1000f\n"
"s_add_u32 m0, s85, 113920\ns_nop 0\nbuffer_load_dwordx4 v216, s[56:59], s90 offen offset:768 lds\ns_add_u32 m0, s85, 114944\ns_nop 0\n"
"buffer_load_dwordx4 v217, s[56:59], s90 offen offset:768 lds\ns_add_u32 m0, s91, 113920\ns_nop 0\n"
"buffer_load_dwordx4 v216, s[56:59], s92 offen offset:768 lds\ns_add_u32 m0, s91, 114944\ns_nop 0\n"
"buffer_load_dwordx4 v217, s[56:59], s92 offen offset:768 lds\ns_add_u32 m0, s85, 130176\ns_nop 0\n"
"buffer_load_dwordx4 v216, s[56:59], s90 offen offset:896 lds\ns_add_u32 m0, s85, 131200\ns_nop 0\n"
"buffer_load_dwordx4 v217, s[56:59], s90 offen offset:896 lds\n1000:\nW 4\nZ 144,176,0\nR 192,244,8192\nM 144,180,8\nR 194,244,8704\nZ 148,176,4\n"
"R 196,244,12288\nM 148,180,12\nR 198,244,12800\nW 4\nZ 152,184,0\nR 176,245,8192\nM 152,188,8\nR 178,245,8704\nZ 156,184,4\nR 180,245,12288\n"
"M 156,188,12\nR 182,245,12800\nW 4\nM 144,192,16\nR 184,244,16384\nM 144,196,24\nR 186,244,16896\nM 148,192,20\nR 188,244,20480\nM 148,196,28\n"
"R 190,244,20992\nW 4\nM 152,176,16\nR 192,245,16384\nM 152,180,24\nR 194,245,16896\nM 156,176,20\nR 196,245,20480\nM 156,180,28\nR 198,245,20992\nW 4\n"
"M 144,184,32\nM 144,188,40\nM 148,184,36\nM 148,188,44\nW 0\nM 152,192,32\nM 152,196,40\nM 156,192,36\nM 156,196,44\nv_lshlrev_b32 v224, 4, v240\n"
"s_lshl_b32 s74, s71, 11\nv_add_u32 v251, s74, v224\nv_lshrrev_b32 v241, 4, v240\nv_lshlrev_b32 v253, 14, v241\nv_and_b32 v242, 0xf0, v224\n"
"v_add_u32 v253, v253, v242\ns_lshr_b32 s74, s71, 2\ns_lshl_b32 s74, s74, 17\ns_and_b32 s75, s71, 3\ns_lshl_b32 s75, s75, 8\ns_add_u32 s74, s74, s75\n"
"s_lshl_b32 s75, s80, 10\ns_add_u32 s74, s74, s75\nv_add_u32 v253, s74, v253\ns_cbranch_vccnz 1001f\ns_add_u32 m0, s85, 130176\ns_nop 0\n"
"buffer_load_dwordx4 v216, s[56:59], s90 offen offset:896 lds\ns_add_u32 m0, s85, 131200\ns_nop 0\n"
"buffer_load_dwordx4 v217, s[56:59], s90 offen offset:896 lds\ns_add_u32 m0, s85, 146432\ns_nop 0\n"
"buffer_load_dwordx4 v216, s[56:59], s90 offen offset:1024 lds\ns_add_u32 m0, s85, 147456\ns_nop 0\n"
"buffer_load_dwordx4 v217, s[56:59], s90 offen offset:1024 lds\ns_add_u32 m0, s91, 146432\ns_nop 0\n"
"buffer_load_dwordx4 v216, s[56:59], s92 offen offset:1024 lds\ns_add_u32 m0, s91, 147456\ns_nop 0\n"
"buffer_load_dwordx4 v217, s[56:59], s92 offen offset:1024 lds\n1001:\ns_cbranch_vccnz 1002f\ns_waitcnt vmcnt(6)\n1002:\ns_cbranch_vccz 1003f\n"
"s_waitcnt vmcnt(6)\n1003:\nv_add_u32 v218, 0xffff4000, v218\nv_add_u32 v219, 0xffff4000, v219\ns_barrier\nds_read_b128 v[48:51], v218 offset:0\n"
"ds_read_b128 v[52:55], v218 offset:2048\nds_read_b128 v[56:59], v219 offset:0\nds_read_b128 v[60:63], v219 offset:2048\n"
"ds_read_b128 v[64:67], v218 offset:16384\nds_read_b128 v[68:71], v218 offset:18432\nds_read_b128 v[72:75], v219 offset:16384\n"
"ds_read_b128 v[76:79], v219 offset:18432\nds_read_b128 v[80:83], v218 offset:32768\nds_read_b128 v[84:87], v218 offset:34816\n"
"ds_read_b128 v[88:91], v219 offset:32768\nds_read_b128 v[92:95], v219 offset:34816\nW 0\nR 176,244,24576\nR 178,244,25088\nR 180,244,28672\n"
"R 182,244,29184\nR 184,245,24576\nR 186,245,25088\nR 188,245,28672\nR 190,245,29184\ns_barrier\ns_cbranch_vccz 1004f\ns_add_u32 m0, s60, 65536\n"
"s_add_u32 s61, s93, 1769472\nbuffer_load_dwordx4 v250, s[48:51], s61 offen lds\ns_sub_u32 m0, m0, 4096\ns_nop 0\n"
"buffer_load_dwordx4 v221, s[48:51], s61 offen lds\ns_add_u32 m0, s60, 73728\ns_add_u32 s61, s94, 0\nbuffer_load_dwordx4 v250, s[48:51], s61 offen lds\n"
"s_sub_u32 m0, m0, 4096\ns_nop 0\nbuffer_load_dwordx4 v221, s[48:51], s61 offen lds\ns_add_u32 m0, s60, 81920\ns_add_u32 s61, s94, 221184\n"
"buffer_load_dwordx4 v250, s[48:51], s61 offen lds\ns_sub_u32 m0, m0, 4096\ns_nop 0\nbuffer_load_dwordx4 v221, s[48:51], s61 offen lds\n1004:\nW 4\n"
"M 144,176,48\nR 192,244,32768\nM 144,180,56\nR 194,244,33280\nM 148,176,52\nR 196,244,36864\nM 148,180,60\nR 198,244,37376\nW 4\nM 152,184,48\n"
"R 176,245,32768\nM 152,188,56\nR 178,245,33280\nM 156,184,52\nR 180,245,36864\nM 156,188,60\nR 182,245,37376\nW 4\nM 144,192,64\nR 184,244,40960\n"
"M 144,196,72\nR 186,244,41472\nM 148,192,68\nR 188,244,45056\nM 148,196,76\nR 190,244,45568\nW 4\nM 152,176,64\nR 192,245,40960\nM 152,180,72\n"
"R 194,245,41472\nM 156,176,68\nR 196,245,45056\nM 156,180,76\nR 198,245,45568\nW 4\nM 144,184,80\nM 144,188,88\nM 148,184,84\nM 148,188,92\nW 0\n"
"M 152,192,80\nM 152,196,88\nM 156,192,84\nM 156,196,92\nv_lshrrev_b32 v241, 2, v240\ns_lshl_b32 s74, s71, 4\nv_add_u32 v241, s74, v241\n"
"v_mul_u32_u24 v207, 0xd80, v241\nv_lshrrev_b32 v241, 4, v240\nv_and_b32 v242, 2, v241\nv_and_b32 v243, 3, v240\nv_xor_b32 v242, v242, v243\n"
"v_lshl_add_u32 v207, v242, 4, v207\nv_bfe_u32 v242, v240, 2, 2\nv_lshl_add_u32 v242, v241, 3, v242\nv_lshlrev_b32 v204, 6, v242\n"
"v_lshl_add_u32 v204, v243, 3, v204\nv_and_b32 v241, 1, v241\ns_lshr_b32 s74, s71, 2\nv_xor_b32 v241, s74, v241\nv_lshl_add_u32 v204, v241, 5, v204\n"
"v_add_u32 v205, 61440, v204\nv_mul_u32_u24 v237, 0xd80, v240\ns_mul_i32 s74, s81, 3456\ns_add_u32 s74, s74, s64\ns_mov_b64 exec, 0x1ff\n"
"buffer_load_dword v234, v237, s[48:51], s74 offen\ns_mov_b64 exec, -1\ns_cbranch_vccnz 1005f\ns_add_u32 m0, s60, 90112\ns_add_u32 s61, s94, 442368\n"
"buffer_load_dwordx4 v250, s[48:51], s61 offen lds\ns_add_u32 m0, m0, 4096\ns_add_u32 s61, s61, 110592\n"
"buffer_load_dwordx4 v250, s[48:51], s61 offen lds\ns_add_u32 m0, s60, 0\ns_add_u32 s61, s94, 663552\nbuffer_load_dwordx4 v250, s[48:51], s61 offen lds\n"
"s_add_u32 m0, m0, 4096\ns_add_u32 s61, s61, 110592\nbuffer_load_dwordx4 v250, s[48:51], s61 offen lds\ns_add_u32 m0, s60, 8192\n"
"s_add_u32 s61, s94, 884736\nbuffer_load_dwordx4 v250, s[48:51], s61 offen lds\ns_add_u32 m0, m0, 4096\ns_add_u32 s61, s61, 110592\n"
"buffer_load_dwordx4 v250, s[48:51], s61 offen lds\ns_add_u32 m0, s60, 16384\ns_add_u32 s61, s94, 1105920\n"
"buffer_load_dwordx4 v250, s[48:51], s61 offen lds\ns_add_u32 m0, m0, 4096\ns_add_u32 s61, s61, 110592\n"
"buffer_load_dwordx4 v250, s[48:51], s61 offen lds\n1005:\ns_cbranch_vccnz 1006f\ns_waitcnt vmcnt(9)\n1006:\ns_cbranch_vccz 1007f\ns_waitcnt vmcnt(1)\n"
"1007:\nv_add_u32 v218, 0xc000, v218\nv_add_u32 v219, 0xc000, v219\ns_barrier\nds_read_b128 v[96:99], v218 offset:0\n"
"ds_read_b128 v[100:103], v218 offset:2048\nds_read_b128 v[104:107], v219 offset:0\nds_read_b128 v[108:111], v219 offset:2048\n"
"ds_read_b128 v[112:115], v218 offset:16384\nds_read_b128 v[116:119], v218 offset:18432\nds_read_b128 v[120:123], v219 offset:16384\n"
"ds_read_b128 v[124:127], v219 offset:18432\nds_read_b128 v[128:131], v218 offset:32768\nds_read_b128 v[132:135], v218 offset:34816\n"
"ds_read_b128 v[136:139], v219 offset:32768\nds_read_b128 v[140:143], v219 offset:34816\nW 0\nR 176,244,49152\nR 178,244,49664\nR 180,244,53248\n"
"R 182,244,53760\nR 184,245,49152\nR 186,245,49664\nR 188,245,53248\nR 190,245,53760\ns_barrier\ns_cbranch_vccz 1008f\ns_add_u32 m0, s60, 98304\n"
"s_add_u32 s61, s94, 1327104\nbuffer_load_dwordx4 v250, s[48:51], s61 offen lds\ns_sub_u32 m0, m0, 4096\ns_nop 0\n"
"buffer_load_dwordx4 v221, s[48:51], s61 offen lds\ns_add_u32 m0, s60, 106496\ns_add_u32 s61, s94, 1548288\n"
"buffer_load_dwordx4 v250, s[48:51], s61 offen lds\ns_sub_u32 m0, m0, 4096\ns_nop 0\nbuffer_load_dwordx4 v221, s[48:51], s61 offen lds\n"
"s_add_u32 m0, s60, 114688\ns_add_u32 s61, s94, 1769472\nbuffer_load_dwordx4 v250, s[48:51], s61 offen lds\ns_sub_u32 m0, m0, 4096\ns_nop 0\n"
"buffer_load_dwordx4 v221, s[48:51], s61 offen lds\n1008:\nW 4\nM 144,176,96\nR 192,244,57344\nM 144,180,104\nR 194,244,57856\nM 148,176,100\n"
"R 196,244,61440\nM 148,180,108\nR 198,244,61952\nW 4\nM 152,184,96\nR 176,245,57344\nM 152,188,104\nR 178,245,57856\nM 156,184,100\nR 180,245,61440\n"
"M 156,188,108\nR 182,245,61952\nW 4\nM 144,192,112\nR 184,246,4096\nM 144,196,120\nR 186,246,4608\nM 148,192,116\nR 188,246,8192\nM 148,196,124\n"
"R 190,246,8704\nW 4\nM 152,176,112\nR 192,247,4096\nM 152,180,120\nR 194,247,4608\nM 156,176,116\nR 196,247,8192\nM 156,180,124\nR 198,247,8704\nW 4\n"
"M 144,184,128\nR 176,246,12288\nM 144,188,136\nR 178,246,12800\nM 148,184,132\nR 180,246,16384\nM 148,188,140\nR 182,246,16896\nW 4\nM 152,192,128\n"
"R 184,247,12288\nM 152,196,136\nR 186,247,12800\nM 156,192,132\nR 188,247,16384\nM 156,196,140\nR 190,247,16896\ns_cbranch_vccz 1009f\n"
"s_waitcnt vmcnt(7)\ns_barrier\n1009:\ns_add_u32 m0, s60, 24576\ns_add_u32 s61, s95, 0\nbuffer_load_dwordx4 v250, s[48:51], s61 offen lds\ns_nop 2\n"
"v_mul_f32 v144, s66, v144\nv_mul_f32 v145, s66, v145\nv_mul_f32 v146, s66, v146\nv_mul_f32 v147, s66, v147\nv_mul_f32 v148, s66, v148\n"
"v_mul_f32 v149, s66, v149\nv_mul_f32 v150, s66, v150\nv_mul_f32 v151, s66, v151\ns_add_u32 m0, s60, 32768\ns_add_u32 s61, s95, 221184\n"
"buffer_load_dwordx4 v250, s[48:51], s61 offen lds\nv_mul_f32 v152, s66, v152\nv_mul_f32 v153, s66, v153\nv_mul_f32 v154, s66, v154\n"
"v_mul_f32 v155, s66, v155\nv_mul_f32 v156, s66, v156\nv_mul_f32 v157, s66, v157\nv_mul_f32 v158, s66, v158\nv_mul_f32 v159, s66, v159\n"
"s_add_u32 m0, s60, 40960\ns_add_u32 s61, s95, 442368\nbuffer_load_dwordx4 v250, s[48:51], s61 offen lds\nv_cvt_pk_bf16_f32 v208, v144, v145\n"
"v_cvt_pk_bf16_f32 v209, v146, v147\nv_cvt_pk_bf16_f32 v210, v148, v149\nv_cvt_pk_bf16_f32 v211, v150, v151\nv_cvt_pk_bf16_f32 v212, v152, v153\n"
"v_cvt_pk_bf16_f32 v213, v154, v155\nv_cvt_pk_bf16_f32 v214, v156, v157\nv_cvt_pk_bf16_f32 v215, v158, v159\ns_lshl_b32 s74, s93, 11\n"
"s_add_u32 s74, s74, 1048576\ns_add_u32 s75, s74, 65536\nbuffer_store_dwordx4 v[208:211], v253, s[52:55], s74 offen sc1\n"
"buffer_store_dwordx4 v[212:215], v253, s[52:55], s75 offen sc1\ns_cbranch_vccnz 1010f\ns_waitcnt vmcnt(7)\ns_barrier\n1010:\nW 4\nZ 144,176,0\n"
"R 192,246,20480\nM 144,180,8\nR 194,246,20992\nZ 148,176,4\nR 196,246,24576\nM 148,180,12\nR 198,246,25088\nW 4\nZ 152,184,0\nR 176,247,20480\n"
"M 152,188,8\nR 178,247,20992\nZ 156,184,4\nR 180,247,24576\nM 156,188,12\nR 182,247,25088\nW 4\nM 144,192,16\nR 184,246,28672\nM 144,196,24\n"
"R 186,246,29184\nM 148,192,20\nR 188,246,32768\nM 148,196,28\nR 190,246,33280\nW 4\nM 152,176,16\nR 192,247,28672\nM 152,180,24\nR 194,247,29184\n"
"M 156,176,20\nR 196,247,32768\nM 156,180,28\nR 198,247,33280\nW 4\nM 144,184,32\nR 176,244,0\nM 144,188,40\nR 178,244,512\nM 148,184,36\n"
"R 180,244,4096\nM 148,188,44\nR 182,244,4608\nW 4\nM 152,192,32\nR 184,245,0\nM 152,196,40\nR 186,245,512\nM 156,192,36\nR 188,245,4096\nM 156,196,44\n"
"R 190,245,4608\ns_cbranch_vccz 1011f\ns_waitcnt vmcnt(7)\ns_barrier\n1011:\ns_add_u32 m0, s60, 49152\ns_add_u32 s61, s95, 663552\n"
"buffer_load_dwordx4 v250, s[48:51], s61 offen lds\ns_add_u32 m0, s60, 57344\ns_add_u32 s61, s95, 884736\n"
"buffer_load_dwordx4 v250, s[48:51], s61 offen lds\ns_add_u32 m0, s60, 65536\ns_add_u32 s61, s95, 1105920\n"
"buffer_load_dwordx4 v250, s[48:51], s61 offen lds\ns_cbranch_vccnz 1012f\ns_waitcnt vmcnt(8)\ns_barrier\n1012:\nW 4\nM 144,176,48\nR 192,244,8192\n"
"M 144,180,56\nR 194,244,8704\nM 148,176,52\nR 196,244,12288\nM 148,180,60\nR 198,244,12800\nW 4\nM 152,184,48\nR 176,245,8192\nM 152,188,56\n"
"R 178,245,8704\nM 156,184,52\nR 180,245,12288\nM 156,188,60\nR 182,245,12800\nW 4\nM 144,192,64\nR 184,244,16384\nM 144,196,72\nR 186,244,16896\n"
"M 148,192,68\nR 188,244,20480\nM 148,196,76\nR 190,244,20992\nW 4\nM 152,176,64\nR 192,245,16384\nM 152,180,72\nR 194,245,16896\nM 156,176,68\n"
"R 196,245,20480\nM 156,180,76\nR 198,245,20992\nW 4\nM 144,184,80\nR 176,246,36864\nM 144,188,88\nR 178,246,37376\nM 148,184,84\nR 180,246,40960\n"
"M 148,188,92\nR 182,246,41472\nW 4\nM 152,192,80\nR 184,247,36864\nM 152,196,88\nR 186,247,37376\nM 156,192,84\nR 188,247,40960\nM 156,196,92\n"
"R 190,247,41472\ns_cbranch_vccz 1013f\ns_waitcnt vmcnt(6)\ns_barrier\n1013:\ns_add_u32 m0, s60, 73728\ns_add_u32 s61, s95, 1327104\n"
"buffer_load_dwordx4 v250, s[48:51], s61 offen lds\ns_add_u32 m0, s60, 81920\ns_add_u32 s61, s95, 1548288\n"
"buffer_load_dwordx4 v250, s[48:51], s61 offen lds\ns_add_u32 m0, s60, 90112\ns_add_u32 s61, s95, 1769472\n"
"buffer_load_dwordx4 v250, s[48:51], s61 offen lds\ns_cbranch_vccnz 1014f\ns_waitcnt vmcnt(9)\ns_barrier\n1014:\nW 4\nM 144,176,96\nR 192,246,45056\n"
"M 144,180,104\nR 194,246,45568\nM 148,176,100\nR 196,246,49152\nM 148,180,108\nR 198,246,49664\nW 4\nM 152,184,96\nR 176,247,45056\nM 152,188,104\n"
"R 178,247,45568\nM 156,184,100\nR 180,247,49152\nM 156,188,108\nR 182,247,49664\nW 4\nM 144,192,112\nR 184,246,53248\nM 144,196,120\nR 186,246,53760\n"
"M 148,192,116\nR 188,246,57344\nM 148,196,124\nR 190,246,57856\nW 4\nM 152,176,112\nR 192,247,53248\nM 152,180,120\nR 194,247,53760\nM 156,176,116\n"
"R 196,247,57344\nM 156,180,124\nR 198,247,57856\nW 4\nM 144,184,128\nR 176,244,24576\nM 144,188,136\nR 178,244,25088\nM 148,184,132\nR 180,244,28672\n"
"M 148,188,140\nR 182,244,29184\nW 4\nM 152,192,128\nR 184,245,24576\nM 152,196,136\nR 186,245,25088\nM 156,192,132\nR 188,245,28672\nM 156,196,140\n"
"R 190,245,29184\ns_cbranch_vccz 1015f\ns_waitcnt vmcnt(4)\ns_barrier\n1015:\ns_add_u32 m0, s60, 0\ns_add_u32 s61, s64, 0\n"
"buffer_load_dwordx4 v207, s[48:51], s61 offen lds\ns_nop 2\nv_mul_f32 v144, s66, v144\nv_mul_f32 v145, s66, v145\nv_mul_f32 v146, s66, v146\n"
"v_mul_f32 v147, s66, v147\nv_mul_f32 v148, s66, v148\nv_mul_f32 v149, s66, v149\nv_mul_f32 v150, s66, v150\nv_mul_f32 v151, s66, v151\n"
"s_add_u32 m0, s60, 8192\ns_add_u32 s61, s64, 442368\nbuffer_load_dwordx4 v207, s[48:51], s61 offen lds\nv_mul_f32 v152, s66, v152\n"
"v_mul_f32 v153, s66, v153\nv_mul_f32 v154, s66, v154\nv_mul_f32 v155, s66, v155\nv_mul_f32 v156, s66, v156\nv_mul_f32 v157, s66, v157\n"
"v_mul_f32 v158, s66, v158\nv_mul_f32 v159, s66, v159\ns_add_u32 m0, s60, 16384\ns_add_u32 s61, s64, 884736\n"
"buffer_load_dwordx4 v207, s[48:51], s61 offen lds\nv_cvt_pk_bf16_f32 v208, v144, v145\nv_cvt_pk_bf16_f32 v209, v146, v147\n"
"v_cvt_pk_bf16_f32 v210, v148, v149\nv_cvt_pk_bf16_f32 v211, v150, v151\nv_cvt_pk_bf16_f32 v212, v152, v153\nv_cvt_pk_bf16_f32 v213, v154, v155\n"
"v_cvt_pk_bf16_f32 v214, v156, v157\nv_cvt_pk_bf16_f32 v215, v158, v159\ns_lshl_b32 s74, s94, 11\ns_add_u32 s74, s74, 1048576\n"
"s_add_u32 s75, s74, 65536\nbuffer_store_dwordx4 v[208:211], v253, s[52:55], s74 offen sc1\n"
"buffer_store_dwordx4 v[212:215], v253, s[52:55], s75 offen sc1\ns_cbranch_vccnz 1016f\ns_waitcnt vmcnt(9)\ns_barrier\n1016:\nW 4\nZ 144,176,0\n"
"R 192,244,32768\nM 144,180,8\nR 194,244,33280\nZ 148,176,4\nR 196,244,36864\nM 148,180,12\nR 198,244,37376\nW 4\nZ 152,184,0\nR 176,245,32768\n"
"M 152,188,8\nR 178,245,33280\nZ 156,184,4\nR 180,245,36864\nM 156,188,12\nR 182,245,37376\nW 4\nM 144,192,16\nR 184,244,40960\nM 144,196,24\n"
"R 186,244,41472\nM 148,192,20\nR 188,244,45056\nM 148,196,28\nR 190,244,45568\nW 4\nM 152,176,16\nR 192,245,40960\nM 152,180,24\nR 194,245,41472\n"
"M 156,176,20\nR 196,245,45056\nM 156,180,28\nR 198,245,45568\nW 4\nM 144,184,32\nR 176,244,49152\nM 144,188,40\nR 178,244,49664\nM 148,184,36\n"
"R 180,244,53248\nM 148,188,44\nR 182,244,53760\nW 4\nM 152,192,32\nR 184,245,49152\nM 152,196,40\nR 186,245,49664\nM 156,192,36\nR 188,245,53248\n"
"M 156,196,44\nR 190,245,53760\ns_cbranch_vccz 1017f\ns_waitcnt vmcnt(6)\ns_barrier\n1017:\ns_add_u32 m0, s60, 98304\ns_add_u32 s61, s64, 1327104\n"
"buffer_load_dwordx4 v207, s[48:51], s61 offen lds\ns_lshl_b32 s74, s80, 2\ns_and_b32 s75, s71, 3\ns_add_u32 s74, s74, s75\ns_lshl_b32 s74, s74, 2\n"
"v_lshrrev_b32 v206, 4, v240\nv_add_u32 v206, s74, v206\nv_mul_u32_u24 v206, 0x120, v206\ns_sub_u32 s74, s64, 2304\ns_lshr_b32 s74, s74, 1\n"
"s_lshr_b32 s75, s71, 2\ns_lshl_b32 s75, s75, 4\ns_add_u32 s74, s74, s75\nv_and_b32 v241, 15, v240\nv_add_u32 v241, s74, v241\n"
"v_mul_u32_u24 v242, 0xe39, v241\nv_lshrrev_b32 v242, 16, v242\nv_mul_u32_u24 v200, 18, v242\nv_sub_u32 v200, v241, v200\nv_and_b32 v243, 1, v242\n"
"s_add_u32 m0, s60, 106496\ns_add_u32 s61, s64, 1769472\nbuffer_load_dwordx4 v207, s[48:51], s61 offen lds\nv_lshlrev_b32 v243, 1, v243\n"
"v_sub_u32 v200, v200, v243\nv_ashrrev_i32 v243, 31, v200\nv_and_b32 v243, 18, v243\nv_add_lshl_u32 v200, v200, v243, 4\n"
"v_mul_u32_u24 v242, 0x12000, v242\nv_add3_u32 v200, v200, v242, v206\ns_cbranch_vccnz 1018f\ns_waitcnt vmcnt(8)\ns_barrier\n1018:\nW 4\nM 144,176,48\n"
"R 192,244,57344\nM 144,180,56\nR 194,244,57856\nM 148,176,52\nR 196,244,61440\nM 148,180,60\nR 198,244,61952\nW 4\nM 152,184,48\nR 176,245,57344\n"
"M 152,188,56\nR 178,245,57856\nM 156,184,52\nR 180,245,61440\nM 156,188,60\nR 182,245,61952\nW 4\nM 144,192,64\nR 184,246,4096\nM 144,196,72\n"
"R 186,246,4608\nM 148,192,68\nR 188,246,8192\nM 148,196,76\nR 190,246,8704\nW 4\nM 152,176,64\nR 192,247,4096\nM 152,180,72\nR 194,247,4608\n"
"M 156,176,68\nR 196,247,8192\nM 156,180,76\nR 198,247,8704\nW 4\nM 144,184,80\nR 176,246,12288\nM 144,188,88\nR 178,246,12800\nM 148,184,84\n"
"R 180,246,16384\nM 148,188,92\nR 182,246,16896\nW 4\nM 152,192,80\nR 184,247,12288\nM 152,196,88\nR 186,247,12800\nM 156,192,84\nR 188,247,16384\n"
"M 156,196,92\nR 190,247,16896\ns_cbranch_vccz 1019f\ns_waitcnt vmcnt(5)\ns_barrier\n1019:\ns_add_u32 m0, s60, 24576\ns_add_u32 s61, s62, 0\n"
"buffer_load_dwordx4 v249, s[40:43], s61 offen lds\ns_add_u32 m0, s60, 32768\ns_add_u32 s61, s62, 589824\n"
"buffer_load_dwordx4 v249, s[40:43], s61 offen lds\ns_add_u32 m0, s60, 40960\ns_add_u32 s61, s62, 1179648\n"
"buffer_load_dwordx4 v249, s[40:43], s61 offen lds\ns_cbranch_vccnz 1020f\ns_waitcnt vmcnt(8)\ns_barrier\n1020:\nW 4\nM 144,176,96\nR 192,246,20480\n"
"M 144,180,104\nR 194,246,20992\nM 148,176,100\nR 196,246,24576\nM 148,180,108\nR 198,246,25088\nW 4\nM 152,184,96\nR 176,247,20480\nM 152,188,104\n"
"R 178,247,20992\nM 156,184,100\nR 180,247,24576\nM 156,188,108\nR 182,247,25088\nW 4\nM 144,192,112\nR 184,246,28672\nM 144,196,120\nR 186,246,29184\n"
"M 148,192,116\nR 188,246,32768\nM 148,196,124\nR 190,246,33280\nW 4\nM 152,176,112\nR 192,247,28672\nM 152,180,120\nR 194,247,29184\nM 156,176,116\n"
"R 196,247,32768\nM 156,180,124\nR 198,247,33280\nW 4\nM 144,184,128\nR 176,204,0\nM 144,188,136\nR 178,204,256\nM 148,184,132\nR 180,204,2048\n"
"M 148,188,140\nR 182,204,2304\nW 4\nM 152,192,128\nR 184,204,4096\nM 152,196,136\nR 186,204,4352\nM 156,192,132\nR 188,204,6144\nM 156,196,140\n"
"R 190,204,6400\ns_cbranch_vccz 1021f\ns_waitcnt vmcnt(3)\ns_barrier\n1021:\ns_add_u32 m0, s60, 49152\ns_add_u32 s61, s62, 1769472\n"
"buffer_load_dwordx4 v249, s[40:43], s61 offen lds\ns_nop 2\nv_mul_f32 v144, s66, v144\nv_mul_f32 v145, s66, v145\nv_mul_f32 v146, s66, v146\n"
"v_mul_f32 v147, s66, v147\nv_mul_f32 v148, s66, v148\nv_mul_f32 v149, s66, v149\nv_mul_f32 v150, s66, v150\nv_mul_f32 v151, s66, v151\n"
"s_add_u32 m0, s60, 57344\ns_add_u32 s61, s62, 2359296\nbuffer_load_dwordx4 v249, s[40:43], s61 offen lds\nv_mul_f32 v152, s66, v152\n"
"v_mul_f32 v153, s66, v153\nv_mul_f32 v154, s66, v154\nv_mul_f32 v155, s66, v155\nv_mul_f32 v156, s66, v156\nv_mul_f32 v157, s66, v157\n"
"v_mul_f32 v158, s66, v158\nv_mul_f32 v159, s66, v159\ns_add_u32 m0, s60, 65536\ns_add_u32 s61, s62, 2949120\n"
"buffer_load_dwordx4 v249, s[40:43], s61 offen lds\nv_cvt_pk_bf16_f32 v208, v144, v145\nv_cvt_pk_bf16_f32 v209, v146, v147\n"
"v_cvt_pk_bf16_f32 v210, v148, v149\nv_cvt_pk_bf16_f32 v211, v150, v151\nv_cvt_pk_bf16_f32 v212, v152, v153\nv_cvt_pk_bf16_f32 v213, v154, v155\n"
"v_cvt_pk_bf16_f32 v214, v156, v157\nv_cvt_pk_bf16_f32 v215, v158, v159\ns_lshl_b32 s74, s95, 11\ns_add_u32 s74, s74, 1048576\n"
"s_add_u32 s75, s74, 65536\nbuffer_store_dwordx4 v[208:211], v253, s[52:55], s74 offen sc1\n"
"buffer_store_dwordx4 v[212:215], v253, s[52:55], s75 offen sc1\ns_cbranch_vccnz 1022f\ns_waitcnt vmcnt(8)\ns_barrier\n1022:\nW 4\nZ 144,0,176\n"
"R 192,204,8192\nM 144,8,180\nR 194,204,8448\nZ 148,4,176\nR 196,204,10240\nM 148,12,180\nR 198,204,10496\nW 4\nM 144,16,184\nR 176,204,12288\n"
"M 144,24,188\nR 178,204,12544\nM 148,20,184\nR 180,204,14336\nM 148,28,188\nR 182,204,14592\nW 4\nM 144,32,192\nR 184,204,16384\nM 144,40,196\n"
"R 186,204,16640\nM 148,36,192\nR 188,204,18432\nM 148,44,196\nR 190,204,18688\nW 4\nM 144,48,176\nR 192,204,20480\nM 144,56,180\nR 194,204,20736\n"
"M 148,52,176\nR 196,204,22528\nM 148,60,180\nR 198,204,22784\nW 4\nM 144,64,184\nR 176,205,36864\nM 144,72,188\nR 178,205,37120\nM 148,68,184\n"
"R 180,205,38912\nM 148,76,188\nR 182,205,39168\nW 4\nM 144,80,192\nR 184,205,40960\nM 144,88,196\nR 186,205,41216\nM 148,84,192\nR 188,205,43008\n"
"M 148,92,196\nR 190,205,43264\ns_cbranch_vccz 1023f\ns_waitcnt vmcnt(6)\ns_barrier\n1023:\ns_add_u32 m0, s60, 73728\ns_add_u32 s61, s62, 3538944\n"
"buffer_load_dwordx4 v249, s[40:43], s61 offen lds\ns_add_u32 m0, s60, 81920\ns_add_u32 s61, s62, 4128768\n"
"buffer_load_dwordx4 v249, s[40:43], s61 offen lds\ns_add_u32 m0, s60, 90112\ns_add_u32 s61, s62, 4718592\n"
"buffer_load_dwordx4 v249, s[40:43], s61 offen lds\ns_cbranch_vccnz 1024f\ns_waitcnt vmcnt(9)\ns_barrier\n1024:\nW 4\nM 144,96,176\nR 192,205,45056\n"
"M 144,104,180\nR 194,205,45312\nM 148,100,176\nR 196,205,47104\nM 148,108,180\nR 198,205,47360\nW 4\nM 144,112,184\nR 176,244,24576\nM 144,120,188\n"
"R 178,244,25088\nM 148,116,184\nR 180,244,28672\nM 148,124,188\nR 182,244,29184\nW 4\nM 144,128,192\nR 184,245,24576\nM 144,136,196\nR 186,245,25088\n"
"M 148,132,192\nR 188,245,28672\nM 148,140,196\nR 190,245,29184\ns_cbranch_vccz 1025f\ns_waitcnt vmcnt(6)\ns_barrier\n1025:\ns_add_u32 m0, s60, 0\n"
"s_add_u32 s61, s62, 0\nbuffer_load_dwordx4 v249, s[44:47], s61 offen lds\ns_nop 2\nv_cvt_pk_bf16_f32 v208, v144, v148\ns_add_u32 m0, s60, 8192\n"
"s_add_u32 s61, s62, 589824\nbuffer_load_dwordx4 v249, s[44:47], s61 offen lds\nv_cvt_pk_bf16_f32 v209, v145, v149\nv_cvt_pk_bf16_f32 v210, v146, v150\n"
"s_add_u32 m0, s60, 16384\ns_add_u32 s61, s62, 1179648\nbuffer_load_dwordx4 v249, s[44:47], s61 offen lds\nv_cvt_pk_bf16_f32 v211, v147, v151\n"
"s_mov_b32 s74, 5767168\nbuffer_store_dwordx4 v[208:211], v200, s[52:55], s74 offen sc1\ns_cbranch_vccnz 1026f\ns_waitcnt vmcnt(10)\ns_barrier\n1026:\n"
"W 4\nZ 144,176,0\nR 192,244,32768\nM 144,180,8\nR 194,244,33280\nZ 148,176,4\nR 196,244,36864\nM 148,180,12\nR 198,244,37376\nW 4\nZ 152,184,0\n"
"R 176,245,32768\nM 152,188,8\nR 178,245,33280\nZ 156,184,4\nR 180,245,36864\nM 156,188,12\nR 182,245,37376\nW 4\nM 144,192,16\nR 184,244,40960\n"
"M 144,196,24\nR 186,244,41472\nM 148,192,20\nR 188,244,45056\nM 148,196,28\nR 190,244,45568\nW 4\nM 152,176,16\nR 192,245,40960\nM 152,180,24\n"
"R 194,245,41472\nM 156,176,20\nR 196,245,45056\nM 156,180,28\nR 198,245,45568\nW 4\nM 144,184,32\nR 176,244,49152\nM 144,188,40\nR 178,244,49664\n"
"M 148,184,36\nR 180,244,53248\nM 148,188,44\nR 182,244,53760\nW 4\nM 152,192,32\nR 184,245,49152\nM 152,196,40\nR 186,245,49664\nM 156,192,36\n"
"R 188,245,53248\nM 156,196,44\nR 190,245,53760\ns_cbranch_vccz 1027f\ns_waitcnt vmcnt(0)\ns_barrier\ns_cmp_lg_u32 s71, 4\ns_cbranch_scc1 999f\n"
"s_mov_b64 exec, 1\nv_mov_b32 v254, s72\nbuffer_store_dword v254, off, s[52:55], s73 sc1\ns_mov_b64 exec, -1\n999:\n1027:\ns_add_u32 m0, s60, 98304\n"
"s_add_u32 s61, s62, 1769472\nbuffer_load_dwordx4 v249, s[44:47], s61 offen lds\ns_and_b32 s69, s84, 7\ns_lshl_b32 s69, s69, 1\ns_lshr_b32 s74, s84, 7\n"
"s_add_u32 s69, s69, s74\ns_mul_i32 s66, s69, 147456\ns_add_u32 s66, s66, 1114112\ns_bfe_u32 s69, s84, 0x10006\ns_mul_i32 s65, s69, 81920\n"
"s_add_u32 s65, s65, s66\ns_sub_u32 s65, s65, 65536\ns_bfe_u32 s74, s84, 0x20004\ns_lshl_b32 s74, s74, 12\ns_lshl_b32 s75, s71, 9\n"
"s_add_u32 s74, s74, s75\nv_lshrrev_b32 v226, 1, v224\nv_and_b32 v226, 0xf8, v226\nv_add_u32 v226, s74, v226\nv_and_b32 v225, 0x200, v224\n"
"v_lshlrev_b32 v225, 6, v225\nv_add_u32 v225, v225, v226\nv_lshrrev_b32 v208, 4, v224\nv_and_b32 v209, 31, v208\nv_lshrrev_b32 v210, 5, v208\n"
"v_lshlrev_b32 v227, 13, v210\nv_lshl_add_u32 v227, v209, 3, v227\ns_add_u32 m0, s60, 106496\ns_add_u32 s61, s62, 2359296\n"
"buffer_load_dwordx4 v249, s[44:47], s61 offen lds\ns_lshl_b32 s74, s69, 12\ns_add_u32 s74, s74, 122880\nv_add_u32 v227, s74, v227\n"
"v_lshlrev_b32 v228, 3, v209\ns_lshl_b32 s74, s69, 14\ns_sub_u32 s74, 139264, s74\nv_add_u32 v228, s74, v228\nv_mul_u32_u24 v229, 288, v210\n"
"v_lshl_add_u32 v229, v209, 4, v229\nv_add_u32 v229, 143360, v229\nv_and_b32 v211, 1, v209\ns_add_u32 m0, s60, 114688\ns_add_u32 s61, s62, 2949120\n"
"buffer_load_dwordx4 v249, s[44:47], s61 offen lds\nv_lshrrev_b32 v212, 1, v209\nv_lshl_add_u32 v211, v211, 4, v212\ns_bfe_u32 s74, s84, 0x20004\n"
"s_lshl_b32 s74, s74, 9\ns_lshl_b32 s75, s71, 6\ns_add_u32 s74, s74, s75\nv_add_u32 v211, s74, v211\nv_lshlrev_b32 v235, 15, v210\n"
"v_lshl_add_u32 v235, v211, 4, v235\nv_lshlrev_b32 v236, 3, v211\ns_and_b32 s74, s84, 7\ns_lshl_b32 s74, s74, 2\ns_lshr_b32 s75, s84, 6\n"
"s_add_u32 s74, s74, s75\ns_lshl_b32 s75, s74, 16\nv_add_u32 v235, s75, v235\ns_lshr_b32 s74, s74, 1\ns_lshl_b32 s74, s74, 14\ns_lshl_b32 s75, s69, 2\n"
"s_add_u32 s74, s74, s75\nv_add_u32 v236, s74, v236\ns_cbranch_vccnz 1028f\ns_waitcnt vmcnt(3)\ns_barrier\n1028:\nW 4\nM 144,176,48\nR 192,244,57344\n"
"M 144,180,56\nR 194,244,57856\nM 148,176,52\nR 196,244,61440\nM 148,180,60\nR 198,244,61952\nW 4\nM 152,184,48\nR 176,245,57344\nM 152,188,56\n"
"R 178,245,57856\nM 156,184,52\nR 180,245,61440\nM 156,188,60\nR 182,245,61952\nW 4\nM 144,192,64\nR 184,246,4096\nM 144,196,72\nR 186,246,4608\n"
"M 148,192,68\nR 188,246,8192\nM 148,196,76\nR 190,246,8704\nW 4\nM 152,176,64\nR 192,247,4096\nM 152,180,72\nR 194,247,4608\nM 156,176,68\n"
"R 196,247,8192\nM 156,180,76\nR 198,247,8704\nW 4\nM 144,184,80\nR 176,246,12288\nM 144,188,88\nR 178,246,12800\nM 148,184,84\nR 180,246,16384\n"
"M 148,188,92\nR 182,246,16896\nW 4\nM 152,192,80\nR 184,247,12288\nM 152,196,88\nR 186,247,12800\nM 156,192,84\nR 188,247,16384\nM 156,196,92\n"
"R 190,247,16896\ns_cbranch_vccz 1029f\ns_waitcnt vmcnt(5)\ns_barrier\n1029:\ns_add_u32 m0, s60, 24576\ns_add_u32 s61, s62, 3538944\n"
"buffer_load_dwordx4 v249, s[44:47], s61 offen lds\ns_and_b32 s80, s84, 7\ns_lshl_b32 s80, s80, 1\ns_lshr_b32 s74, s84, 7\ns_add_u32 s80, s80, s74\n"
"s_bfe_u32 s81, s84, 0x30004\nv_lshrrev_b32 v211, 3, v208\ns_lshl_b32 s74, s71, 3\nv_add_u32 v212, s74, v211\nv_mul_u32_u24 v230, 0x480, v212\n"
"v_and_b32 v211, 6, v211\nv_and_b32 v212, 7, v208\nv_xor_b32 v211, v211, v212\ns_add_u32 m0, s60, 32768\ns_add_u32 s61, s62, 4128768\n"
"buffer_load_dwordx4 v249, s[44:47], s61 offen lds\nv_lshl_add_u32 v230, v211, 4, v230\ns_lshl_b32 s74, s81, 7\nv_add_u32 v230, s74, v230\n"
"v_mul_u32_u24 v231, 0x480, v208\ns_lshl_b32 s74, s81, 4\ns_add_u32 s74, s74, 1024\nv_add_u32 v231, s74, v231\ns_mov_b64 s[48:49], s[82:83]\n"
"s_mov_b32 s50, 663552\ns_mov_b64 s[56:57], s[86:87]\ns_mov_b32 s58, 5308416\nv_lshlrev_b32 v234, 2, v224\ns_and_b32 s74, s84, 1\n"
"s_lshl_b32 s74, s74, 3\ns_bfe_u32 s64, s84, 0x30004\ns_or_b32 s74, s74, s64\ns_and_b32 s75, s84, 6\ns_lshr_b32 s64, s84, 7\ns_add_u32 s75, s75, s64\n"
"s_mul_i32 s67, s75, 663552\ns_and_b32 s64, s74, 7\ns_lshl_b32 s64, s64, 3\ns_add_u32 s64, s64, s71\ns_mul_i32 s64, s64, 10368\ns_add_u32 s67, s67, s64\n"
"s_lshl_b32 s90, s64, 3\ns_mul_i32 s64, s75, 1152\ns_add_u32 s90, s90, s64\ns_add_u32 s64, s76, -4\ns_add_u32 m0, s60, 40960\n"
"s_add_u32 s61, s62, 4718592\nbuffer_load_dwordx4 v249, s[44:47], s61 offen lds\nv_lshrrev_b32 v239, 2, v208\nv_mul_u32_u24 v239, 0x2400, v239\n"
"v_and_b32 v211, 3, v208\nv_add_u32 v211, s64, v211\nv_ashrrev_i32 v212, 31, v211\nv_and_b32 v212, 9, v212\nv_add_u32 v211, v211, v212\n"
"v_lshl_add_u32 v239, v211, 7, v239\ns_cbranch_vccnz 1030f\ns_waitcnt vmcnt(8)\ns_barrier\n1030:\nW 4\nM 144,176,96\nR 192,246,20480\nM 144,180,104\n"
"R 194,246,20992\nM 148,176,100\nR 196,246,24576\nM 148,180,108\nR 198,246,25088\nW 4\nM 152,184,96\nR 176,247,20480\nM 152,188,104\nR 178,247,20992\n"
"M 156,184,100\nR 180,247,24576\nM 156,188,108\nR 182,247,25088\nW 4\nM 144,192,112\nR 184,246,28672\nM 144,196,120\nR 186,246,29184\nM 148,192,116\n"
"R 188,246,32768\nM 148,196,124\nR 190,246,33280\nW 4\nM 152,176,112\nR 192,247,28672\nM 152,180,120\nR 194,247,29184\nM 156,176,116\nR 196,247,32768\n"
"M 156,180,124\nR 198,247,33280\nW 4\nM 144,184,128\nR 176,244,0\nM 144,188,136\nR 178,244,512\nM 148,184,132\nR 180,244,4096\nM 148,188,140\n"
"R 182,244,4608\nW 4\nM 152,192,128\nR 184,245,0\nM 152,196,136\nR 186,245,512\nM 156,192,132\nR 188,245,4096\nM 156,196,140\nR 190,245,4608\n"
"s_cbranch_vccz 1031f\ns_waitcnt vmcnt(4)\ns_waitcnt vmcnt(0)\ns_barrier\ns_cmp_lg_u32 s71, 5\ns_cbranch_scc1 998f\nv_lshlrev_b32 v204, 2, v224\n"
"s_mov_b32 s74, 4096\ns_mov_b32 s75, 8192\ns_mov_b32 s85, 53248\ns_mov_b32 s91, 57344\nbuffer_load_dword v200, v204, s[52:55], s74 offen sc1\n"
"buffer_load_dword v201, v204, s[52:55], s75 offen sc1\n998:\n1031:\ns_nop 5\nv_mul_f32 v240, v144, v144\nv_mul_f32 v241, v145, v145\n"
"v_mul_f32 v242, v146, v146\nv_mul_f32 v243, v147, v147\nv_fma_f32 v240, v240, s70, v248\nv_fma_f32 v241, v241, s70, v248\n"
"v_fma_f32 v242, v242, s70, v248\nv_fma_f32 v243, v243, s70, v248\nv_mul_f32 v240, v240, v144\nv_mul_f32 v241, v241, v145\nv_mul_f32 v242, v242, v146\n"
"v_mul_f32 v243, v243, v147\nE 240\nE 241\nE 242\nE 243\nv_add_f32 v240, 1.0, v240\nv_add_f32 v241, 1.0, v241\nv_add_f32 v242, 1.0, v242\n"
"v_add_f32 v243, 1.0, v243\nv_rcp_f32 v240, v240\nv_rcp_f32 v241, v241\nv_rcp_f32 v242, v242\ns_mov_b64 s[92:93], s[40:41]\n"
"s_mov_b64 s[94:95], s[42:43]\ns_mov_b64 s[96:97], s[44:45]\ns_mov_b64 s[98:99], s[46:47]\ns_and_b32 s62, s84, 7\ns_lshl_b32 s62, s62, 2\n"
"s_lshr_b32 s63, s84, 6\ns_add_u32 s62, s62, s63\ns_lshl_b32 s47, s71, 10\ns_mul_i32 s43, s62, 73728\ns_add_u32 s43, s43, s47\n"
"s_add_u32 s43, s43, 5771264\ns_lshr_b32 s63, s62, 1\ns_mul_i32 s63, s63, 9\ns_and_b32 s62, s62, 1\ns_lshl_b32 s62, s62, 2\ns_add_u32 s62, s62, s63\n"
"s_lshr_b32 s63, s71, 2\ns_add_u32 s62, s62, s63\ns_lshl_b32 s62, s62, 14\ns_and_b32 s63, s47, 0xc00\ns_add_u32 s62, s62, s63\n"
"s_add_u32 s40, s62, 3407872\ns_add_u32 s41, s40, 32768\ns_add_u32 s62, s40, 65536\ns_sub_u32 s63, s43, 8192\ns_cmp_lt_u32 s71, 4\n"
"s_cselect_b32 s42, s62, s63\ns_movk_i32 s62, 0x4800\ns_cselect_b32 s45, 0x1000, s62\ns_cmp_lt_u32 s71, 6\ns_cselect_b32 s62, 0x2000, 0\n"
"s_add_u32 s44, s43, s62\ns_add_u32 s46, s47, s62\ns_add_u32 s46, s46, 24576\ns_cbranch_vccnz 1032f\ns_waitcnt vmcnt(4)\ns_barrier\n1032:\nW 4\n"
"Z 160,176,0\nR 192,244,8192\nM 160,180,8\nR 194,244,8704\nZ 164,176,4\nR 196,244,12288\nM 164,180,12\nR 198,244,12800\nW 4\nZ 168,184,0\n"
"R 176,245,8192\nM 168,188,8\nR 178,245,8704\nZ 172,184,4\nR 180,245,12288\nM 172,188,12\nR 182,245,12800\nW 4\nM 160,192,16\nR 184,244,16384\n"
"M 160,196,24\nR 186,244,16896\nM 164,192,20\nR 188,244,20480\nM 164,196,28\nR 190,244,20992\nW 4\nM 168,176,16\nR 192,245,16384\nM 168,180,24\n"
"R 194,245,16896\nM 172,176,20\nR 196,245,20480\nM 172,180,28\nR 198,245,20992\nW 4\nM 160,184,32\nR 176,246,36864\nM 160,188,40\nR 178,246,37376\n"
"M 164,184,36\nR 180,246,40960\nM 164,188,44\nR 182,246,41472\nW 4\nM 168,192,32\nR 184,247,36864\nM 168,196,40\nR 186,247,37376\nM 172,192,36\n"
"R 188,247,40960\nM 172,196,44\nR 190,247,41472\ns_cbranch_vccz 1033f\ns_barrier\ns_cmp_lg_u32 s71, 5\ns_cbranch_scc1 997f\ns_mov_b32 s61, 0x40000\n"
"996:\ns_waitcnt vmcnt(0)\nv_xor_b32 v200, s72, v200\nv_xor_b32 v201, s72, v201\nv_or_b32 v200, v200, v201\nv_cmp_ne_u32 s[62:63], 0, v200\n"
"s_sub_u32 s61, s61, 1\ns_cmp_eq_u32 s61, 0\ns_cbranch_scc1 995f\ns_cmp_eq_u64 s[62:63], 0\ns_cbranch_scc1 995f\ns_sleep 1\n"
"buffer_load_dword v200, v204, s[52:55], s74 offen sc1\nbuffer_load_dword v201, v204, s[52:55], s75 offen sc1\ns_branch 996b\n995:\nbuffer_inv sc1\n"
"997:\n1033:\nv_rcp_f32 v243, v243\nv_mul_f32 v144, v240, v144\nv_mul_f32 v145, v241, v145\nv_mul_f32 v146, v242, v146\nv_mul_f32 v147, v243, v147\n"
"v_mul_f32 v240, v148, v148\nv_mul_f32 v241, v149, v149\nv_mul_f32 v242, v150, v150\nv_mul_f32 v243, v151, v151\nv_fma_f32 v240, v240, s70, v248\n"
"v_fma_f32 v241, v241, s70, v248\nv_fma_f32 v242, v242, s70, v248\nv_fma_f32 v243, v243, s70, v248\nv_mul_f32 v240, v240, v148\n"
"v_mul_f32 v241, v241, v149\nv_mul_f32 v242, v242, v150\nv_mul_f32 v243, v243, v151\nE 240\nE 241\nE 242\nE 243\nv_add_f32 v240, 1.0, v240\n"
"v_add_f32 v241, 1.0, v241\ns_cbranch_vccnz 1034f\ns_waitcnt vmcnt(1)\ns_barrier\n1034:\nW 4\nM 160,176,48\nR 192,246,45056\nM 160,180,56\n"
"R 194,246,45568\nM 164,176,52\nR 196,246,49152\nM 164,180,60\nR 198,246,49664\nW 4\nM 168,184,48\nR 176,247,45056\nM 168,188,56\nR 178,247,45568\n"
"M 172,184,52\nR 180,247,49152\nM 172,188,60\nR 182,247,49664\nW 4\nM 160,192,64\nR 184,246,53248\nM 160,196,72\nR 186,246,53760\nM 164,192,68\n"
"R 188,246,57344\nM 164,196,76\nR 190,246,57856\nW 4\nM 168,176,64\nR 192,247,53248\nM 168,180,72\nR 194,247,53760\nM 172,176,68\nR 196,247,57344\n"
"M 172,180,76\nR 198,247,57856\nW 4\nM 160,184,80\nR 176,244,24576\nM 160,188,88\nR 178,244,25088\nM 164,184,84\nR 180,244,28672\nM 164,188,92\n"
"R 182,244,29184\nW 4\nM 168,192,80\nR 184,245,24576\nM 168,196,88\nR 186,245,25088\nM 172,192,84\nR 188,245,28672\nM 172,196,92\nR 190,245,29184\n"
"s_cbranch_vccz 1035f\ns_waitcnt vmcnt(0)\ns_barrier\n1035:\nv_add_f32 v242, 1.0, v242\nv_add_f32 v243, 1.0, v243\nv_rcp_f32 v240, v240\n"
"v_rcp_f32 v241, v241\nv_rcp_f32 v242, v242\nv_rcp_f32 v243, v243\nv_mul_f32 v148, v240, v148\nv_mul_f32 v149, v241, v149\nv_mul_f32 v150, v242, v150\n"
"v_mul_f32 v151, v243, v151\nv_mul_f32 v240, v152, v152\nv_mul_f32 v241, v153, v153\nv_mul_f32 v242, v154, v154\nv_mul_f32 v243, v155, v155\n"
"v_fma_f32 v240, v240, s70, v248\nv_fma_f32 v241, v241, s70, v248\nv_fma_f32 v242, v242, s70, v248\nv_fma_f32 v243, v243, s70, v248\n"
"v_mul_f32 v240, v240, v152\nv_mul_f32 v241, v241, v153\nv_mul_f32 v242, v242, v154\nv_mul_f32 v243, v243, v155\nv_mov_b32 v208, 0\n"
"s_mov_b32 exec_lo, 0x140000\ns_mov_b32 exec_hi, 0x140000\nv_mov_b32 v208, 0x3f803f80\ns_mov_b64 exec, -1\nv_mov_b32 v209, v208\n"
"v_mov_b64 v[210:211], v[208:209]\nv_mov_b64 v[212:213], v[208:209]\nv_mov_b64 v[214:215], v[208:209]\nv_mov_b64 v[216:217], v[208:209]\n"
"v_mov_b64 v[218:219], v[208:209]\nv_mov_b64 v[220:221], v[208:209]\nv_mov_b64 v[222:223], v[208:209]\nv_mov_b32 v205, 0xdddddddd\n"
"v_mov_b64 v[48:49], 0\nv_mov_b64 v[50:51], 0\nv_mov_b64 v[52:53], 0\nv_mov_b64 v[54:55], 0\nv_mov_b64 v[56:57], 0\nv_mov_b64 v[58:59], 0\n"
"v_mov_b64 v[60:61], 0\nv_mov_b64 v[62:63], 0\nv_mov_b64 v[64:65], 0\nv_mov_b64 v[66:67], 0\nv_mov_b64 v[68:69], 0\nv_mov_b64 v[70:71], 0\n"
"v_mov_b64 v[72:73], 0\nv_mov_b64 v[74:75], 0\nv_mov_b64 v[76:77], 0\nv_mov_b64 v[78:79], 0\nv_mov_b64 v[80:81], 0\nv_mov_b64 v[82:83], 0\n"
"v_mov_b64 v[84:85], 0\nv_mov_b64 v[86:87], 0\nv_mov_b64 v[88:89], 0\nv_mov_b64 v[90:91], 0\nv_mov_b64 v[92:93], 0\nv_mov_b64 v[94:95], 0\n"
"s_add_u32 s73, s73, 12288\ns_cbranch_vccnz 1036f\ns_waitcnt vmcnt(0)\ns_barrier\n1036:\nW 4\nM 160,176,96\nR 192,244,32768\nM 160,180,104\n"
"R 194,244,33280\nM 164,176,100\nR 196,244,36864\nM 164,180,108\nR 198,244,37376\nW 4\nM 168,184,96\nR 176,245,32768\nM 168,188,104\nR 178,245,33280\n"
"M 172,184,100\nR 180,245,36864\nM 172,188,108\nR 182,245,37376\nW 4\nM 160,192,112\nR 184,244,40960\nM 160,196,120\nR 186,244,41472\nM 164,192,116\n"
"R 188,244,45056\nM 164,196,124\nR 190,244,45568\nW 4\nM 168,176,112\nR 192,245,40960\nM 168,180,120\nR 194,245,41472\nM 172,176,116\nR 196,245,45056\n"
"M 172,180,124\nR 198,245,45568\nW 4\nM 160,184,128\nM 160,188,136\nM 164,184,132\nM 164,188,140\nW 0\nM 168,192,128\nM 168,196,136\nM 172,192,132\n"
"M 172,196,140\ns_cbranch_vccz 1037f\ns_barrier\n1037:\ns_nop 6\ns_add_u32 m0, s60, 122880\ns_nop 0\nbuffer_load_dwordx4 v224, s[52:55], s40 offen lds\n"
"s_add_u32 m0, s60, 131072\ns_nop 0\nbuffer_load_dwordx4 v224, s[52:55], s41 offen lds\ns_add_u32 m0, s60, 139264\ns_nop 0\n"
"buffer_load_dwordx4 v224, s[52:55], s42 offen lds\ns_add_u32 m0, s60, 147456\ns_nop 0\nbuffer_load_dwordx4 v224, s[52:55], s43 offen lds\n"
"s_add_u32 m0, s46, 122880\ns_nop 0\nbuffer_load_dwordx4 v224, s[52:55], s44 offen lds\ns_add_u32 s74, s65, 16384\n"
"buffer_load_dwordx2 v[128:129], v225, s[52:55], s65 offen offset:0\nbuffer_load_dwordx2 v[130:131], v225, s[52:55], s74 offen offset:0\n"
"buffer_load_dwordx2 v[136:137], v226, s[52:55], s66 offen offset:0\nbuffer_load_dwordx2 v[132:133], v225, s[52:55], s65 offen offset:256\n"
"buffer_load_dwordx2 v[134:135], v225, s[52:55], s74 offen offset:256\nbuffer_load_dwordx2 v[138:139], v226, s[52:55], s66 offen offset:256\n"
"s_cbranch_vccnz 1038f\ns_barrier\n1038:\ns_branch 73f\n71:\nR 176,244,0\nR 178,244,512\nR 180,244,4096\nR 182,244,4608\nR 184,245,0\nR 186,245,512\n"
"R 188,245,4096\nR 190,245,4608\ns_cbranch_vccz 2000f\ns_add_u32 m0, s85, 113920\ns_nop 0\nbuffer_load_dwordx4 v216, s[56:59], s90 offen offset:768 lds\n"
"s_add_u32 m0, s85, 114944\ns_nop 0\nbuffer_load_dwordx4 v217, s[56:59], s90 offen offset:768 lds\ns_add_u32 m0, s91, 113920\ns_nop 0\n"
"buffer_load_dwordx4 v216, s[56:59], s92 offen offset:768 lds\ns_add_u32 m0, s91, 114944\ns_nop 0\n"
"buffer_load_dwordx4 v217, s[56:59], s92 offen offset:768 lds\ns_add_u32 m0, s85, 130176\ns_nop 0\n"
"buffer_load_dwordx4 v216, s[56:59], s90 offen offset:896 lds\ns_add_u32 m0, s85, 131200\ns_nop 0\n"
"buffer_load_dwordx4 v217, s[56:59], s90 offen offset:896 lds\n2000:\nW 4\nZ 144,176,0\nR 192,244,8192\nM 144,180,8\nR 194,244,8704\nZ 148,176,4\n"
"R 196,244,12288\nM 148,180,12\nR 198,244,12800\nW 4\nZ 152,184,0\nR 176,245,8192\nM 152,188,8\nR 178,245,8704\nZ 156,184,4\nR 180,245,12288\n"
"M 156,188,12\nR 182,245,12800\nW 4\nM 144,192,16\nR 184,244,16384\nM 144,196,24\nR 186,244,16896\nM 148,192,20\nR 188,244,20480\nM 148,196,28\n"
"R 190,244,20992\nW 4\nM 152,176,16\nR 192,245,16384\nM 152,180,24\nR 194,245,16896\nM 156,176,20\nR 196,245,20480\nM 156,180,28\nR 198,245,20992\nW 4\n"
"M 144,184,32\nM 144,188,40\nM 148,184,36\nM 148,188,44\nW 0\nM 152,192,32\nM 152,196,40\nM 156,192,36\nM 156,196,44\nv_lshlrev_b32 v224, 4, v240\n"
"s_lshl_b32 s74, s71, 11\nv_add_u32 v251, s74, v224\nv_lshrrev_b32 v241, 4, v240\nv_lshlrev_b32 v253, 14, v241\nv_and_b32 v242, 0xf0, v224\n"
"v_add_u32 v253, v253, v242\ns_lshr_b32 s74, s71, 2\ns_lshl_b32 s74, s74, 17\ns_and_b32 s75, s71, 3\ns_lshl_b32 s75, s75, 8\ns_add_u32 s74, s74, s75\n"
"s_lshl_b32 s75, s80, 10\ns_add_u32 s74, s74, s75\nv_add_u32 v253, s74, v253\ns_cbranch_vccnz 2001f\ns_add_u32 m0, s85, 130176\ns_nop 0\n"
"buffer_load_dwordx4 v216, s[56:59], s90 offen offset:896 lds\ns_add_u32 m0, s85, 131200\ns_nop 0\n"
"buffer_load_dwordx4 v217, s[56:59], s90 offen offset:896 lds\ns_add_u32 m0, s85, 146432\ns_nop 0\n"
"buffer_load_dwordx4 v216, s[56:59], s90 offen offset:1024 lds\ns_add_u32 m0, s85, 147456\ns_nop 0\n"
"buffer_load_dwordx4 v217, s[56:59], s90 offen offset:1024 lds\ns_add_u32 m0, s91, 146432\ns_nop 0\n"
"buffer_load_dwordx4 v216, s[56:59], s92 offen offset:1024 lds\ns_add_u32 m0, s91, 147456\ns_nop 0\n"
"buffer_load_dwordx4 v217, s[56:59], s92 offen offset:1024 lds\n2001:\ns_cbranch_vccnz 2002f\ns_waitcnt vmcnt(6)\n2002:\ns_cbranch_vccz 2003f\n"
"s_waitcnt vmcnt(6)\n2003:\nv_add_u32 v218, 0xffff4000, v218\nv_add_u32 v219, 0xffff4000, v219\ns_barrier\nds_read_b128 v[48:51], v218 offset:0\n"
"ds_read_b128 v[52:55], v218 offset:2048\nds_read_b128 v[56:59], v219 offset:0\nds_read_b128 v[60:63], v219 offset:2048\n"
"ds_read_b128 v[64:67], v218 offset:16384\nds_read_b128 v[68:71], v218 offset:18432\nds_read_b128 v[72:75], v219 offset:16384\n"
"ds_read_b128 v[76:79], v219 offset:18432\nds_read_b128 v[80:83], v218 offset:32768\nds_read_b128 v[84:87], v218 offset:34816\n"
"ds_read_b128 v[88:91], v219 offset:32768\nds_read_b128 v[92:95], v219 offset:34816\nW 0\nR 176,244,24576\nR 178,244,25088\nR 180,244,28672\n"
"R 182,244,29184\nR 184,245,24576\nR 186,245,25088\nR 188,245,28672\nR 190,245,29184\ns_barrier\ns_cbranch_vccz 2004f\ns_add_u32 m0, s60, 65536\n"
"s_add_u32 s61, s93, 1769472\nbuffer_load_dwordx4 v250, s[48:51], s61 offen lds\ns_sub_u32 m0, m0, 4096\ns_nop 0\n"
"buffer_load_dwordx4 v221, s[48:51], s61 offen lds\ns_add_u32 m0, s60, 73728\ns_add_u32 s61, s94, 0\nbuffer_load_dwordx4 v250, s[48:51], s61 offen lds\n"
"s_sub_u32 m0, m0, 4096\ns_nop 0\nbuffer_load_dwordx4 v221, s[48:51], s61 offen lds\ns_add_u32 m0, s60, 81920\ns_add_u32 s61, s94, 221184\n"
"buffer_load_dwordx4 v250, s[48:51], s61 offen lds\ns_sub_u32 m0, m0, 4096\ns_nop 0\nbuffer_load_dwordx4 v221, s[48:51], s61 offen lds\n2004:\nW 4\n"
"M 144,176,48\nR 192,244,32768\nM 144,180,56\nR 194,244,33280\nM 148,176,52\nR 196,244,36864\nM 148,180,60\nR 198,244,37376\nW 4\nM 152,184,48\n"
"R 176,245,32768\nM 152,188,56\nR 178,245,33280\nM 156,184,52\nR 180,245,36864\nM 156,188,60\nR 182,245,37376\nW 4\nM 144,192,64\nR 184,244,40960\n"
"M 144,196,72\nR 186,244,41472\nM 148,192,68\nR 188,244,45056\nM 148,196,76\nR 190,244,45568\nW 4\nM 152,176,64\nR 192,245,40960\nM 152,180,72\n"
"R 194,245,41472\nM 156,176,68\nR 196,245,45056\nM 156,180,76\nR 198,245,45568\nW 4\nM 144,184,80\nM 144,188,88\nM 148,184,84\nM 148,188,92\nW 0\n"
"M 152,192,80\nM 152,196,88\nM 156,192,84\nM 156,196,92\nv_lshrrev_b32 v241, 2, v240\ns_lshl_b32 s74, s71, 4\nv_add_u32 v241, s74, v241\n"
"v_mul_u32_u24 v207, 0xd80, v241\nv_lshrrev_b32 v241, 4, v240\nv_and_b32 v242, 2, v241\nv_and_b32 v243, 3, v240\nv_xor_b32 v242, v242, v243\n"
"v_lshl_add_u32 v207, v242, 4, v207\nv_bfe_u32 v242, v240, 2, 2\nv_lshl_add_u32 v242, v241, 3, v242\nv_lshlrev_b32 v204, 6, v242\n"
"v_lshl_add_u32 v204, v243, 3, v204\nv_and_b32 v241, 1, v241\ns_lshr_b32 s74, s71, 2\nv_xor_b32 v241, s74, v241\nv_lshl_add_u32 v204, v241, 5, v204\n"
"v_add_u32 v205, 61440, v204\nv_mul_u32_u24 v237, 0xd80, v240\ns_mul_i32 s74, s81, 3456\ns_add_u32 s74, s74, s64\ns_mov_b64 exec, 0x1ff\n"
"buffer_load_dword v234, v237, s[48:51], s74 offen\ns_mov_b64 exec, -1\ns_cbranch_vccnz 2005f\ns_add_u32 m0, s60, 90112\ns_add_u32 s61, s94, 442368\n"
"buffer_load_dwordx4 v250, s[48:51], s61 offen lds\ns_add_u32 m0, m0, 4096\ns_add_u32 s61, s61, 110592\n"
"buffer_load_dwordx4 v250, s[48:51], s61 offen lds\ns_add_u32 m0, s60, 0\ns_add_u32 s61, s94, 663552\nbuffer_load_dwordx4 v250, s[48:51], s61 offen lds\n"
"s_add_u32 m0, m0, 4096\ns_add_u32 s61, s61, 110592\nbuffer_load_dwordx4 v250, s[48:51], s61 offen lds\ns_add_u32 m0, s60, 8192\n"
"s_add_u32 s61, s94, 884736\nbuffer_load_dwordx4 v250, s[48:51], s61 offen lds\ns_add_u32 m0, m0, 4096\ns_add_u32 s61, s61, 110592\n"
"buffer_load_dwordx4 v250, s[48:51], s61 offen lds\ns_add_u32 m0, s60, 16384\ns_add_u32 s61, s94, 1105920\n"
"buffer_load_dwordx4 v250, s[48:51], s61 offen lds\ns_add_u32 m0, m0, 4096\ns_add_u32 s61, s61, 110592\n"
"buffer_load_dwordx4 v250, s[48:51], s61 offen lds\n2005:\ns_cbranch_vccnz 2006f\ns_waitcnt vmcnt(9)\n2006:\ns_cbranch_vccz 2007f\ns_waitcnt vmcnt(1)\n"
"2007:\nv_add_u32 v218, 0xc000, v218\nv_add_u32 v219, 0xc000, v219\ns_barrier\nds_read_b128 v[96:99], v218 offset:0\n"
"ds_read_b128 v[100:103], v218 offset:2048\nds_read_b128 v[104:107], v219 offset:0\nds_read_b128 v[108:111], v219 offset:2048\n"
"ds_read_b128 v[112:115], v218 offset:16384\nds_read_b128 v[116:119], v218 offset:18432\nds_read_b128 v[120:123], v219 offset:16384\n"
"ds_read_b128 v[124:127], v219 offset:18432\nds_read_b128 v[128:131], v218 offset:32768\nds_read_b128 v[132:135], v218 offset:34816\n"
"ds_read_b128 v[136:139], v219 offset:32768\nds_read_b128 v[140:143], v219 offset:34816\nW 0\nR 176,244,49152\nR 178,244,49664\nR 180,244,53248\n"
"R 182,244,53760\nR 184,245,49152\nR 186,245,49664\nR 188,245,53248\nR 190,245,53760\ns_barrier\ns_cbranch_vccz 2008f\ns_add_u32 m0, s60, 98304\n"
"s_add_u32 s61, s94, 1327104\nbuffer_load_dwordx4 v250, s[48:51], s61 offen lds\ns_sub_u32 m0, m0, 4096\ns_nop 0\n"
"buffer_load_dwordx4 v221, s[48:51], s61 offen lds\ns_add_u32 m0, s60, 106496\ns_add_u32 s61, s94, 1548288\n"
"buffer_load_dwordx4 v250, s[48:51], s61 offen lds\ns_sub_u32 m0, m0, 4096\ns_nop 0\nbuffer_load_dwordx4 v221, s[48:51], s61 offen lds\n"
"s_add_u32 m0, s60, 114688\ns_add_u32 s61, s94, 1769472\nbuffer_load_dwordx4 v250, s[48:51], s61 offen lds\ns_sub_u32 m0, m0, 4096\ns_nop 0\n"
"buffer_load_dwordx4 v221, s[48:51], s61 offen lds\n2008:\nW 4\nM 144,176,96\nR 192,244,57344\nM 144,180,104\nR 194,244,57856\nM 148,176,100\n"
"R 196,244,61440\nM 148,180,108\nR 198,244,61952\nW 4\nM 152,184,96\nR 176,245,57344\nM 152,188,104\nR 178,245,57856\nM 156,184,100\nR 180,245,61440\n"
"M 156,188,108\nR 182,245,61952\nW 4\nM 144,192,112\nR 184,246,4096\nM 144,196,120\nR 186,246,4608\nM 148,192,116\nR 188,246,8192\nM 148,196,124\n"
"R 190,246,8704\nW 4\nM 152,176,112\nR 192,247,4096\nM 152,180,120\nR 194,247,4608\nM 156,176,116\nR 196,247,8192\nM 156,180,124\nR 198,247,8704\nW 4\n"
"M 144,184,128\nR 176,246,12288\nM 144,188,136\nR 178,246,12800\nM 148,184,132\nR 180,246,16384\nM 148,188,140\nR 182,246,16896\nW 4\nM 152,192,128\n"
"R 184,247,12288\nM 152,196,136\nR 186,247,12800\nM 156,192,132\nR 188,247,16384\nM 156,196,140\nR 190,247,16896\ns_cbranch_vccz 2009f\n"
"s_waitcnt vmcnt(7)\ns_barrier\n2009:\ns_add_u32 m0, s60, 24576\ns_add_u32 s61, s95, 0\nbuffer_load_dwordx4 v250, s[48:51], s61 offen lds\ns_nop 2\n"
"v_cvt_pk_bf16_f32 v208, v144, v145\nv_cvt_pk_bf16_f32 v209, v146, v147\nv_cvt_pk_bf16_f32 v210, v148, v149\ns_add_u32 m0, s60, 32768\n"
"s_add_u32 s61, s95, 221184\nbuffer_load_dwordx4 v250, s[48:51], s61 offen lds\nv_cvt_pk_bf16_f32 v211, v150, v151\nv_cvt_pk_bf16_f32 v212, v152, v153\n"
"v_cvt_pk_bf16_f32 v213, v154, v155\ns_add_u32 m0, s60, 40960\ns_add_u32 s61, s95, 442368\nbuffer_load_dwordx4 v250, s[48:51], s61 offen lds\n"
"v_cvt_pk_bf16_f32 v214, v156, v157\nv_cvt_pk_bf16_f32 v215, v158, v159\ns_lshl_b32 s74, s93, 11\ns_add_u32 s74, s74, 1048576\n"
"s_add_u32 s75, s74, 65536\nbuffer_store_dwordx4 v[208:211], v253, s[52:55], s74 offen sc1\n"
"buffer_store_dwordx4 v[212:215], v253, s[52:55], s75 offen sc1\ns_cbranch_vccnz 2010f\ns_waitcnt vmcnt(7)\ns_barrier\n2010:\nW 4\nZ 144,176,0\n"
"R 192,246,20480\nM 144,180,8\nR 194,246,20992\nZ 148,176,4\nR 196,246,24576\nM 148,180,12\nR 198,246,25088\nW 4\nZ 152,184,0\nR 176,247,20480\n"
"M 152,188,8\nR 178,247,20992\nZ 156,184,4\nR 180,247,24576\nM 156,188,12\nR 182,247,25088\nW 4\nM 144,192,16\nR 184,246,28672\nM 144,196,24\n"
"R 186,246,29184\nM 148,192,20\nR 188,246,32768\nM 148,196,28\nR 190,246,33280\nW 4\nM 152,176,16\nR 192,247,28672\nM 152,180,24\nR 194,247,29184\n"
"M 156,176,20\nR 196,247,32768\nM 156,180,28\nR 198,247,33280\nW 4\nM 144,184,32\nR 176,244,0\nM 144,188,40\nR 178,244,512\nM 148,184,36\n"
"R 180,244,4096\nM 148,188,44\nR 182,244,4608\nW 4\nM 152,192,32\nR 184,245,0\nM 152,196,40\nR 186,245,512\nM 156,192,36\nR 188,245,4096\nM 156,196,44\n"
"R 190,245,4608\ns_cbranch_vccz 2011f\ns_waitcnt vmcnt(7)\ns_barrier\n2011:\ns_add_u32 m0, s60, 49152\ns_add_u32 s61, s95, 663552\n"
"buffer_load_dwordx4 v250, s[48:51], s61 offen lds\ns_add_u32 m0, s60, 57344\ns_add_u32 s61, s95, 884736\n"
"buffer_load_dwordx4 v250, s[48:51], s61 offen lds\ns_add_u32 m0, s60, 65536\ns_add_u32 s61, s95, 1105920\n"
"buffer_load_dwordx4 v250, s[48:51], s61 offen lds\ns_cbranch_vccnz 2012f\ns_waitcnt vmcnt(8)\ns_barrier\n2012:\nW 4\nM 144,176,48\nR 192,244,8192\n"
"M 144,180,56\nR 194,244,8704\nM 148,176,52\nR 196,244,12288\nM 148,180,60\nR 198,244,12800\nW 4\nM 152,184,48\nR 176,245,8192\nM 152,188,56\n"
"R 178,245,8704\nM 156,184,52\nR 180,245,12288\nM 156,188,60\nR 182,245,12800\nW 4\nM 144,192,64\nR 184,244,16384\nM 144,196,72\nR 186,244,16896\n"
"M 148,192,68\nR 188,244,20480\nM 148,196,76\nR 190,244,20992\nW 4\nM 152,176,64\nR 192,245,16384\nM 152,180,72\nR 194,245,16896\nM 156,176,68\n"
"R 196,245,20480\nM 156,180,76\nR 198,245,20992\nW 4\nM 144,184,80\nR 176,246,36864\nM 144,188,88\nR 178,246,37376\nM 148,184,84\nR 180,246,40960\n"
"M 148,188,92\nR 182,246,41472\nW 4\nM 152,192,80\nR 184,247,36864\nM 152,196,88\nR 186,247,37376\nM 156,192,84\nR 188,247,40960\nM 156,196,92\n"
"R 190,247,41472\ns_cbranch_vccz 2013f\ns_waitcnt vmcnt(6)\ns_barrier\n2013:\ns_add_u32 m0, s60, 73728\ns_add_u32 s61, s95, 1327104\n"
"buffer_load_dwordx4 v250, s[48:51], s61 offen lds\ns_add_u32 m0, s60, 81920\ns_add_u32 s61, s95, 1548288\n"
"buffer_load_dwordx4 v250, s[48:51], s61 offen lds\ns_add_u32 m0, s60, 90112\ns_add_u32 s61, s95, 1769472\n"
"buffer_load_dwordx4 v250, s[48:51], s61 offen lds\ns_cbranch_vccnz 2014f\ns_waitcnt vmcnt(9)\ns_barrier\n2014:\nW 4\nM 144,176,96\nR 192,246,45056\n"
"M 144,180,104\nR 194,246,45568\nM 148,176,100\nR 196,246,49152\nM 148,180,108\nR 198,246,49664\nW 4\nM 152,184,96\nR 176,247,45056\nM 152,188,104\n"
"R 178,247,45568\nM 156,184,100\nR 180,247,49152\nM 156,188,108\nR 182,247,49664\nW 4\nM 144,192,112\nR 184,246,53248\nM 144,196,120\nR 186,246,53760\n"
"M 148,192,116\nR 188,246,57344\nM 148,196,124\nR 190,246,57856\nW 4\nM 152,176,112\nR 192,247,53248\nM 152,180,120\nR 194,247,53760\nM 156,176,116\n"
"R 196,247,57344\nM 156,180,124\nR 198,247,57856\nW 4\nM 144,184,128\nR 176,244,24576\nM 144,188,136\nR 178,244,25088\nM 148,184,132\nR 180,244,28672\n"
"M 148,188,140\nR 182,244,29184\nW 4\nM 152,192,128\nR 184,245,24576\nM 152,196,136\nR 186,245,25088\nM 156,192,132\nR 188,245,28672\nM 156,196,140\n"
"R 190,245,29184\ns_cbranch_vccz 2015f\ns_waitcnt vmcnt(4)\ns_barrier\n2015:\ns_add_u32 m0, s60, 0\ns_add_u32 s61, s64, 0\n"
"buffer_load_dwordx4 v207, s[48:51], s61 offen lds\ns_nop 2\nv_cvt_pk_bf16_f32 v208, v144, v145\nv_cvt_pk_bf16_f32 v209, v146, v147\n"
"v_cvt_pk_bf16_f32 v210, v148, v149\ns_add_u32 m0, s60, 8192\ns_add_u32 s61, s64, 442368\nbuffer_load_dwordx4 v207, s[48:51], s61 offen lds\n"
"v_cvt_pk_bf16_f32 v211, v150, v151\nv_cvt_pk_bf16_f32 v212, v152, v153\nv_cvt_pk_bf16_f32 v213, v154, v155\ns_add_u32 m0, s60, 16384\n"
"s_add_u32 s61, s64, 884736\nbuffer_load_dwordx4 v207, s[48:51], s61 offen lds\nv_cvt_pk_bf16_f32 v214, v156, v157\nv_cvt_pk_bf16_f32 v215, v158, v159\n"
"s_lshl_b32 s74, s94, 11\ns_add_u32 s74, s74, 1048576\ns_add_u32 s75, s74, 65536\nbuffer_store_dwordx4 v[208:211], v253, s[52:55], s74 offen sc1\n"
"buffer_store_dwordx4 v[212:215], v253, s[52:55], s75 offen sc1\ns_cbranch_vccnz 2016f\ns_waitcnt vmcnt(9)\ns_barrier\n2016:\nW 4\nZ 144,176,0\n"
"R 192,244,32768\nM 144,180,8\nR 194,244,33280\nZ 148,176,4\nR 196,244,36864\nM 148,180,12\nR 198,244,37376\nW 4\nZ 152,184,0\nR 176,245,32768\n"
"M 152,188,8\nR 178,245,33280\nZ 156,184,4\nR 180,245,36864\nM 156,188,12\nR 182,245,37376\nW 4\nM 144,192,16\nR 184,244,40960\nM 144,196,24\n"
"R 186,244,41472\nM 148,192,20\nR 188,244,45056\nM 148,196,28\nR 190,244,45568\nW 4\nM 152,176,16\nR 192,245,40960\nM 152,180,24\nR 194,245,41472\n"
"M 156,176,20\nR 196,245,45056\nM 156,180,28\nR 198,245,45568\nW 4\nM 144,184,32\nR 176,244,49152\nM 144,188,40\nR 178,244,49664\nM 148,184,36\n"
"R 180,244,53248\nM 148,188,44\nR 182,244,53760\nW 4\nM 152,192,32\nR 184,245,49152\nM 152,196,40\nR 186,245,49664\nM 156,192,36\nR 188,245,53248\n"
"M 156,196,44\nR 190,245,53760\ns_cbranch_vccz 2017f\ns_waitcnt vmcnt(6)\ns_barrier\n2017:\ns_add_u32 m0, s60, 98304\ns_add_u32 s61, s64, 1327104\n"
"buffer_load_dwordx4 v207, s[48:51], s61 offen lds\ns_lshl_b32 s74, s80, 2\ns_and_b32 s75, s71, 3\ns_add_u32 s74, s74, s75\ns_lshl_b32 s74, s74, 2\n"
"v_lshrrev_b32 v206, 4, v240\nv_add_u32 v206, s74, v206\nv_mul_u32_u24 v206, 0x120, v206\ns_sub_u32 s74, s64, 2304\ns_lshr_b32 s74, s74, 1\n"
"s_lshr_b32 s75, s71, 2\ns_lshl_b32 s75, s75, 4\ns_add_u32 s74, s74, s75\nv_and_b32 v241, 15, v240\nv_add_u32 v241, s74, v241\n"
"v_mul_u32_u24 v242, 0xe39, v241\nv_lshrrev_b32 v242, 16, v242\nv_mul_u32_u24 v200, 18, v242\nv_sub_u32 v200, v241, v200\nv_and_b32 v243, 1, v242\n"
"s_add_u32 m0, s60, 106496\ns_add_u32 s61, s64, 1769472\nbuffer_load_dwordx4 v207, s[48:51], s61 offen lds\nv_lshlrev_b32 v243, 1, v243\n"
"v_sub_u32 v200, v200, v243\nv_ashrrev_i32 v243, 31, v200\nv_and_b32 v243, 18, v243\nv_add_lshl_u32 v200, v200, v243, 4\n"
"v_mul_u32_u24 v242, 0x12000, v242\nv_add3_u32 v200, v200, v242, v206\ns_cbranch_vccnz 2018f\ns_waitcnt vmcnt(8)\ns_barrier\n2018:\nW 4\nM 144,176,48\n"
"R 192,244,57344\nM 144,180,56\nR 194,244,57856\nM 148,176,52\nR 196,244,61440\nM 148,180,60\nR 198,244,61952\nW 4\nM 152,184,48\nR 176,245,57344\n"
"M 152,188,56\nR 178,245,57856\nM 156,184,52\nR 180,245,61440\nM 156,188,60\nR 182,245,61952\nW 4\nM 144,192,64\nR 184,246,4096\nM 144,196,72\n"
"R 186,246,4608\nM 148,192,68\nR 188,246,8192\nM 148,196,76\nR 190,246,8704\nW 4\nM 152,176,64\nR 192,247,4096\nM 152,180,72\nR 194,247,4608\n"
"M 156,176,68\nR 196,247,8192\nM 156,180,76\nR 198,247,8704\nW 4\nM 144,184,80\nR 176,246,12288\nM 144,188,88\nR 178,246,12800\nM 148,184,84\n"
"R 180,246,16384\nM 148,188,92\nR 182,246,16896\nW 4\nM 152,192,80\nR 184,247,12288\nM 152,196,88\nR 186,247,12800\nM 156,192,84\nR 188,247,16384\n"
"M 156,196,92\nR 190,247,16896\ns_cbranch_vccz 2019f\ns_waitcnt vmcnt(5)\ns_barrier\n2019:\ns_add_u32 m0, s60, 24576\ns_add_u32 s61, s62, 0\n"
"buffer_load_dwordx4 v249, s[40:43], s61 offen lds\ns_add_u32 m0, s60, 32768\ns_add_u32 s61, s62, 589824\n"
"buffer_load_dwordx4 v249, s[40:43], s61 offen lds\ns_add_u32 m0, s60, 40960\ns_add_u32 s61, s62, 1179648\n"
"buffer_load_dwordx4 v249, s[40:43], s61 offen lds\ns_cbranch_vccnz 2020f\ns_waitcnt vmcnt(8)\ns_barrier\n2020:\nW 4\nM 144,176,96\nR 192,246,20480\n"
"M 144,180,104\nR 194,246,20992\nM 148,176,100\nR 196,246,24576\nM 148,180,108\nR 198,246,25088\nW 4\nM 152,184,96\nR 176,247,20480\nM 152,188,104\n"
"R 178,247,20992\nM 156,184,100\nR 180,247,24576\nM 156,188,108\nR 182,247,25088\nW 4\nM 144,192,112\nR 184,246,28672\nM 144,196,120\nR 186,246,29184\n"
"M 148,192,116\nR 188,246,32768\nM 148,196,124\nR 190,246,33280\nW 4\nM 152,176,112\nR 192,247,28672\nM 152,180,120\nR 194,247,29184\nM 156,176,116\n"
"R 196,247,32768\nM 156,180,124\nR 198,247,33280\nW 4\nM 144,184,128\nR 176,204,0\nM 144,188,136\nR 178,204,256\nM 148,184,132\nR 180,204,2048\n"
"M 148,188,140\nR 182,204,2304\nW 4\nM 152,192,128\nR 184,204,4096\nM 152,196,136\nR 186,204,4352\nM 156,192,132\nR 188,204,6144\nM 156,196,140\n"
"R 190,204,6400\ns_cbranch_vccz 2021f\ns_waitcnt vmcnt(3)\ns_barrier\n2021:\ns_add_u32 m0, s60, 49152\ns_add_u32 s61, s62, 1769472\n"
"buffer_load_dwordx4 v249, s[40:43], s61 offen lds\ns_nop 2\nv_cvt_pk_bf16_f32 v208, v144, v145\nv_cvt_pk_bf16_f32 v209, v146, v147\n"
"v_cvt_pk_bf16_f32 v210, v148, v149\ns_add_u32 m0, s60, 57344\ns_add_u32 s61, s62, 2359296\nbuffer_load_dwordx4 v249, s[40:43], s61 offen lds\n"
"v_cvt_pk_bf16_f32 v211, v150, v151\nv_cvt_pk_bf16_f32 v212, v152, v153\nv_cvt_pk_bf16_f32 v213, v154, v155\ns_add_u32 m0, s60, 65536\n"
"s_add_u32 s61, s62, 2949120\nbuffer_load_dwordx4 v249, s[40:43], s61 offen lds\nv_cvt_pk_bf16_f32 v214, v156, v157\nv_cvt_pk_bf16_f32 v215, v158, v159\n"
"s_lshl_b32 s74, s95, 11\ns_add_u32 s74, s74, 1048576\ns_add_u32 s75, s74, 65536\nbuffer_store_dwordx4 v[208:211], v253, s[52:55], s74 offen sc1\n"
"buffer_store_dwordx4 v[212:215], v253, s[52:55], s75 offen sc1\ns_cbranch_vccnz 2022f\ns_waitcnt vmcnt(8)\ns_barrier\n2022:\nW 4\nZ 144,0,176\n"
"R 192,204,8192\nM 144,8,180\nR 194,204,8448\nZ 148,4,176\nR 196,204,10240\nM 148,12,180\nR 198,204,10496\nW 4\nM 144,16,184\nR 176,204,12288\n"
"M 144,24,188\nR 178,204,12544\nM 148,20,184\nR 180,204,14336\nM 148,28,188\nR 182,204,14592\nW 4\nM 144,32,192\nR 184,204,16384\nM 144,40,196\n"
"R 186,204,16640\nM 148,36,192\nR 188,204,18432\nM 148,44,196\nR 190,204,18688\nW 4\nM 144,48,176\nR 192,204,20480\nM 144,56,180\nR 194,204,20736\n"
"M 148,52,176\nR 196,204,22528\nM 148,60,180\nR 198,204,22784\nW 4\nM 144,64,184\nR 176,205,36864\nM 144,72,188\nR 178,205,37120\nM 148,68,184\n"
"R 180,205,38912\nM 148,76,188\nR 182,205,39168\nW 4\nM 144,80,192\nR 184,205,40960\nM 144,88,196\nR 186,205,41216\nM 148,84,192\nR 188,205,43008\n"
"M 148,92,196\nR 190,205,43264\ns_cbranch_vccz 2023f\ns_waitcnt vmcnt(6)\ns_barrier\n2023:\ns_add_u32 m0, s60, 73728\ns_add_u32 s61, s62, 3538944\n"
"buffer_load_dwordx4 v249, s[40:43], s61 offen lds\ns_add_u32 m0, s60, 81920\ns_add_u32 s61, s62, 4128768\n"
"buffer_load_dwordx4 v249, s[40:43], s61 offen lds\ns_add_u32 m0, s60, 90112\ns_add_u32 s61, s62, 4718592\n"
"buffer_load_dwordx4 v249, s[40:43], s61 offen lds\ns_cbranch_vccnz 2024f\ns_waitcnt vmcnt(9)\ns_barrier\n2024:\nW 4\nM 144,96,176\nR 192,205,45056\n"
"M 144,104,180\nR 194,205,45312\nM 148,100,176\nR 196,205,47104\nM 148,108,180\nR 198,205,47360\nW 4\nM 144,112,184\nR 176,244,24576\nM 144,120,188\n"
"R 178,244,25088\nM 148,116,184\nR 180,244,28672\nM 148,124,188\nR 182,244,29184\nW 4\nM 144,128,192\nR 184,245,24576\nM 144,136,196\nR 186,245,25088\n"
"M 148,132,192\nR 188,245,28672\nM 148,140,196\nR 190,245,29184\ns_cbranch_vccz 2025f\ns_waitcnt vmcnt(6)\ns_barrier\n2025:\ns_add_u32 m0, s60, 0\n"
"s_add_u32 s61, s62, 0\nbuffer_load_dwordx4 v249, s[44:47], s61 offen lds\ns_nop 2\nv_cvt_pk_bf16_f32 v208, v144, v148\ns_add_u32 m0, s60, 8192\n"
"s_add_u32 s61, s62, 589824\nbuffer_load_dwordx4 v249, s[44:47], s61 offen lds\nv_cvt_pk_bf16_f32 v209, v145, v149\nv_cvt_pk_bf16_f32 v210, v146, v150\n"
"s_add_u32 m0, s60, 16384\ns_add_u32 s61, s62, 1179648\nbuffer_load_dwordx4 v249, s[44:47], s61 offen lds\nv_cvt_pk_bf16_f32 v211, v147, v151\n"
"s_mov_b32 s74, 5767168\nbuffer_store_dwordx4 v[208:211], v200, s[52:55], s74 offen sc1\ns_cbranch_vccnz 2026f\ns_waitcnt vmcnt(10)\ns_barrier\n2026:\n"
"W 4\nZ 144,176,0\nR 192,244,32768\nM 144,180,8\nR 194,244,33280\nZ 148,176,4\nR 196,244,36864\nM 148,180,12\nR 198,244,37376\nW 4\nZ 152,184,0\n"
"R 176,245,32768\nM 152,188,8\nR 178,245,33280\nZ 156,184,4\nR 180,245,36864\nM 156,188,12\nR 182,245,37376\nW 4\nM 144,192,16\nR 184,244,40960\n"
"M 144,196,24\nR 186,244,41472\nM 148,192,20\nR 188,244,45056\nM 148,196,28\nR 190,244,45568\nW 4\nM 152,176,16\nR 192,245,40960\nM 152,180,24\n"
"R 194,245,41472\nM 156,176,20\nR 196,245,45056\nM 156,180,28\nR 198,245,45568\nW 4\nM 144,184,32\nR 176,244,49152\nM 144,188,40\nR 178,244,49664\n"
"M 148,184,36\nR 180,244,53248\nM 148,188,44\nR 182,244,53760\nW 4\nM 152,192,32\nR 184,245,49152\nM 152,196,40\nR 186,245,49664\nM 156,192,36\n"
"R 188,245,53248\nM 156,196,44\nR 190,245,53760\ns_cbranch_vccz 2027f\ns_waitcnt vmcnt(0)\ns_barrier\ns_cmp_lg_u32 s71, 4\ns_cbranch_scc1 1999f\n"
"s_mov_b64 exec, 1\nv_mov_b32 v254, s72\nbuffer_store_dword v254, off, s[52:55], s73 sc1\ns_mov_b64 exec, -1\n1999:\n2027:\ns_add_u32 m0, s60, 98304\n"
"s_add_u32 s61, s62, 1769472\nbuffer_load_dwordx4 v249, s[44:47], s61 offen lds\ns_and_b32 s69, s84, 7\ns_lshl_b32 s69, s69, 1\ns_lshr_b32 s74, s84, 7\n"
"s_add_u32 s69, s69, s74\ns_mul_i32 s66, s69, 147456\ns_add_u32 s66, s66, 1114112\ns_bfe_u32 s69, s84, 0x10006\ns_mul_i32 s65, s69, 81920\n"
"s_add_u32 s65, s65, s66\ns_sub_u32 s65, s65, 65536\ns_bfe_u32 s74, s84, 0x20004\ns_lshl_b32 s74, s74, 12\ns_lshl_b32 s75, s71, 9\n"
"s_add_u32 s74, s74, s75\nv_lshrrev_b32 v226, 1, v224\nv_and_b32 v226, 0xf8, v226\nv_add_u32 v226, s74, v226\nv_and_b32 v225, 0x200, v224\n"
"v_lshlrev_b32 v225, 6, v225\nv_add_u32 v225, v225, v226\nv_lshrrev_b32 v208, 4, v224\nv_and_b32 v209, 31, v208\nv_lshrrev_b32 v210, 5, v208\n"
"v_lshlrev_b32 v227, 13, v210\nv_lshl_add_u32 v227, v209, 3, v227\ns_add_u32 m0, s60, 106496\ns_add_u32 s61, s62, 2359296\n"
"buffer_load_dwordx4 v249, s[44:47], s61 offen lds\ns_lshl_b32 s74, s69, 12\ns_add_u32 s74, s74, 122880\nv_add_u32 v227, s74, v227\n"
"v_lshlrev_b32 v228, 3, v209\ns_lshl_b32 s74, s69, 14\ns_sub_u32 s74, 139264, s74\nv_add_u32 v228, s74, v228\nv_mul_u32_u24 v229, 288, v210\n"
"v_lshl_add_u32 v229, v209, 4, v229\nv_add_u32 v229, 143360, v229\nv_and_b32 v211, 1, v209\ns_add_u32 m0, s60, 114688\ns_add_u32 s61, s62, 2949120\n"
"buffer_load_dwordx4 v249, s[44:47], s61 offen lds\nv_lshrrev_b32 v212, 1, v209\nv_lshl_add_u32 v211, v211, 4, v212\ns_bfe_u32 s74, s84, 0x20004\n"
"s_lshl_b32 s74, s74, 9\ns_lshl_b32 s75, s71, 6\ns_add_u32 s74, s74, s75\nv_add_u32 v211, s74, v211\nv_lshlrev_b32 v235, 15, v210\n"
"v_lshl_add_u32 v235, v211, 4, v235\nv_lshlrev_b32 v236, 3, v211\ns_and_b32 s74, s84, 7\ns_lshl_b32 s74, s74, 2\ns_lshr_b32 s75, s84, 6\n"
"s_add_u32 s74, s74, s75\ns_lshl_b32 s75, s74, 16\nv_add_u32 v235, s75, v235\ns_lshr_b32 s74, s74, 1\ns_lshl_b32 s74, s74, 14\ns_lshl_b32 s75, s69, 2\n"
"s_add_u32 s74, s74, s75\nv_add_u32 v236, s74, v236\ns_cbranch_vccnz 2028f\ns_waitcnt vmcnt(3)\ns_barrier\n2028:\nW 4\nM 144,176,48\nR 192,244,57344\n"
"M 144,180,56\nR 194,244,57856\nM 148,176,52\nR 196,244,61440\nM 148,180,60\nR 198,244,61952\nW 4\nM 152,184,48\nR 176,245,57344\nM 152,188,56\n"
"R 178,245,57856\nM 156,184,52\nR 180,245,61440\nM 156,188,60\nR 182,245,61952\nW 4\nM 144,192,64\nR 184,246,4096\nM 144,196,72\nR 186,246,4608\n"
"M 148,192,68\nR 188,246,8192\nM 148,196,76\nR 190,246,8704\nW 4\nM 152,176,64\nR 192,247,4096\nM 152,180,72\nR 194,247,4608\nM 156,176,68\n"
"R 196,247,8192\nM 156,180,76\nR 198,247,8704\nW 4\nM 144,184,80\nR 176,246,12288\nM 144,188,88\nR 178,246,12800\nM 148,184,84\nR 180,246,16384\n"
"M 148,188,92\nR 182,246,16896\nW 4\nM 152,192,80\nR 184,247,12288\nM 152,196,88\nR 186,247,12800\nM 156,192,84\nR 188,247,16384\nM 156,196,92\n"
"R 190,247,16896\ns_cbranch_vccz 2029f\ns_waitcnt vmcnt(5)\ns_barrier\n2029:\ns_add_u32 m0, s60, 24576\ns_add_u32 s61, s62, 3538944\n"
"buffer_load_dwordx4 v249, s[44:47], s61 offen lds\ns_and_b32 s80, s84, 7\ns_lshl_b32 s80, s80, 1\ns_lshr_b32 s74, s84, 7\ns_add_u32 s80, s80, s74\n"
"s_bfe_u32 s81, s84, 0x30004\nv_lshrrev_b32 v211, 3, v208\ns_lshl_b32 s74, s71, 3\nv_add_u32 v212, s74, v211\nv_mul_u32_u24 v230, 0x480, v212\n"
"v_and_b32 v211, 6, v211\nv_and_b32 v212, 7, v208\nv_xor_b32 v211, v211, v212\ns_add_u32 m0, s60, 32768\ns_add_u32 s61, s62, 4128768\n"
"buffer_load_dwordx4 v249, s[44:47], s61 offen lds\nv_lshl_add_u32 v230, v211, 4, v230\ns_lshl_b32 s74, s81, 7\nv_add_u32 v230, s74, v230\n"
"v_mul_u32_u24 v231, 0x480, v208\ns_lshl_b32 s74, s81, 4\ns_add_u32 s74, s74, 1024\nv_add_u32 v231, s74, v231\ns_mov_b64 s[48:49], s[82:83]\n"
"s_mov_b32 s50, 663552\ns_mov_b64 s[56:57], s[86:87]\ns_mov_b32 s58, 5308416\nv_lshlrev_b32 v234, 2, v224\ns_and_b32 s74, s84, 1\n"
"s_lshl_b32 s74, s74, 3\ns_bfe_u32 s64, s84, 0x30004\ns_or_b32 s74, s74, s64\ns_and_b32 s75, s84, 6\ns_lshr_b32 s64, s84, 7\ns_add_u32 s75, s75, s64\n"
"s_mul_i32 s67, s75, 663552\ns_and_b32 s64, s74, 7\ns_lshl_b32 s64, s64, 3\ns_add_u32 s64, s64, s71\ns_mul_i32 s64, s64, 10368\ns_add_u32 s67, s67, s64\n"
"s_lshl_b32 s90, s64, 3\ns_mul_i32 s64, s75, 1152\ns_add_u32 s90, s90, s64\ns_add_u32 s64, s76, -4\ns_add_u32 m0, s60, 40960\n"
"s_add_u32 s61, s62, 4718592\nbuffer_load_dwordx4 v249, s[44:47], s61 offen lds\nv_lshrrev_b32 v239, 2, v208\nv_mul_u32_u24 v239, 0x2400, v239\n"
"v_and_b32 v211, 3, v208\nv_add_u32 v211, s64, v211\nv_ashrrev_i32 v212, 31, v211\nv_and_b32 v212, 9, v212\nv_add_u32 v211, v211, v212\n"
"v_lshl_add_u32 v239, v211, 7, v239\ns_cbranch_vccnz 2030f\ns_waitcnt vmcnt(8)\ns_barrier\n2030:\nW 4\nM 144,176,96\nR 192,246,20480\nM 144,180,104\n"
"R 194,246,20992\nM 148,176,100\nR 196,246,24576\nM 148,180,108\nR 198,246,25088\nW 4\nM 152,184,96\nR 176,247,20480\nM 152,188,104\nR 178,247,20992\n"
"M 156,184,100\nR 180,247,24576\nM 156,188,108\nR 182,247,25088\nW 4\nM 144,192,112\nR 184,246,28672\nM 144,196,120\nR 186,246,29184\nM 148,192,116\n"
"R 188,246,32768\nM 148,196,124\nR 190,246,33280\nW 4\nM 152,176,112\nR 192,247,28672\nM 152,180,120\nR 194,247,29184\nM 156,176,116\nR 196,247,32768\n"
"M 156,180,124\nR 198,247,33280\nW 4\nM 144,184,128\nR 176,244,0\nM 144,188,136\nR 178,244,512\nM 148,184,132\nR 180,244,4096\nM 148,188,140\n"
"R 182,244,4608\nW 4\nM 152,192,128\nR 184,245,0\nM 152,196,136\nR 186,245,512\nM 156,192,132\nR 188,245,4096\nM 156,196,140\nR 190,245,4608\n"
"s_cbranch_vccz 2031f\ns_waitcnt vmcnt(4)\ns_waitcnt vmcnt(0)\ns_barrier\ns_cmp_lg_u32 s71, 5\ns_cbranch_scc1 1998f\nv_lshlrev_b32 v204, 2, v224\n"
"s_mov_b32 s74, 4096\ns_mov_b32 s75, 8192\ns_mov_b32 s85, 53248\ns_mov_b32 s91, 57344\nbuffer_load_dword v200, v204, s[52:55], s74 offen sc1\n"
"buffer_load_dword v201, v204, s[52:55], s75 offen sc1\n1998:\n2031:\ns_nop 5\nv_mul_f32 v240, v144, v144\nv_mul_f32 v241, v145, v145\n"
"v_mul_f32 v242, v146, v146\nv_mul_f32 v243, v147, v147\nv_fma_f32 v240, v240, s70, v248\nv_fma_f32 v241, v241, s70, v248\n"
"v_fma_f32 v242, v242, s70, v248\nv_fma_f32 v243, v243, s70, v248\nv_mul_f32 v240, v240, v144\nv_mul_f32 v241, v241, v145\nv_mul_f32 v242, v242, v146\n"
"v_mul_f32 v243, v243, v147\nE 240\nE 241\nE 242\nE 243\nv_add_f32 v240, 1.0, v240\nv_add_f32 v241, 1.0, v241\nv_add_f32 v242, 1.0, v242\n"
"v_add_f32 v243, 1.0, v243\nv_rcp_f32 v240, v240\nv_rcp_f32 v241, v241\nv_rcp_f32 v242, v242\ns_mov_b64 s[92:93], s[40:41]\n"
"s_mov_b64 s[94:95], s[42:43]\ns_mov_b64 s[96:97], s[44:45]\ns_mov_b64 s[98:99], s[46:47]\ns_and_b32 s62, s84, 7\ns_lshl_b32 s62, s62, 2\n"
"s_lshr_b32 s63, s84, 6\ns_add_u32 s62, s62, s63\ns_lshl_b32 s47, s71, 10\ns_mul_i32 s43, s62, 73728\ns_add_u32 s43, s43, s47\n"
"s_add_u32 s43, s43, 5771264\ns_lshr_b32 s63, s62, 1\ns_mul_i32 s63, s63, 9\ns_and_b32 s62, s62, 1\ns_lshl_b32 s62, s62, 2\ns_add_u32 s62, s62, s63\n"
"s_lshr_b32 s63, s71, 2\ns_add_u32 s62, s62, s63\ns_lshl_b32 s62, s62, 14\ns_and_b32 s63, s47, 0xc00\ns_add_u32 s62, s62, s63\n"
"s_add_u32 s40, s62, 3407872\ns_add_u32 s41, s40, 32768\ns_add_u32 s62, s40, 65536\ns_sub_u32 s63, s43, 8192\ns_cmp_lt_u32 s71, 4\n"
"s_cselect_b32 s42, s62, s63\ns_movk_i32 s62, 0x4800\ns_cselect_b32 s45, 0x1000, s62\ns_cmp_lt_u32 s71, 6\ns_cselect_b32 s62, 0x2000, 0\n"
"s_add_u32 s44, s43, s62\ns_add_u32 s46, s47, s62\ns_add_u32 s46, s46, 24576\ns_cbranch_vccnz 2032f\ns_waitcnt vmcnt(4)\ns_barrier\n2032:\nW 4\n"
"Z 160,176,0\nR 192,244,8192\nM 160,180,8\nR 194,244,8704\nZ 164,176,4\nR 196,244,12288\nM 164,180,12\nR 198,244,12800\nW 4\nZ 168,184,0\n"
"R 176,245,8192\nM 168,188,8\nR 178,245,8704\nZ 172,184,4\nR 180,245,12288\nM 172,188,12\nR 182,245,12800\nW 4\nM 160,192,16\nR 184,244,16384\n"
"M 160,196,24\nR 186,244,16896\nM 164,192,20\nR 188,244,20480\nM 164,196,28\nR 190,244,20992\nW 4\nM 168,176,16\nR 192,245,16384\nM 168,180,24\n"
"R 194,245,16896\nM 172,176,20\nR 196,245,20480\nM 172,180,28\nR 198,245,20992\nW 4\nM 160,184,32\nR 176,246,36864\nM 160,188,40\nR 178,246,37376\n"
"M 164,184,36\nR 180,246,40960\nM 164,188,44\nR 182,246,41472\nW 4\nM 168,192,32\nR 184,247,36864\nM 168,196,40\nR 186,247,37376\nM 172,192,36\n"
"R 188,247,40960\nM 172,196,44\nR 190,247,41472\ns_cbranch_vccz 2033f\ns_barrier\ns_cmp_lg_u32 s71, 5\ns_cbranch_scc1 1997f\ns_mov_b32 s61, 0x40000\n"
"1996:\ns_waitcnt vmcnt(0)\nv_xor_b32 v200, s72, v200\nv_xor_b32 v201, s72, v201\nv_or_b32 v200, v200, v201\nv_cmp_ne_u32 s[62:63], 0, v200\n"
"s_sub_u32 s61, s61, 1\ns_cmp_eq_u32 s61, 0\ns_cbranch_scc1 1995f\ns_cmp_eq_u64 s[62:63], 0\ns_cbranch_scc1 1995f\ns_sleep 1\n"
"buffer_load_dword v200, v204, s[52:55], s74 offen sc1\nbuffer_load_dword v201, v204, s[52:55], s75 offen sc1\ns_branch 1996b\n1995:\nbuffer_inv sc1\n"
"1997:\n2033:\nv_rcp_f32 v243, v243\nv_mul_f32 v144, v240, v144\nv_mul_f32 v145, v241, v145\nv_mul_f32 v146, v242, v146\nv_mul_f32 v147, v243, v147\n"
"v_mul_f32 v240, v148, v148\nv_mul_f32 v241, v149, v149\nv_mul_f32 v242, v150, v150\nv_mul_f32 v243, v151, v151\nv_fma_f32 v240, v240, s70, v248\n"
"v_fma_f32 v241, v241, s70, v248\nv_fma_f32 v242, v242, s70, v248\nv_fma_f32 v243, v243, s70, v248\nv_mul_f32 v240, v240, v148\n"
"v_mul_f32 v241, v241, v149\nv_mul_f32 v242, v242, v150\nv_mul_f32 v243, v243, v151\nE 240\nE 241\nE 242\nE 243\nv_add_f32 v240, 1.0, v240\n"
"v_add_f32 v241, 1.0, v241\ns_cbranch_vccnz 2034f\ns_waitcnt vmcnt(1)\ns_barrier\n2034:\nW 4\nM 160,176,48\nR 192,246,45056\nM 160,180,56\n"
"R 194,246,45568\nM 164,176,52\nR 196,246,49152\nM 164,180,60\nR 198,246,49664\nW 4\nM 168,184,48\nR 176,247,45056\nM 168,188,56\nR 178,247,45568\n"
"M 172,184,52\nR 180,247,49152\nM 172,188,60\nR 182,247,49664\nW 4\nM 160,192,64\nR 184,246,53248\nM 160,196,72\nR 186,246,53760\nM 164,192,68\n"
"R 188,246,57344\nM 164,196,76\nR 190,246,57856\nW 4\nM 168,176,64\nR 192,247,53248\nM 168,180,72\nR 194,247,53760\nM 172,176,68\nR 196,247,57344\n"
"M 172,180,76\nR 198,247,57856\nW 4\nM 160,184,80\nR 176,244,24576\nM 160,188,88\nR 178,244,25088\nM 164,184,84\nR 180,244,28672\nM 164,188,92\n"
"R 182,244,29184\nW 4\nM 168,192,80\nR 184,245,24576\nM 168,196,88\nR 186,245,25088\nM 172,192,84\nR 188,245,28672\nM 172,196,92\nR 190,245,29184\n"
"s_cbranch_vccz 2035f\ns_waitcnt vmcnt(0)\ns_barrier\n2035:\nv_add_f32 v242, 1.0, v242\nv_add_f32 v243, 1.0, v243\nv_rcp_f32 v240, v240\n"
"v_rcp_f32 v241, v241\nv_rcp_f32 v242, v242\nv_rcp_f32 v243, v243\nv_mul_f32 v148, v240, v148\nv_mul_f32 v149, v241, v149\nv_mul_f32 v150, v242, v150\n"
"v_mul_f32 v151, v243, v151\nv_mul_f32 v240, v152, v152\nv_mul_f32 v241, v153, v153\nv_mul_f32 v242, v154, v154\nv_mul_f32 v243, v155, v155\n"
"v_fma_f32 v240, v240, s70, v248\nv_fma_f32 v241, v241, s70, v248\nv_fma_f32 v242, v242, s70, v248\nv_fma_f32 v243, v243, s70, v248\n"
"v_mul_f32 v240, v240, v152\nv_mul_f32 v241, v241, v153\nv_mul_f32 v242, v242, v154\nv_mul_f32 v243, v243, v155\nv_mov_b32 v208, 0\n"
"s_mov_b32 exec_lo, 0x140000\ns_mov_b32 exec_hi, 0x140000\nv_mov_b32 v208, 0x3f803f80\ns_mov_b64 exec, -1\nv_mov_b32 v209, v208\n"
"v_mov_b64 v[210:211], v[208:209]\nv_mov_b64 v[212:213], v[208:209]\nv_mov_b64 v[214:215], v[208:209]\nv_mov_b64 v[216:217], v[208:209]\n"
"v_mov_b64 v[218:219], v[208:209]\nv_mov_b64 v[220:221], v[208:209]\nv_mov_b64 v[222:223], v[208:209]\nv_mov_b32 v205, 0xdddddddd\n"
"v_mov_b64 v[48:49], 0\nv_mov_b64 v[50:51], 0\nv_mov_b64 v[52:53], 0\nv_mov_b64 v[54:55], 0\nv_mov_b64 v[56:57], 0\nv_mov_b64 v[58:59], 0\n"
"v_mov_b64 v[60:61], 0\nv_mov_b64 v[62:63], 0\nv_mov_b64 v[64:65], 0\nv_mov_b64 v[66:67], 0\nv_mov_b64 v[68:69], 0\nv_mov_b64 v[70:71], 0\n"
"v_mov_b64 v[72:73], 0\nv_mov_b64 v[74:75], 0\nv_mov_b64 v[76:77], 0\nv_mov_b64 v[78:79], 0\nv_mov_b64 v[80:81], 0\nv_mov_b64 v[82:83], 0\n"
"v_mov_b64 v[84:85], 0\nv_mov_b64 v[86:87], 0\nv_mov_b64 v[88:89], 0\nv_mov_b64 v[90:91], 0\nv_mov_b64 v[92:93], 0\nv_mov_b64 v[94:95], 0\n"
"s_add_u32 s73, s73, 12288\ns_cbranch_vccnz 2036f\ns_waitcnt vmcnt(0)\ns_barrier\n2036:\nW 4\nM 160,176,96\nR 192,244,32768\nM 160,180,104\n"
"R 194,244,33280\nM 164,176,100\nR 196,244,36864\nM 164,180,108\nR 198,244,37376\nW 4\nM 168,184,96\nR 176,245,32768\nM 168,188,104\nR 178,245,33280\n"
"M 172,184,100\nR 180,245,36864\nM 172,188,108\nR 182,245,37376\nW 4\nM 160,192,112\nR 184,244,40960\nM 160,196,120\nR 186,244,41472\nM 164,192,116\n"
"R 188,244,45056\nM 164,196,124\nR 190,244,45568\nW 4\nM 168,176,112\nR 192,245,40960\nM 168,180,120\nR 194,245,41472\nM 172,176,116\nR 196,245,45056\n"
"M 172,180,124\nR 198,245,45568\nW 4\nM 160,184,128\nM 160,188,136\nM 164,184,132\nM 164,188,140\nW 0\nM 168,192,128\nM 168,196,136\nM 172,192,132\n"
"M 172,196,140\ns_cbranch_vccz 2037f\ns_barrier\n2037:\ns_nop 6\ns_add_u32 m0, s60, 122880\ns_nop 0\nbuffer_load_dwordx4 v224, s[52:55], s40 offen lds\n"
"s_add_u32 m0, s60, 131072\ns_nop 0\nbuffer_load_dwordx4 v224, s[52:55], s41 offen lds\ns_add_u32 m0, s60, 139264\ns_nop 0\n"
"buffer_load_dwordx4 v224, s[52:55], s42 offen lds\ns_add_u32 m0, s60, 147456\ns_nop 0\nbuffer_load_dwordx4 v224, s[52:55], s43 offen lds\n"
"s_add_u32 m0, s46, 122880\ns_nop 0\nbuffer_load_dwordx4 v224, s[52:55], s44 offen lds\ns_add_u32 s74, s65, 16384\n"
"buffer_load_dwordx2 v[128:129], v225, s[52:55], s65 offen offset:0\nbuffer_load_dwordx2 v[130:131], v225, s[52:55], s74 offen offset:0\n"
"buffer_load_dwordx2 v[136:137], v226, s[52:55], s66 offen offset:0\nbuffer_load_dwordx2 v[132:133], v225, s[52:55], s65 offen offset:256\n"
"buffer_load_dwordx2 v[134:135], v225, s[52:55], s74 offen offset:256\nbuffer_load_dwordx2 v[138:139], v226, s[52:55], s66 offen offset:256\n"
"s_cbranch_vccnz 2038f\ns_barrier\n2038:\ns_branch 73f\n72:\nR 176,244,0\nR 178,244,512\nR 180,244,4096\nR 182,244,4608\nR 184,245,0\nR 186,245,512\n"
"R 188,245,4096\nR 190,245,4608\ns_cbranch_vccz 3000f\ns_add_u32 m0, s85, 113920\ns_nop 0\nbuffer_load_dwordx4 v216, s[56:59], s90 offen offset:768 lds\n"
"s_add_u32 m0, s85, 114944\ns_nop 0\nbuffer_load_dwordx4 v217, s[56:59], s90 offen offset:768 lds\ns_add_u32 m0, s91, 113920\ns_nop 0\n"
"buffer_load_dwordx4 v216, s[56:59], s92 offen offset:768 lds\ns_add_u32 m0, s91, 114944\ns_nop 0\n"
"buffer_load_dwordx4 v217, s[56:59], s92 offen offset:768 lds\ns_add_u32 m0, s85, 130176\ns_nop 0\n"
"buffer_load_dwordx4 v216, s[56:59], s90 offen offset:896 lds\ns_add_u32 m0, s85, 131200\ns_nop 0\n"
"buffer_load_dwordx4 v217, s[56:59], s90 offen offset:896 lds\n3000:\nW 4\nZ 144,0,176\nR 192,244,8192\nM 144,8,180\nR 194,244,8704\nZ 148,4,176\n"
"R 196,244,12288\nM 148,12,180\nR 198,244,12800\nW 4\nZ 152,0,184\nR 176,245,8192\nM 152,8,188\nR 178,245,8704\nZ 156,4,184\nR 180,245,12288\n"
"M 156,12,188\nR 182,245,12800\nW 4\nM 144,16,192\nR 184,244,16384\nM 144,24,196\nR 186,244,16896\nM 148,20,192\nR 188,244,20480\nM 148,28,196\n"
"R 190,244,20992\nW 4\nM 152,16,176\nR 192,245,16384\nM 152,24,180\nR 194,245,16896\nM 156,20,176\nR 196,245,20480\nM 156,28,180\nR 198,245,20992\nW 4\n"
"M 144,32,184\nM 144,40,188\nM 148,36,184\nM 148,44,188\nW 0\nM 152,32,192\nM 152,40,196\nM 156,36,192\nM 156,44,196\nv_lshlrev_b32 v224, 4, v240\n"
"s_lshl_b32 s74, s71, 11\nv_add_u32 v251, s74, v224\nv_lshrrev_b32 v241, 4, v240\nv_lshlrev_b32 v253, 14, v241\nv_and_b32 v242, 0xf0, v224\n"
"v_add_u32 v253, v253, v242\ns_lshr_b32 s74, s71, 2\ns_lshl_b32 s74, s74, 17\ns_and_b32 s75, s71, 3\ns_lshl_b32 s75, s75, 8\ns_add_u32 s74, s74, s75\n"
"s_lshl_b32 s75, s80, 10\ns_add_u32 s74, s74, s75\nv_add_u32 v253, s74, v253\ns_lshl_b32 s74, s80, 2\ns_and_b32 s75, s71, 3\ns_add_u32 s74, s74, s75\n"
"s_lshl_b32 s74, s74, 2\nv_lshrrev_b32 v206, 4, v240\nv_add_u32 v206, s74, v206\nv_mul_u32_u24 v206, 0x120, v206\ns_sub_u32 s74, s93, 2304\n"
"s_lshr_b32 s74, s74, 1\ns_lshr_b32 s75, s71, 2\ns_lshl_b32 s75, s75, 5\ns_add_u32 s74, s74, s75\nv_and_b32 v241, 15, v240\nv_add_u32 v241, s74, v241\n"
"v_mul_u32_u24 v242, 0xe39, v241\nv_lshrrev_b32 v242, 16, v242\nv_mul_u32_u24 v200, 18, v242\nv_sub_u32 v200, v241, v200\nv_and_b32 v243, 1, v242\n"
"v_lshlrev_b32 v243, 1, v243\nv_sub_u32 v200, v200, v243\nv_ashrrev_i32 v243, 31, v200\nv_and_b32 v243, 18, v243\nv_add_lshl_u32 v200, v200, v243, 4\n"
"v_mul_u32_u24 v242, 0x12000, v242\nv_add3_u32 v200, v200, v242, v206\ns_cbranch_vccnz 3001f\ns_add_u32 m0, s85, 130176\ns_nop 0\n"
"buffer_load_dwordx4 v216, s[56:59], s90 offen offset:896 lds\ns_add_u32 m0, s85, 131200\ns_nop 0\n"
"buffer_load_dwordx4 v217, s[56:59], s90 offen offset:896 lds\ns_add_u32 m0, s85, 146432\ns_nop 0\n"
"buffer_load_dwordx4 v216, s[56:59], s90 offen offset:1024 lds\ns_add_u32 m0, s85, 147456\ns_nop 0\n"
"buffer_load_dwordx4 v217, s[56:59], s90 offen offset:1024 lds\ns_add_u32 m0, s91, 146432\ns_nop 0\n"
"buffer_load_dwordx4 v216, s[56:59], s92 offen offset:1024 lds\ns_add_u32 m0, s91, 147456\ns_nop 0\n"
"buffer_load_dwordx4 v217, s[56:59], s92 offen offset:1024 lds\n3001:\ns_cbranch_vccnz 3002f\ns_waitcnt vmcnt(6)\n3002:\ns_cbranch_vccz 3003f\n"
"s_waitcnt vmcnt(6)\n3003:\nv_add_u32 v218, 0xffff4000, v218\nv_add_u32 v219, 0xffff4000, v219\ns_barrier\nds_read_b128 v[48:51], v218 offset:0\n"
"ds_read_b128 v[52:55], v218 offset:2048\nds_read_b128 v[56:59], v219 offset:0\nds_read_b128 v[60:63], v219 offset:2048\n"
"ds_read_b128 v[64:67], v218 offset:16384\nds_read_b128 v[68:71], v218 offset:18432\nds_read_b128 v[72:75], v219 offset:16384\n"
"ds_read_b128 v[76:79], v219 offset:18432\nds_read_b128 v[80:83], v218 offset:32768\nds_read_b128 v[84:87], v218 offset:34816\n"
"ds_read_b128 v[88:91], v219 offset:32768\nds_read_b128 v[92:95], v219 offset:34816\nW 0\nR 176,244,24576\nR 178,244,25088\nR 180,244,28672\n"
"R 182,244,29184\nR 184,245,24576\nR 186,245,25088\nR 188,245,28672\nR 190,245,29184\ns_barrier\ns_cbranch_vccz 3004f\ns_add_u32 m0, s60, 65536\n"
"s_add_u32 s61, s93, 1769472\nbuffer_load_dwordx4 v250, s[48:51], s61 offen lds\ns_sub_u32 m0, m0, 4096\ns_nop 0\n"
"buffer_load_dwordx4 v221, s[48:51], s61 offen lds\ns_add_u32 m0, s60, 73728\ns_add_u32 s61, s94, 0\nbuffer_load_dwordx4 v250, s[48:51], s61 offen lds\n"
"s_sub_u32 m0, m0, 4096\ns_nop 0\nbuffer_load_dwordx4 v221, s[48:51], s61 offen lds\ns_add_u32 m0, s60, 81920\ns_add_u32 s61, s94, 221184\n"
"buffer_load_dwordx4 v250, s[48:51], s61 offen lds\ns_sub_u32 m0, m0, 4096\ns_nop 0\nbuffer_load_dwordx4 v221, s[48:51], s61 offen lds\n3004:\nW 4\n"
"M 144,48,176\nR 192,244,32768\nM 144,56,180\nR 194,244,33280\nM 148,52,176\nR 196,244,36864\nM 148,60,180\nR 198,244,37376\nW 4\nM 152,48,184\n"
"R 176,245,32768\nM 152,56,188\nR 178,245,33280\nM 156,52,184\nR 180,245,36864\nM 156,60,188\nR 182,245,37376\nW 4\nM 144,64,192\nR 184,244,40960\n"
"M 144,72,196\nR 186,244,41472\nM 148,68,192\nR 188,244,45056\nM 148,76,196\nR 190,244,45568\nW 4\nM 152,64,176\nR 192,245,40960\nM 152,72,180\n"
"R 194,245,41472\nM 156,68,176\nR 196,245,45056\nM 156,76,180\nR 198,245,45568\nW 4\nM 144,80,184\nM 144,88,188\nM 148,84,184\nM 148,92,188\nW 0\n"
"M 152,80,192\nM 152,88,196\nM 156,84,192\nM 156,92,196\nv_add_u32 v241, 16, v241\nv_mul_u32_u24 v242, 0xe39, v241\nv_lshrrev_b32 v242, 16, v242\n"
"v_mul_u32_u24 v201, 18, v242\nv_sub_u32 v201, v241, v201\nv_and_b32 v243, 1, v242\nv_lshlrev_b32 v243, 1, v243\nv_sub_u32 v201, v201, v243\n"
"v_ashrrev_i32 v243, 31, v201\nv_and_b32 v243, 18, v243\nv_add_lshl_u32 v201, v201, v243, 4\nv_mul_u32_u24 v242, 0x12000, v242\n"
"v_add3_u32 v201, v201, v242, v206\ns_cbranch_vccnz 3005f\ns_add_u32 m0, s60, 90112\ns_add_u32 s61, s94, 442368\n"
"buffer_load_dwordx4 v250, s[48:51], s61 offen lds\ns_add_u32 m0, m0, 4096\ns_add_u32 s61, s61, 110592\n"
"buffer_load_dwordx4 v250, s[48:51], s61 offen lds\ns_add_u32 m0, s60, 0\ns_add_u32 s61, s94, 663552\nbuffer_load_dwordx4 v250, s[48:51], s61 offen lds\n"
"s_add_u32 m0, m0, 4096\ns_add_u32 s61, s61, 110592\nbuffer_load_dwordx4 v250, s[48:51], s61 offen lds\ns_add_u32 m0, s60, 8192\n"
"s_add_u32 s61, s94, 884736\nbuffer_load_dwordx4 v250, s[48:51], s61 offen lds\ns_add_u32 m0, m0, 4096\ns_add_u32 s61, s61, 110592\n"
"buffer_load_dwordx4 v250, s[48:51], s61 offen lds\ns_add_u32 m0, s60, 16384\ns_add_u32 s61, s94, 1105920\n"
"buffer_load_dwordx4 v250, s[48:51], s61 offen lds\ns_add_u32 m0, m0, 4096\ns_add_u32 s61, s61, 110592\n"
"buffer_load_dwordx4 v250, s[48:51], s61 offen lds\n3005:\ns_cbranch_vccnz 3006f\ns_waitcnt vmcnt(8)\n3006:\ns_cbranch_vccz 3007f\ns_waitcnt vmcnt(0)\n"
"3007:\nv_add_u32 v218, 0xc000, v218\nv_add_u32 v219, 0xc000, v219\ns_barrier\nds_read_b128 v[96:99], v218 offset:0\n"
"ds_read_b128 v[100:103], v218 offset:2048\nds_read_b128 v[104:107], v219 offset:0\nds_read_b128 v[108:111], v219 offset:2048\n"
"ds_read_b128 v[112:115], v218 offset:16384\nds_read_b128 v[116:119], v218 offset:18432\nds_read_b128 v[120:123], v219 offset:16384\n"
"ds_read_b128 v[124:127], v219 offset:18432\nds_read_b128 v[128:131], v218 offset:32768\nds_read_b128 v[132:135], v218 offset:34816\n"
"ds_read_b128 v[136:139], v219 offset:32768\nds_read_b128 v[140:143], v219 offset:34816\nW 0\nR 176,244,49152\nR 178,244,49664\nR 180,244,53248\n"
"R 182,244,53760\nR 184,245,49152\nR 186,245,49664\nR 188,245,53248\nR 190,245,53760\ns_barrier\ns_cbranch_vccz 3008f\ns_add_u32 m0, s60, 98304\n"
"s_add_u32 s61, s94, 1327104\nbuffer_load_dwordx4 v250, s[48:51], s61 offen lds\ns_sub_u32 m0, m0, 4096\ns_nop 0\n"
"buffer_load_dwordx4 v221, s[48:51], s61 offen lds\ns_add_u32 m0, s60, 106496\ns_add_u32 s61, s94, 1548288\n"
"buffer_load_dwordx4 v250, s[48:51], s61 offen lds\ns_sub_u32 m0, m0, 4096\ns_nop 0\nbuffer_load_dwordx4 v221, s[48:51], s61 offen lds\n"
"s_add_u32 m0, s60, 114688\ns_add_u32 s61, s94, 1769472\nbuffer_load_dwordx4 v250, s[48:51], s61 offen lds\ns_sub_u32 m0, m0, 4096\ns_nop 0\n"
"buffer_load_dwordx4 v221, s[48:51], s61 offen lds\n3008:\nW 4\nM 144,96,176\nR 192,244,57344\nM 144,104,180\nR 194,244,57856\nM 148,100,176\n"
"R 196,244,61440\nM 148,108,180\nR 198,244,61952\nW 4\nM 152,96,184\nR 176,245,57344\nM 152,104,188\nR 178,245,57856\nM 156,100,184\nR 180,245,61440\n"
"M 156,108,188\nR 182,245,61952\nW 4\nM 144,112,192\nR 184,246,4096\nM 144,120,196\nR 186,246,4608\nM 148,116,192\nR 188,246,8192\nM 148,124,196\n"
"R 190,246,8704\nW 4\nM 152,112,176\nR 192,247,4096\nM 152,120,180\nR 194,247,4608\nM 156,116,176\nR 196,247,8192\nM 156,124,180\nR 198,247,8704\nW 4\n"
"M 144,128,184\nR 176,246,12288\nM 144,136,188\nR 178,246,12800\nM 148,132,184\nR 180,246,16384\nM 148,140,188\nR 182,246,16896\nW 4\nM 152,128,192\n"
"R 184,247,12288\nM 152,136,196\nR 186,247,12800\nM 156,132,192\nR 188,247,16384\nM 156,140,196\nR 190,247,16896\ns_cbranch_vccz 3009f\n"
"s_waitcnt vmcnt(6)\ns_barrier\n3009:\ns_add_u32 m0, s60, 24576\ns_add_u32 s61, s95, 0\nbuffer_load_dwordx4 v250, s[48:51], s61 offen lds\ns_nop 2\n"
"v_cvt_pk_bf16_f32 v208, v144, v148\nv_cvt_pk_bf16_f32 v209, v145, v149\nv_cvt_pk_bf16_f32 v210, v146, v150\ns_add_u32 m0, s60, 32768\n"
"s_add_u32 s61, s95, 221184\nbuffer_load_dwordx4 v250, s[48:51], s61 offen lds\nv_cvt_pk_bf16_f32 v211, v147, v151\nv_cvt_pk_bf16_f32 v212, v152, v156\n"
"v_cvt_pk_bf16_f32 v213, v153, v157\ns_add_u32 m0, s60, 40960\ns_add_u32 s61, s95, 442368\nbuffer_load_dwordx4 v250, s[48:51], s61 offen lds\n"
"v_cvt_pk_bf16_f32 v214, v154, v158\nv_cvt_pk_bf16_f32 v215, v155, v159\ns_mov_b32 s74, 5767168\n"
"buffer_store_dwordx4 v[208:211], v200, s[52:55], s74 offen sc1\nbuffer_store_dwordx4 v[212:215], v201, s[52:55], s74 offen sc1\n"
"s_mov_b64 s[56:57], s[88:89]\ns_movk_i32 s58, 0x480\ns_and_b32 s74, s60, 0x400\ns_add_u32 m0, s74, 122880\ns_nop 0\n"
"buffer_load_dwordx4 v224, s[56:59], s74 offen lds\ns_cbranch_vccnz 3010f\ns_waitcnt vmcnt(8)\ns_barrier\n3010:\nW 4\nZ 144,0,176\nR 192,246,20480\n"
"M 144,8,180\nR 194,246,20992\nZ 148,4,176\nR 196,246,24576\nM 148,12,180\nR 198,246,25088\nW 4\nZ 152,0,184\nR 176,247,20480\nM 152,8,188\n"
"R 178,247,20992\nZ 156,4,184\nR 180,247,24576\nM 156,12,188\nR 182,247,25088\nW 4\nM 144,16,192\nR 184,246,28672\nM 144,24,196\nR 186,246,29184\n"
"M 148,20,192\nR 188,246,32768\nM 148,28,196\nR 190,246,33280\nW 4\nM 152,16,176\nR 192,247,28672\nM 152,24,180\nR 194,247,29184\nM 156,20,176\n"
"R 196,247,32768\nM 156,28,180\nR 198,247,33280\nW 4\nM 144,32,184\nR 176,244,0\nM 144,40,188\nR 178,244,512\nM 148,36,184\nR 180,244,4096\n"
"M 148,44,188\nR 182,244,4608\nW 4\nM 152,32,192\nR 184,245,0\nM 152,40,196\nR 186,245,512\nM 156,36,192\nR 188,245,4096\nM 156,44,196\nR 190,245,4608\n"
"s_cbranch_vccz 3011f\ns_waitcnt vmcnt(8)\ns_barrier\n3011:\ns_add_u32 m0, s60, 49152\ns_add_u32 s61, s95, 663552\n"
"buffer_load_dwordx4 v250, s[48:51], s61 offen lds\ns_sub_u32 s74, s94, 2304\ns_lshr_b32 s74, s74, 1\ns_lshr_b32 s75, s71, 2\ns_lshl_b32 s75, s75, 5\n"
"s_add_u32 s74, s74, s75\nv_and_b32 v241, 15, v240\nv_add_u32 v241, s74, v241\nv_mul_u32_u24 v242, 0xe39, v241\nv_lshrrev_b32 v242, 16, v242\n"
"v_mul_u32_u24 v202, 18, v242\ns_add_u32 m0, s60, 57344\ns_add_u32 s61, s95, 884736\nbuffer_load_dwordx4 v250, s[48:51], s61 offen lds\n"
"v_sub_u32 v202, v241, v202\nv_and_b32 v243, 1, v242\nv_lshlrev_b32 v243, 1, v243\nv_sub_u32 v202, v202, v243\ns_add_u32 m0, s60, 65536\n"
"s_add_u32 s61, s95, 1105920\nbuffer_load_dwordx4 v250, s[48:51], s61 offen lds\nv_ashrrev_i32 v243, 31, v202\nv_and_b32 v243, 18, v243\n"
"v_add_lshl_u32 v202, v202, v243, 4\nv_mul_u32_u24 v242, 0x12000, v242\nv_add3_u32 v202, v202, v242, v206\ns_cbranch_vccnz 3012f\ns_waitcnt vmcnt(9)\n"
"s_barrier\n3012:\nW 4\nM 144,48,176\nR 192,244,8192\nM 144,56,180\nR 194,244,8704\nM 148,52,176\nR 196,244,12288\nM 148,60,180\nR 198,244,12800\nW 4\n"
"M 152,48,184\nR 176,245,8192\nM 152,56,188\nR 178,245,8704\nM 156,52,184\nR 180,245,12288\nM 156,60,188\nR 182,245,12800\nW 4\nM 144,64,192\n"
"R 184,244,16384\nM 144,72,196\nR 186,244,16896\nM 148,68,192\nR 188,244,20480\nM 148,76,196\nR 190,244,20992\nW 4\nM 152,64,176\nR 192,245,16384\n"
"M 152,72,180\nR 194,245,16896\nM 156,68,176\nR 196,245,20480\nM 156,76,180\nR 198,245,20992\nW 4\nM 144,80,184\nR 176,246,36864\nM 144,88,188\n"
"R 178,246,37376\nM 148,84,184\nR 180,246,40960\nM 148,92,188\nR 182,246,41472\nW 4\nM 152,80,192\nR 184,247,36864\nM 152,88,196\nR 186,247,37376\n"
"M 156,84,192\nR 188,247,40960\nM 156,92,196\nR 190,247,41472\ns_cbranch_vccz 3013f\ns_waitcnt vmcnt(3)\ns_barrier\n3013:\ns_add_u32 m0, s60, 73728\n"
"s_add_u32 s61, s95, 1327104\nbuffer_load_dwordx4 v250, s[48:51], s61 offen lds\nv_add_u32 v241, 16, v241\nv_mul_u32_u24 v242, 0xe39, v241\n"
"v_lshrrev_b32 v242, 16, v242\nv_mul_u32_u24 v203, 18, v242\ns_add_u32 m0, s60, 81920\ns_add_u32 s61, s95, 1548288\n"
"buffer_load_dwordx4 v250, s[48:51], s61 offen lds\nv_sub_u32 v203, v241, v203\nv_and_b32 v243, 1, v242\nv_lshlrev_b32 v243, 1, v243\n"
"v_sub_u32 v203, v203, v243\ns_add_u32 m0, s60, 90112\ns_add_u32 s61, s95, 1769472\nbuffer_load_dwordx4 v250, s[48:51], s61 offen lds\n"
"v_ashrrev_i32 v243, 31, v203\nv_and_b32 v243, 18, v243\nv_add_lshl_u32 v203, v203, v243, 4\nv_mul_u32_u24 v242, 0x12000, v242\n"
"v_add3_u32 v203, v203, v242, v206\ns_cbranch_vccnz 3014f\ns_waitcnt vmcnt(6)\ns_barrier\n3014:\nW 4\nM 144,96,176\nR 192,246,45056\nM 144,104,180\n"
"R 194,246,45568\nM 148,100,176\nR 196,246,49152\nM 148,108,180\nR 198,246,49664\nW 4\nM 152,96,184\nR 176,247,45056\nM 152,104,188\nR 178,247,45568\n"
"M 156,100,184\nR 180,247,49152\nM 156,108,188\nR 182,247,49664\nW 4\nM 144,112,192\nR 184,246,53248\nM 144,120,196\nR 186,246,53760\nM 148,116,192\n"
"R 188,246,57344\nM 148,124,196\nR 190,246,57856\nW 4\nM 152,112,176\nR 192,247,53248\nM 152,120,180\nR 194,247,53760\nM 156,116,176\nR 196,247,57344\n"
"M 156,124,180\nR 198,247,57856\nW 4\nM 144,128,184\nR 176,244,24576\nM 144,136,188\nR 178,244,25088\nM 148,132,184\nR 180,244,28672\nM 148,140,188\n"
"R 182,244,29184\nW 4\nM 152,128,192\nR 184,245,24576\nM 152,136,196\nR 186,245,25088\nM 156,132,192\nR 188,245,28672\nM 156,140,196\nR 190,245,29184\n"
"s_cbranch_vccz 3015f\ns_waitcnt vmcnt(4)\ns_barrier\n3015:\ns_add_u32 m0, s60, 0\ns_add_u32 s61, s62, 0\n"
"buffer_load_dwordx4 v249, s[40:43], s61 offen lds\ns_nop 2\nv_cvt_pk_bf16_f32 v208, v144, v148\nv_cvt_pk_bf16_f32 v209, v145, v149\n"
"v_cvt_pk_bf16_f32 v210, v146, v150\ns_add_u32 m0, s60, 8192\ns_add_u32 s61, s62, 589824\nbuffer_load_dwordx4 v249, s[40:43], s61 offen lds\n"
"v_cvt_pk_bf16_f32 v211, v147, v151\nv_cvt_pk_bf16_f32 v212, v152, v156\nv_cvt_pk_bf16_f32 v213, v153, v157\ns_add_u32 m0, s60, 16384\n"
"s_add_u32 s61, s62, 1179648\nbuffer_load_dwordx4 v249, s[40:43], s61 offen lds\nv_cvt_pk_bf16_f32 v214, v154, v158\nv_cvt_pk_bf16_f32 v215, v155, v159\n"
"s_mov_b32 s74, 5767168\nbuffer_store_dwordx4 v[208:211], v202, s[52:55], s74 offen sc1\nbuffer_store_dwordx4 v[212:215], v203, s[52:55], s74 offen sc1\n"
"s_cbranch_vccnz 3016f\ns_waitcnt vmcnt(9)\ns_barrier\n3016:\nW 4\nZ 144,0,176\nR 192,244,32768\nM 144,8,180\nR 194,244,33280\nZ 148,4,176\n"
"R 196,244,36864\nM 148,12,180\nR 198,244,37376\nW 4\nZ 152,0,184\nR 176,245,32768\nM 152,8,188\nR 178,245,33280\nZ 156,4,184\nR 180,245,36864\n"
"M 156,12,188\nR 182,245,37376\nW 4\nM 144,16,192\nR 184,244,40960\nM 144,24,196\nR 186,244,41472\nM 148,20,192\nR 188,244,45056\nM 148,28,196\n"
"R 190,244,45568\nW 4\nM 152,16,176\nR 192,245,40960\nM 152,24,180\nR 194,245,41472\nM 156,20,176\nR 196,245,45056\nM 156,28,180\nR 198,245,45568\nW 4\n"
"M 144,32,184\nR 176,244,49152\nM 144,40,188\nR 178,244,49664\nM 148,36,184\nR 180,244,53248\nM 148,44,188\nR 182,244,53760\nW 4\nM 152,32,192\n"
"R 184,245,49152\nM 152,40,196\nR 186,245,49664\nM 156,36,192\nR 188,245,53248\nM 156,44,196\nR 190,245,53760\ns_cbranch_vccz 3017f\ns_waitcnt vmcnt(6)\n"
"s_barrier\n3017:\ns_add_u32 m0, s60, 98304\ns_add_u32 s61, s62, 1769472\nbuffer_load_dwordx4 v249, s[40:43], s61 offen lds\ns_sub_u32 s74, s95, 2304\n"
"s_lshr_b32 s74, s74, 1\ns_lshr_b32 s75, s71, 2\ns_lshl_b32 s75, s75, 5\ns_add_u32 s74, s74, s75\nv_and_b32 v241, 15, v240\nv_add_u32 v241, s74, v241\n"
"v_mul_u32_u24 v242, 0xe39, v241\nv_lshrrev_b32 v242, 16, v242\nv_mul_u32_u24 v204, 18, v242\ns_add_u32 m0, s60, 106496\ns_add_u32 s61, s62, 2359296\n"
"buffer_load_dwordx4 v249, s[40:43], s61 offen lds\nv_sub_u32 v204, v241, v204\nv_and_b32 v243, 1, v242\nv_lshlrev_b32 v243, 1, v243\n"
"v_sub_u32 v204, v204, v243\nv_ashrrev_i32 v243, 31, v204\ns_add_u32 m0, s60, 114688\ns_add_u32 s61, s62, 2949120\n"
"buffer_load_dwordx4 v249, s[40:43], s61 offen lds\nv_and_b32 v243, 18, v243\nv_add_lshl_u32 v204, v204, v243, 4\nv_mul_u32_u24 v242, 0x12000, v242\n"
"v_add3_u32 v204, v204, v242, v206\ns_cbranch_vccnz 3018f\nv_lshrrev_b32 v227, 4, v224\nv_and_b32 v227, 0x30, v227\nv_add_u32 v227, 122880, v227\n"
"ds_read_b128 v[208:211], v227 offset:0\nds_read_b128 v[212:215], v227 offset:64\nds_read_b128 v[216:219], v227 offset:128\n"
"ds_read_b128 v[220:223], v227 offset:192\nW 3\nZ 160,208,0\nZ 164,208,4\nds_read_b128 v[208:211], v227 offset:256\nW 3\nM 160,212,8\nM 164,212,12\n"
"ds_read_b128 v[212:215], v227 offset:320\nW 3\nM 160,216,16\nM 164,216,20\nds_read_b128 v[216:219], v227 offset:384\nW 3\nM 160,220,24\nM 164,220,28\n"
"ds_read_b128 v[220:223], v227 offset:448\nW 3\nM 160,208,32\nM 164,208,36\nds_read_b128 v[208:211], v227 offset:512\nW 3\nM 160,212,40\nM 164,212,44\n"
"ds_read_b128 v[212:215], v227 offset:576\nW 3\nM 160,216,48\nM 164,216,52\nds_read_b128 v[216:219], v227 offset:640\nW 3\nM 160,220,56\nM 164,220,60\n"
"ds_read_b128 v[220:223], v227 offset:704\nW 3\nM 160,208,64\nM 164,208,68\nds_read_b128 v[208:211], v227 offset:768\nW 3\nM 160,212,72\nM 164,212,76\n"
"ds_read_b128 v[212:215], v227 offset:832\nW 3\nM 160,216,80\nM 164,216,84\nds_read_b128 v[216:219], v227 offset:896\nW 3\nM 160,220,88\nM 164,220,92\n"
"ds_read_b128 v[220:223], v227 offset:960\nW 3\nM 160,208,96\nM 164,208,100\nds_read_b128 v[208:211], v227 offset:1024\nW 3\nM 160,212,104\n"
"M 164,212,108\nds_read_b128 v[212:215], v227 offset:1088\nW 3\nM 160,216,112\nM 164,216,116\nW 2\nM 160,220,120\nM 164,220,124\nW 1\nM 160,208,128\n"
"M 164,208,132\nW 0\nM 160,212,136\nM 164,212,140\nv_and_b32 v227, 0x3fff, v253\nv_lshrrev_b32 v227, 1, v227\ns_mov_b32 s74, 12582912\ns_nop 4\n"
"v_mul_f32 v160, 0xbfb8aa3b, v160\nv_mul_f32 v161, 0xbfb8aa3b, v164\nE 160\nE 161\ns_nop 0\nv_add_f32 v160, 1.0, v160\nv_add_f32 v161, 1.0, v161\n"
"v_rcp_f32 v160, v160\nv_rcp_f32 v161, v161\ns_mov_b64 exec, 0xffff\nbuffer_store_dwordx2 v[160:161], v227, s[52:55], s74 offen sc1\ns_mov_b64 exec, -1\n"
"3018:\ns_cbranch_vccnz 3019f\ns_waitcnt vmcnt(10)\ns_barrier\n3019:\nW 4\nM 144,48,176\nR 192,244,57344\nM 144,56,180\nR 194,244,57856\nM 148,52,176\n"
"R 196,244,61440\nM 148,60,180\nR 198,244,61952\nW 4\nM 152,48,184\nR 176,245,57344\nM 152,56,188\nR 178,245,57856\nM 156,52,184\nR 180,245,61440\n"
"M 156,60,188\nR 182,245,61952\nW 4\nM 144,64,192\nR 184,246,4096\nM 144,72,196\nR 186,246,4608\nM 148,68,192\nR 188,246,8192\nM 148,76,196\n"
"R 190,246,8704\nW 4\nM 152,64,176\nR 192,247,4096\nM 152,72,180\nR 194,247,4608\nM 156,68,176\nR 196,247,8192\nM 156,76,180\nR 198,247,8704\nW 4\n"
"M 144,80,184\nR 176,246,12288\nM 144,88,188\nR 178,246,12800\nM 148,84,184\nR 180,246,16384\nM 148,92,188\nR 182,246,16896\nW 4\nM 152,80,192\n"
"R 184,247,12288\nM 152,88,196\nR 186,247,12800\nM 156,84,192\nR 188,247,16384\nM 156,92,196\nR 190,247,16896\ns_cbranch_vccz 3020f\ns_waitcnt vmcnt(6)\n"
"s_barrier\n3020:\ns_add_u32 m0, s60, 24576\ns_add_u32 s61, s62, 3538944\nbuffer_load_dwordx4 v249, s[40:43], s61 offen lds\nv_add_u32 v241, 16, v241\n"
"v_mul_u32_u24 v242, 0xe39, v241\nv_lshrrev_b32 v242, 16, v242\nv_mul_u32_u24 v205, 18, v242\ns_add_u32 m0, s60, 32768\ns_add_u32 s61, s62, 4128768\n"
"buffer_load_dwordx4 v249, s[40:43], s61 offen lds\nv_sub_u32 v205, v241, v205\nv_and_b32 v243, 1, v242\nv_lshlrev_b32 v243, 1, v243\n"
"v_sub_u32 v205, v205, v243\ns_add_u32 m0, s60, 40960\ns_add_u32 s61, s62, 4718592\nbuffer_load_dwordx4 v249, s[40:43], s61 offen lds\n"
"v_ashrrev_i32 v243, 31, v205\nv_and_b32 v243, 18, v243\nv_add_lshl_u32 v205, v205, v243, 4\nv_mul_u32_u24 v242, 0x12000, v242\n"
"v_add3_u32 v205, v205, v242, v206\ns_cbranch_vccnz 3021f\ns_waitcnt vmcnt(10)\ns_barrier\n3021:\nW 4\nM 144,96,176\nR 192,246,20480\nM 144,104,180\n"
"R 194,246,20992\nM 148,100,176\nR 196,246,24576\nM 148,108,180\nR 198,246,25088\nW 4\nM 152,96,184\nR 176,247,20480\nM 152,104,188\nR 178,247,20992\n"
"M 156,100,184\nR 180,247,24576\nM 156,108,188\nR 182,247,25088\nW 4\nM 144,112,192\nR 184,246,28672\nM 144,120,196\nR 186,246,29184\nM 148,116,192\n"
"R 188,246,32768\nM 148,124,196\nR 190,246,33280\nW 4\nM 152,112,176\nR 192,247,28672\nM 152,120,180\nR 194,247,29184\nM 156,116,176\nR 196,247,32768\n"
"M 156,124,180\nR 198,247,33280\nW 4\nM 144,128,184\nR 176,244,0\nM 144,136,188\nR 178,244,512\nM 148,132,184\nR 180,244,4096\nM 148,140,188\n"
"R 182,244,4608\nW 4\nM 152,128,192\nR 184,245,0\nM 152,136,196\nR 186,245,512\nM 156,132,192\nR 188,245,4096\nM 156,140,196\nR 190,245,4608\n"
"s_cbranch_vccz 3022f\ns_waitcnt vmcnt(4)\ns_barrier\n3022:\ns_add_u32 m0, s60, 49152\ns_add_u32 s61, s62, 0\n"
"buffer_load_dwordx4 v249, s[44:47], s61 offen lds\ns_nop 2\nv_cvt_pk_bf16_f32 v208, v144, v148\nv_cvt_pk_bf16_f32 v209, v145, v149\n"
"v_cvt_pk_bf16_f32 v210, v146, v150\ns_add_u32 m0, s60, 57344\ns_add_u32 s61, s62, 589824\nbuffer_load_dwordx4 v249, s[44:47], s61 offen lds\n"
"v_cvt_pk_bf16_f32 v211, v147, v151\nv_cvt_pk_bf16_f32 v212, v152, v156\nv_cvt_pk_bf16_f32 v213, v153, v157\ns_add_u32 m0, s60, 65536\n"
"s_add_u32 s61, s62, 1179648\nbuffer_load_dwordx4 v249, s[44:47], s61 offen lds\nv_cvt_pk_bf16_f32 v214, v154, v158\nv_cvt_pk_bf16_f32 v215, v155, v159\n"
"s_mov_b32 s74, 5767168\nbuffer_store_dwordx4 v[208:211], v204, s[52:55], s74 offen sc1\nbuffer_store_dwordx4 v[212:215], v205, s[52:55], s74 offen sc1\n"
"s_cbranch_vccnz 3023f\ns_waitcnt vmcnt(10)\ns_barrier\n3023:\nW 4\nZ 144,176,0\nR 192,244,8192\nM 144,180,8\nR 194,244,8704\nZ 148,176,4\n"
"R 196,244,12288\nM 148,180,12\nR 198,244,12800\nW 4\nZ 152,184,0\nR 176,245,8192\nM 152,188,8\nR 178,245,8704\nZ 156,184,4\nR 180,245,12288\n"
"M 156,188,12\nR 182,245,12800\nW 4\nM 144,192,16\nR 184,244,16384\nM 144,196,24\nR 186,244,16896\nM 148,192,20\nR 188,244,20480\nM 148,196,28\n"
"R 190,244,20992\nW 4\nM 152,176,16\nR 192,245,16384\nM 152,180,24\nR 194,245,16896\nM 156,176,20\nR 196,245,20480\nM 156,180,28\nR 198,245,20992\nW 4\n"
"M 144,184,32\nR 176,246,36864\nM 144,188,40\nR 178,246,37376\nM 148,184,36\nR 180,246,40960\nM 148,188,44\nR 182,246,41472\nW 4\nM 152,192,32\n"
"R 184,247,36864\nM 152,196,40\nR 186,247,37376\nM 156,192,36\nR 188,247,40960\nM 156,196,44\nR 190,247,41472\ns_cbranch_vccz 3024f\ns_waitcnt vmcnt(0)\n"
"s_barrier\ns_cmp_lg_u32 s71, 4\ns_cbranch_scc1 2999f\ns_mov_b64 exec, 1\nv_mov_b32 v254, s72\nbuffer_store_dword v254, off, s[52:55], s73 sc1\n"
"s_mov_b64 exec, -1\n2999:\n3024:\ns_add_u32 m0, s60, 73728\ns_add_u32 s61, s62, 1769472\nbuffer_load_dwordx4 v249, s[44:47], s61 offen lds\n"
"s_and_b32 s69, s84, 7\ns_lshl_b32 s69, s69, 1\ns_lshr_b32 s74, s84, 7\ns_add_u32 s69, s69, s74\ns_mul_i32 s66, s69, 147456\n"
"s_add_u32 s66, s66, 1114112\ns_bfe_u32 s69, s84, 0x10006\ns_mul_i32 s65, s69, 81920\ns_add_u32 s65, s65, s66\ns_sub_u32 s65, s65, 65536\n"
"s_bfe_u32 s74, s84, 0x20004\ns_lshl_b32 s74, s74, 12\ns_lshl_b32 s75, s71, 9\ns_add_u32 s74, s74, s75\nv_lshrrev_b32 v226, 1, v224\n"
"v_and_b32 v226, 0xf8, v226\nv_add_u32 v226, s74, v226\nv_and_b32 v225, 0x200, v224\nv_lshlrev_b32 v225, 6, v225\nv_add_u32 v225, v225, v226\n"
"v_lshrrev_b32 v208, 4, v224\nv_and_b32 v209, 31, v208\nv_lshrrev_b32 v210, 5, v208\nv_lshlrev_b32 v227, 13, v210\nv_lshl_add_u32 v227, v209, 3, v227\n"
"s_add_u32 m0, s60, 81920\ns_add_u32 s61, s62, 2359296\nbuffer_load_dwordx4 v249, s[44:47], s61 offen lds\ns_lshl_b32 s74, s69, 12\n"
"s_add_u32 s74, s74, 122880\nv_add_u32 v227, s74, v227\nv_lshlrev_b32 v228, 3, v209\ns_lshl_b32 s74, s69, 14\ns_sub_u32 s74, 139264, s74\n"
"v_add_u32 v228, s74, v228\nv_mul_u32_u24 v229, 288, v210\nv_lshl_add_u32 v229, v209, 4, v229\nv_add_u32 v229, 143360, v229\nv_and_b32 v211, 1, v209\n"
"s_add_u32 m0, s60, 90112\ns_add_u32 s61, s62, 2949120\nbuffer_load_dwordx4 v249, s[44:47], s61 offen lds\nv_lshrrev_b32 v212, 1, v209\n"
"v_lshl_add_u32 v211, v211, 4, v212\ns_bfe_u32 s74, s84, 0x20004\ns_lshl_b32 s74, s74, 9\ns_lshl_b32 s75, s71, 6\ns_add_u32 s74, s74, s75\n"
"v_add_u32 v211, s74, v211\nv_lshlrev_b32 v235, 15, v210\nv_lshl_add_u32 v235, v211, 4, v235\nv_lshlrev_b32 v236, 3, v211\ns_and_b32 s74, s84, 7\n"
"s_lshl_b32 s74, s74, 2\ns_lshr_b32 s75, s84, 6\ns_add_u32 s74, s74, s75\ns_lshl_b32 s75, s74, 16\nv_add_u32 v235, s75, v235\ns_lshr_b32 s74, s74, 1\n"
"s_lshl_b32 s74, s74, 14\ns_lshl_b32 s75, s69, 2\ns_add_u32 s74, s74, s75\nv_add_u32 v236, s74, v236\ns_cbranch_vccnz 3025f\ns_waitcnt vmcnt(3)\n"
"s_barrier\n3025:\nW 4\nM 144,176,48\nR 192,246,45056\nM 144,180,56\nR 194,246,45568\nM 148,176,52\nR 196,246,49152\nM 148,180,60\nR 198,246,49664\nW 4\n"
"M 152,184,48\nR 176,247,45056\nM 152,188,56\nR 178,247,45568\nM 156,184,52\nR 180,247,49152\nM 156,188,60\nR 182,247,49664\nW 4\nM 144,192,64\n"
"R 184,246,53248\nM 144,196,72\nR 186,246,53760\nM 148,192,68\nR 188,246,57344\nM 148,196,76\nR 190,246,57856\nW 4\nM 152,176,64\nR 192,247,53248\n"
"M 152,180,72\nR 194,247,53760\nM 156,176,68\nR 196,247,57344\nM 156,180,76\nR 198,247,57856\nW 4\nM 144,184,80\nR 176,244,24576\nM 144,188,88\n"
"R 178,244,25088\nM 148,184,84\nR 180,244,28672\nM 148,188,92\nR 182,244,29184\nW 4\nM 152,192,80\nR 184,245,24576\nM 152,196,88\nR 186,245,25088\n"
"M 156,192,84\nR 188,245,28672\nM 156,196,92\nR 190,245,29184\ns_cbranch_vccz 3026f\ns_waitcnt vmcnt(6)\ns_barrier\n3026:\ns_add_u32 m0, s60, 0\n"
"s_add_u32 s61, s62, 3538944\nbuffer_load_dwordx4 v249, s[44:47], s61 offen lds\ns_and_b32 s80, s84, 7\ns_lshl_b32 s80, s80, 1\ns_lshr_b32 s74, s84, 7\n"
"s_add_u32 s80, s80, s74\ns_bfe_u32 s81, s84, 0x30004\nv_lshrrev_b32 v211, 3, v208\ns_lshl_b32 s74, s71, 3\nv_add_u32 v212, s74, v211\n"
"v_mul_u32_u24 v230, 0x480, v212\nv_and_b32 v211, 6, v211\nv_and_b32 v212, 7, v208\nv_xor_b32 v211, v211, v212\ns_add_u32 m0, s60, 8192\n"
"s_add_u32 s61, s62, 4128768\nbuffer_load_dwordx4 v249, s[44:47], s61 offen lds\nv_lshl_add_u32 v230, v211, 4, v230\ns_lshl_b32 s74, s81, 7\n"
"v_add_u32 v230, s74, v230\nv_mul_u32_u24 v231, 0x480, v208\ns_lshl_b32 s74, s81, 4\ns_add_u32 s74, s74, 1024\nv_add_u32 v231, s74, v231\n"
"s_mov_b64 s[48:49], s[82:83]\ns_mov_b32 s50, 663552\ns_mov_b64 s[56:57], s[86:87]\ns_mov_b32 s58, 5308416\nv_lshlrev_b32 v234, 2, v224\n"
"s_and_b32 s74, s84, 1\ns_lshl_b32 s74, s74, 3\ns_bfe_u32 s64, s84, 0x30004\ns_or_b32 s74, s74, s64\ns_and_b32 s75, s84, 6\ns_lshr_b32 s64, s84, 7\n"
"s_add_u32 s75, s75, s64\ns_mul_i32 s67, s75, 663552\ns_and_b32 s64, s74, 7\ns_lshl_b32 s64, s64, 3\ns_add_u32 s64, s64, s71\ns_mul_i32 s64, s64, 10368\n"
"s_add_u32 s67, s67, s64\ns_lshl_b32 s90, s64, 3\ns_mul_i32 s64, s75, 1152\ns_add_u32 s90, s90, s64\ns_add_u32 s64, s76, -4\ns_add_u32 m0, s60, 16384\n"
"s_add_u32 s61, s62, 4718592\nbuffer_load_dwordx4 v249, s[44:47], s61 offen lds\nv_lshrrev_b32 v239, 2, v208\nv_mul_u32_u24 v239, 0x2400, v239\n"
"v_and_b32 v211, 3, v208\nv_add_u32 v211, s64, v211\nv_ashrrev_i32 v212, 31, v211\nv_and_b32 v212, 9, v212\nv_add_u32 v211, v211, v212\n"
"v_lshl_add_u32 v239, v211, 7, v239\ns_cbranch_vccnz 3027f\ns_waitcnt vmcnt(9)\ns_barrier\n3027:\nW 4\nM 144,176,96\nR 192,244,32768\nM 144,180,104\n"
"R 194,244,33280\nM 148,176,100\nR 196,244,36864\nM 148,180,108\nR 198,244,37376\nW 4\nM 152,184,96\nR 176,245,32768\nM 152,188,104\nR 178,245,33280\n"
"M 156,184,100\nR 180,245,36864\nM 156,188,108\nR 182,245,37376\nW 4\nM 144,192,112\nR 184,244,40960\nM 144,196,120\nR 186,244,41472\nM 148,192,116\n"
"R 188,244,45056\nM 148,196,124\nR 190,244,45568\nW 4\nM 152,176,112\nR 192,245,40960\nM 152,180,120\nR 194,245,41472\nM 156,176,116\nR 196,245,45056\n"
"M 156,180,124\nR 198,245,45568\nW 4\nM 144,184,128\nR 176,244,49152\nM 144,188,136\nR 178,244,49664\nM 148,184,132\nR 180,244,53248\nM 148,188,140\n"
"R 182,244,53760\nW 4\nM 152,192,128\nR 184,245,49152\nM 152,196,136\nR 186,245,49664\nM 156,192,132\nR 188,245,53248\nM 156,196,140\nR 190,245,53760\n"
"s_cbranch_vccz 3028f\ns_waitcnt vmcnt(4)\ns_waitcnt vmcnt(0)\ns_barrier\ns_cmp_lg_u32 s71, 5\ns_cbranch_scc1 2998f\nv_lshlrev_b32 v204, 2, v224\n"
"s_mov_b32 s74, 4096\ns_mov_b32 s75, 8192\ns_mov_b32 s85, 53248\ns_mov_b32 s91, 57344\nbuffer_load_dword v200, v204, s[52:55], s74 offen sc1\n"
"buffer_load_dword v201, v204, s[52:55], s75 offen sc1\n2998:\n3028:\ns_nop 5\nv_mul_f32 v240, v144, v144\nv_mul_f32 v241, v145, v145\n"
"v_mul_f32 v242, v146, v146\nv_mul_f32 v243, v147, v147\nv_fma_f32 v240, v240, s70, v248\nv_fma_f32 v241, v241, s70, v248\n"
"v_fma_f32 v242, v242, s70, v248\nv_fma_f32 v243, v243, s70, v248\nv_mul_f32 v240, v240, v144\nv_mul_f32 v241, v241, v145\nv_mul_f32 v242, v242, v146\n"
"v_mul_f32 v243, v243, v147\nE 240\nE 241\nE 242\nE 243\nv_add_f32 v240, 1.0, v240\nv_add_f32 v241, 1.0, v241\nv_add_f32 v242, 1.0, v242\n"
"v_add_f32 v243, 1.0, v243\nv_rcp_f32 v240, v240\nv_rcp_f32 v241, v241\nv_rcp_f32 v242, v242\ns_mov_b64 s[92:93], s[40:41]\n"
"s_mov_b64 s[94:95], s[42:43]\ns_mov_b64 s[96:97], s[44:45]\ns_mov_b64 s[98:99], s[46:47]\ns_and_b32 s62, s84, 7\ns_lshl_b32 s62, s62, 2\n"
"s_lshr_b32 s63, s84, 6\ns_add_u32 s62, s62, s63\ns_lshl_b32 s47, s71, 10\ns_mul_i32 s43, s62, 73728\ns_add_u32 s43, s43, s47\n"
"s_add_u32 s43, s43, 5771264\ns_lshr_b32 s63, s62, 1\ns_mul_i32 s63, s63, 9\ns_and_b32 s62, s62, 1\ns_lshl_b32 s62, s62, 2\ns_add_u32 s62, s62, s63\n"
"s_lshr_b32 s63, s71, 2\ns_add_u32 s62, s62, s63\ns_lshl_b32 s62, s62, 14\ns_and_b32 s63, s47, 0xc00\ns_add_u32 s62, s62, s63\n"
"s_add_u32 s40, s62, 3407872\ns_add_u32 s41, s40, 32768\ns_add_u32 s62, s40, 65536\ns_sub_u32 s63, s43, 8192\ns_cmp_lt_u32 s71, 4\n"
"s_cselect_b32 s42, s62, s63\ns_movk_i32 s62, 0x4800\ns_cselect_b32 s45, 0x1000, s62\ns_cmp_lt_u32 s71, 6\ns_cselect_b32 s62, 0x2000, 0\n"
"s_add_u32 s44, s43, s62\ns_add_u32 s46, s47, s62\ns_add_u32 s46, s46, 24576\ns_cbranch_vccnz 3029f\ns_waitcnt vmcnt(4)\ns_barrier\n3029:\nW 4\n"
"Z 160,176,0\nR 192,244,57344\nM 160,180,8\nR 194,244,57856\nZ 164,176,4\nR 196,244,61440\nM 164,180,12\nR 198,244,61952\nW 4\nZ 168,184,0\n"
"R 176,245,57344\nM 168,188,8\nR 178,245,57856\nZ 172,184,4\nR 180,245,61440\nM 172,188,12\nR 182,245,61952\nW 4\nM 160,192,16\nR 184,246,4096\n"
"M 160,196,24\nR 186,246,4608\nM 164,192,20\nR 188,246,8192\nM 164,196,28\nR 190,246,8704\nW 4\nM 168,176,16\nR 192,247,4096\nM 168,180,24\n"
"R 194,247,4608\nM 172,176,20\nR 196,247,8192\nM 172,180,28\nR 198,247,8704\nW 4\nM 160,184,32\nR 176,246,12288\nM 160,188,40\nR 178,246,12800\n"
"M 164,184,36\nR 180,246,16384\nM 164,188,44\nR 182,246,16896\nW 4\nM 168,192,32\nR 184,247,12288\nM 168,196,40\nR 186,247,12800\nM 172,192,36\n"
"R 188,247,16384\nM 172,196,44\nR 190,247,16896\ns_cbranch_vccz 3030f\ns_barrier\ns_cmp_lg_u32 s71, 5\ns_cbranch_scc1 2997f\ns_mov_b32 s61, 0x40000\n"
"2996:\ns_waitcnt vmcnt(0)\nv_xor_b32 v200, s72, v200\nv_xor_b32 v201, s72, v201\nv_or_b32 v200, v200, v201\nv_cmp_ne_u32 s[62:63], 0, v200\n"
"s_sub_u32 s61, s61, 1\ns_cmp_eq_u32 s61, 0\ns_cbranch_scc1 2995f\ns_cmp_eq_u64 s[62:63], 0\ns_cbranch_scc1 2995f\ns_sleep 1\n"
"buffer_load_dword v200, v204, s[52:55], s74 offen sc1\nbuffer_load_dword v201, v204, s[52:55], s75 offen sc1\ns_branch 2996b\n2995:\nbuffer_inv sc1\n"
"2997:\n3030:\nv_rcp_f32 v243, v243\nv_mul_f32 v144, v240, v144\nv_mul_f32 v145, v241, v145\nv_mul_f32 v146, v242, v146\nv_mul_f32 v147, v243, v147\n"
"v_mul_f32 v240, v148, v148\nv_mul_f32 v241, v149, v149\nv_mul_f32 v242, v150, v150\nv_mul_f32 v243, v151, v151\nv_fma_f32 v240, v240, s70, v248\n"
"v_fma_f32 v241, v241, s70, v248\nv_fma_f32 v242, v242, s70, v248\nv_fma_f32 v243, v243, s70, v248\nv_mul_f32 v240, v240, v148\n"
"v_mul_f32 v241, v241, v149\nv_mul_f32 v242, v242, v150\nv_mul_f32 v243, v243, v151\nE 240\nE 241\nE 242\nE 243\nv_add_f32 v240, 1.0, v240\n"
"v_add_f32 v241, 1.0, v241\ns_cbranch_vccnz 3031f\ns_waitcnt vmcnt(1)\ns_barrier\n3031:\nW 4\nM 160,176,48\nR 192,246,20480\nM 160,180,56\n"
"R 194,246,20992\nM 164,176,52\nR 196,246,24576\nM 164,180,60\nR 198,246,25088\nW 4\nM 168,184,48\nR 176,247,20480\nM 168,188,56\nR 178,247,20992\n"
"M 172,184,52\nR 180,247,24576\nM 172,188,60\nR 182,247,25088\nW 4\nM 160,192,64\nR 184,246,28672\nM 160,196,72\nR 186,246,29184\nM 164,192,68\n"
"R 188,246,32768\nM 164,196,76\nR 190,246,33280\nW 4\nM 168,176,64\nR 192,247,28672\nM 168,180,72\nR 194,247,29184\nM 172,176,68\nR 196,247,32768\n"
"M 172,180,76\nR 198,247,33280\nW 4\nM 160,184,80\nR 176,244,0\nM 160,188,88\nR 178,244,512\nM 164,184,84\nR 180,244,4096\nM 164,188,92\nR 182,244,4608\n"
"W 4\nM 168,192,80\nR 184,245,0\nM 168,196,88\nR 186,245,512\nM 172,192,84\nR 188,245,4096\nM 172,196,92\nR 190,245,4608\ns_cbranch_vccz 3032f\n"
"s_waitcnt vmcnt(0)\ns_barrier\n3032:\nv_add_f32 v242, 1.0, v242\nv_add_f32 v243, 1.0, v243\nv_rcp_f32 v240, v240\nv_rcp_f32 v241, v241\n"
"v_rcp_f32 v242, v242\nv_rcp_f32 v243, v243\nv_mul_f32 v148, v240, v148\nv_mul_f32 v149, v241, v149\nv_mul_f32 v150, v242, v150\n"
"v_mul_f32 v151, v243, v151\nv_mul_f32 v240, v152, v152\nv_mul_f32 v241, v153, v153\nv_mul_f32 v242, v154, v154\nv_mul_f32 v243, v155, v155\n"
"v_fma_f32 v240, v240, s70, v248\nv_fma_f32 v241, v241, s70, v248\nv_fma_f32 v242, v242, s70, v248\nv_fma_f32 v243, v243, s70, v248\n"
"v_mul_f32 v240, v240, v152\nv_mul_f32 v241, v241, v153\nv_mul_f32 v242, v242, v154\nv_mul_f32 v243, v243, v155\nv_mov_b32 v208, 0\n"
"s_mov_b32 exec_lo, 0x140000\ns_mov_b32 exec_hi, 0x140000\nv_mov_b32 v208, 0x3f803f80\ns_mov_b64 exec, -1\nv_mov_b32 v209, v208\n"
"v_mov_b64 v[210:211], v[208:209]\nv_mov_b64 v[212:213], v[208:209]\nv_mov_b64 v[214:215], v[208:209]\nv_mov_b64 v[216:217], v[208:209]\n"
"v_mov_b64 v[218:219], v[208:209]\nv_mov_b64 v[220:221], v[208:209]\nv_mov_b64 v[222:223], v[208:209]\nv_mov_b32 v205, 0xdddddddd\n"
"v_mov_b64 v[48:49], 0\nv_mov_b64 v[50:51], 0\nv_mov_b64 v[52:53], 0\nv_mov_b64 v[54:55], 0\nv_mov_b64 v[56:57], 0\nv_mov_b64 v[58:59], 0\n"
"v_mov_b64 v[60:61], 0\nv_mov_b64 v[62:63], 0\nv_mov_b64 v[64:65], 0\nv_mov_b64 v[66:67], 0\nv_mov_b64 v[68:69], 0\nv_mov_b64 v[70:71], 0\n"
"v_mov_b64 v[72:73], 0\nv_mov_b64 v[74:75], 0\nv_mov_b64 v[76:77], 0\nv_mov_b64 v[78:79], 0\nv_mov_b64 v[80:81], 0\nv_mov_b64 v[82:83], 0\n"
"v_mov_b64 v[84:85], 0\nv_mov_b64 v[86:87], 0\nv_mov_b64 v[88:89], 0\nv_mov_b64 v[90:91], 0\nv_mov_b64 v[92:93], 0\nv_mov_b64 v[94:95], 0\n"
"s_add_u32 s73, s73, 12288\ns_cbranch_vccnz 3033f\ns_waitcnt vmcnt(0)\ns_barrier\n3033:\nW 4\nM 160,176,96\nR 192,244,8192\nM 160,180,104\n"
"R 194,244,8704\nM 164,176,100\nR 196,244,12288\nM 164,180,108\nR 198,244,12800\nW 4\nM 168,184,96\nR 176,245,8192\nM 168,188,104\nR 178,245,8704\n"
"M 172,184,100\nR 180,245,12288\nM 172,188,108\nR 182,245,12800\nW 4\nM 160,192,112\nR 184,244,16384\nM 160,196,120\nR 186,244,16896\nM 164,192,116\n"
"R 188,244,20480\nM 164,196,124\nR 190,244,20992\nW 4\nM 168,176,112\nR 192,245,16384\nM 168,180,120\nR 194,245,16896\nM 172,176,116\nR 196,245,20480\n"
"M 172,180,124\nR 198,245,20992\nW 4\nM 160,184,128\nM 160,188,136\nM 164,184,132\nM 164,188,140\nW 0\nM 168,192,128\nM 168,196,136\nM 172,192,132\n"
"M 172,196,140\ns_cbranch_vccz 3034f\ns_barrier\n3034:\ns_nop 6\ns_add_u32 m0, s60, 122880\ns_nop 0\nbuffer_load_dwordx4 v224, s[52:55], s40 offen lds\n"
"s_add_u32 m0, s60, 131072\ns_nop 0\nbuffer_load_dwordx4 v224, s[52:55], s41 offen lds\ns_add_u32 m0, s60, 139264\ns_nop 0\n"
"buffer_load_dwordx4 v224, s[52:55], s42 offen lds\ns_add_u32 m0, s60, 147456\ns_nop 0\nbuffer_load_dwordx4 v224, s[52:55], s43 offen lds\n"
"s_add_u32 m0, s46, 122880\ns_nop 0\nbuffer_load_dwordx4 v224, s[52:55], s44 offen lds\ns_add_u32 s74, s65, 16384\n"
"buffer_load_dwordx2 v[128:129], v225, s[52:55], s65 offen offset:0\nbuffer_load_dwordx2 v[130:131], v225, s[52:55], s74 offen offset:0\n"
"buffer_load_dwordx2 v[136:137], v226, s[52:55], s66 offen offset:0\nbuffer_load_dwordx2 v[132:133], v225, s[52:55], s65 offen offset:256\n"
"buffer_load_dwordx2 v[134:135], v225, s[52:55], s74 offen offset:256\nbuffer_load_dwordx2 v[138:139], v226, s[52:55], s66 offen offset:256\n"
"s_cbranch_vccnz 3035f\ns_barrier\n3035:\n73:\nE 240\nE 241\nE 242\nE 243\nv_add_f32 v240, 1.0, v240\nv_add_f32 v241, 1.0, v241\n"
"v_add_f32 v242, 1.0, v242\nv_add_f32 v243, 1.0, v243\nv_rcp_f32 v240, v240\nv_rcp_f32 v241, v241\nv_rcp_f32 v242, v242\nv_rcp_f32 v243, v243\n"
"v_mul_f32 v152, v240, v152\nv_mul_f32 v153, v241, v153\nv_mul_f32 v154, v242, v154\nv_mul_f32 v155, v243, v155\nv_mul_f32 v240, v156, v156\n"
"v_mul_f32 v241, v157, v157\nv_mul_f32 v242, v158, v158\nv_mul_f32 v243, v159, v159\nv_fma_f32 v240, v240, s70, v248\nv_fma_f32 v241, v241, s70, v248\n"
"v_fma_f32 v242, v242, s70, v248\nv_fma_f32 v243, v243, s70, v248\nv_mul_f32 v240, v240, v156\nv_mul_f32 v241, v241, v157\nv_mul_f32 v242, v242, v158\n"
"v_mul_f32 v243, v243, v159\nE 240\nE 241\nE 242\nE 243\nv_add_f32 v240, 1.0, v240\nv_add_f32 v241, 1.0, v241\nv_add_f32 v242, 1.0, v242\n"
"v_add_f32 v243, 1.0, v243\nv_rcp_f32 v240, v240\nv_rcp_f32 v241, v241\nv_rcp_f32 v242, v242\nv_rcp_f32 v243, v243\nv_mul_f32 v156, v240, v156\n"
"v_mul_f32 v157, v241, v157\nv_mul_f32 v158, v242, v158\nv_mul_f32 v159, v243, v159\nv_mul_f32 v144, v144, v160\nv_mul_f32 v145, v145, v161\n"
"v_mul_f32 v146, v146, v162\nv_mul_f32 v147, v147, v163\nv_mul_f32 v148, v148, v164\nv_mul_f32 v149, v149, v165\nv_mul_f32 v150, v150, v166\n"
"v_mul_f32 v151, v151, v167\nv_cvt_pk_bf16_f32 v0, v144, v145\nv_cvt_pk_bf16_f32 v1, v146, v147\nv_cvt_pk_bf16_f32 v4, v148, v149\n"
"v_cvt_pk_bf16_f32 v5, v150, v151\nv_mul_f32 v152, v152, v168\nv_mul_f32 v153, v153, v169\nv_mul_f32 v154, v154, v170\nv_mul_f32 v155, v155, v171\n"
"v_mul_f32 v156, v156, v172\nv_mul_f32 v157, v157, v173\nv_mul_f32 v158, v158, v174\nv_mul_f32 v159, v159, v175\nv_cvt_pk_bf16_f32 v2, v152, v153\n"
"v_cvt_pk_bf16_f32 v3, v154, v155\nv_cvt_pk_bf16_f32 v6, v156, v157\nv_cvt_pk_bf16_f32 v7, v158, v159\n"
"buffer_store_dwordx4 v[0:3], v251, s[52:55], s68 offen sc1\nbuffer_store_dwordx4 v[4:7], v251, s[52:55], s68 offen offset:1024 sc1\n"
"s_mov_b32 s62, 0x3ffff\ns_mov_b32 s63, 0x3ffff\ns_mov_b32 s64, 3\ns_add_u32 s46, s46, 83968\ns_add_u32 s47, s47, 83968\ns_sub_u32 s74, s69, 1\n"
"s_not_b32 s75, s74\ns_waitcnt vmcnt(2)\ns_barrier\nds_read_b64 v[144:145], v227 offset:0\nds_read_b64 v[146:147], v227 offset:4096\n"
"ds_read_b64 v[152:153], v228 offset:0\ns_mov_b64 exec, s[62:63]\nds_read_b128 v[208:211], v229 offset:0\nds_read_b128 v[212:215], v229 offset:576\n"
"s_mov_b64 exec, -1\nds_read_b64 v[148:149], v227 offset:256\nds_read_b64 v[150:151], v227 offset:4352\nds_read_b64 v[154:155], v228 offset:256\n"
"s_mov_b32 exec_lo, 0xffffffff\ns_mov_b32 exec_hi, 15\nbuffer_load_dword v237, v239, s[92:95], s90 offen\n"
"buffer_load_dword v238, v239, s[96:99], s90 offen\ns_mov_b64 exec, -1\nv_and_b32 v136, s74, v136\nv_and_b32 v137, s75, v137\nv_and_b32 v138, s74, v138\n"
"v_and_b32 v139, s75, v139\ns_mov_b32 exec_lo, 0\nv_mov_b32 v136, 0\nv_mov_b32 v137, 0\nv_mov_b32 v138, 0\nv_mov_b32 v139, 0\ns_mov_b32 exec_lo, -1\n"
"W 5\nv_mfma_f32_32x32x8_bf16 v[0:15], v[152:153], v[136:137], 0\nv_mfma_f32_32x32x16_bf16 v[0:15], v[144:147], v[128:131], v[0:15]\n"
"v_mfma_f32_32x32x8_bf16 v[16:31], v[152:153], v[138:139], 0\nv_mfma_f32_32x32x16_bf16 v[16:31], v[144:147], v[132:135], v[16:31]\ns_nop 9\n"
"v_max3_f32 v206, v0, v1, v2\nv_max3_f32 v206, v206, v3, v4\nv_max3_f32 v206, v206, v5, v6\nv_max3_f32 v206, v206, v7, v8\n"
"v_max3_f32 v206, v206, v9, v10\nv_max3_f32 v206, v206, v11, v12\nv_max3_f32 v206, v206, v13, v14\nv_max_f32 v206, v206, v15\nv_mov_b32 v0, v206\n"
"s_nop 1\nv_permlane32_swap_b32 v0, v206\nv_max_f32 v206, v206, v0\nv_xor_b32 v96, 0x80000000, v206\nv_xor_b32 v97, 0x80000000, v206\n"
"v_xor_b32 v98, 0x80000000, v206\nv_xor_b32 v99, 0x80000000, v206\nv_xor_b32 v100, 0x80000000, v206\nv_xor_b32 v101, 0x80000000, v206\n"
"v_xor_b32 v102, 0x80000000, v206\nv_xor_b32 v103, 0x80000000, v206\nv_xor_b32 v104, 0x80000000, v206\nv_xor_b32 v105, 0x80000000, v206\n"
"v_xor_b32 v106, 0x80000000, v206\nv_xor_b32 v107, 0x80000000, v206\nv_xor_b32 v108, 0x80000000, v206\nv_xor_b32 v109, 0x80000000, v206\n"
"v_xor_b32 v110, 0x80000000, v206\nv_xor_b32 v111, 0x80000000, v206\nv_max3_f32 v207, v16, v17, v18\nv_max3_f32 v207, v207, v19, v20\n"
"v_max3_f32 v207, v207, v21, v22\nv_max3_f32 v207, v207, v23, v24\nv_max3_f32 v207, v207, v25, v26\nv_max3_f32 v207, v207, v27, v28\n"
"v_max3_f32 v207, v207, v29, v30\nv_max_f32 v207, v207, v31\nv_mov_b32 v16, v207\ns_nop 1\nv_permlane32_swap_b32 v16, v207\nv_max_f32 v207, v207, v16\n"
"v_xor_b32 v112, 0x80000000, v207\nv_xor_b32 v113, 0x80000000, v207\nv_xor_b32 v114, 0x80000000, v207\nv_xor_b32 v115, 0x80000000, v207\n"
"v_xor_b32 v116, 0x80000000, v207\nv_xor_b32 v117, 0x80000000, v207\nv_xor_b32 v118, 0x80000000, v207\nv_xor_b32 v119, 0x80000000, v207\n"
"v_xor_b32 v120, 0x80000000, v207\nv_xor_b32 v121, 0x80000000, v207\nv_xor_b32 v122, 0x80000000, v207\nv_xor_b32 v123, 0x80000000, v207\n"
"v_xor_b32 v124, 0x80000000, v207\nv_xor_b32 v125, 0x80000000, v207\nv_xor_b32 v126, 0x80000000, v207\nv_xor_b32 v127, 0x80000000, v207\ns_nop 1\n"
"v_mfma_f32_32x32x8_bf16 v[0:15], v[152:153], v[136:137], v[96:111]\nv_mfma_f32_32x32x16_bf16 v[0:15], v[144:147], v[128:131], v[0:15]\ns_nop 10\n1:\n"
"s_setprio 3\nv_mfma_f32_32x32x8_bf16 v[16:31], v[152:153], v[138:139], v[112:127]\nE 0\nE 1\nE 2\nE 3\nE 4\n"
"v_mfma_f32_32x32x16_bf16 v[16:31], v[144:147], v[132:135], v[16:31]\nds_read_b64 v[144:145], v227 offset:512\nds_read_b64 v[146:147], v227 offset:4608\n"
"ds_read_b64 v[152:153], v228 offset:512\nE 5\nE 6\nE 7\nv_smfmac_f32_32x32x32_bf16 v[80:95], v[220:223], v[56:63], v205\ns_mov_b64 exec, s[62:63]\n"
"ds_read_b128 v[216:219], v229 offset:1152\nds_read_b128 v[220:223], v229 offset:1728\ns_mov_b64 exec, -1\nE 8\nE 9\nE 10\nE 11\nW 5\n"
"v_smfmac_f32_32x32x32_bf16 v[64:79], v[208:211], v[0:7], v205\nE 12\nE 13\nE 14\nE 15\ns_setprio 2\n"
"v_mfma_f32_32x32x8_bf16 v[32:47], v[154:155], v[136:137], v[96:111]\nE 16\nE 17\nE 18\nE 19\nE 20\n"
"v_mfma_f32_32x32x16_bf16 v[32:47], v[148:151], v[128:131], v[32:47]\ns_add_u32 m0, s47, 0\ns_add_u32 s40, s40, 4096\n"
"buffer_load_dwordx4 v224, s[52:55], s40 offen lds\nE 21\nE 22\nE 23\nv_smfmac_f32_32x32x32_bf16 v[64:79], v[212:215], v[8:15], v205\nE 24\nE 25\nE 26\n"
"E 27\nv_smfmac_f32_32x32x32_bf16 v[80:95], v[208:211], v[16:23], v205\nE 28\nE 29\nE 30\nE 31\ns_setprio 1\n"
"v_mfma_f32_32x32x8_bf16 v[48:63], v[154:155], v[138:139], v[112:127]\nE 32\nE 33\nE 34\nE 35\nE 36\n"
"v_mfma_f32_32x32x16_bf16 v[48:63], v[148:151], v[132:135], v[48:63]\nds_read_b64 v[148:149], v227 offset:768\nds_read_b64 v[150:151], v227 offset:4864\n"
"ds_read_b64 v[154:155], v228 offset:768\nE 37\nE 38\nE 39\nv_smfmac_f32_32x32x32_bf16 v[80:95], v[212:215], v[24:31], v205\ns_mov_b64 exec, s[62:63]\n"
"ds_read_b128 v[208:211], v229 offset:2304\nds_read_b128 v[212:215], v229 offset:2880\ns_mov_b64 exec, -1\nE 40\nE 41\nE 42\nE 43\nW 5\n"
"v_smfmac_f32_32x32x32_bf16 v[64:79], v[216:219], v[32:39], v205\nE 44\nE 45\nE 46\nE 47\ns_setprio 0\n"
"v_mfma_f32_32x32x8_bf16 v[0:15], v[152:153], v[136:137], v[96:111]\nE 48\nE 49\nE 50\nE 51\nE 52\n"
"v_mfma_f32_32x32x16_bf16 v[0:15], v[144:147], v[128:131], v[0:15]\nE 53\nE 54\nE 55\nv_smfmac_f32_32x32x32_bf16 v[64:79], v[220:223], v[40:47], v205\n"
"E 56\nE 57\nE 58\nE 59\nv_smfmac_f32_32x32x32_bf16 v[80:95], v[216:219], v[48:55], v205\nE 60\nE 61\nE 62\nE 63\ns_setprio 3\n"
"v_mfma_f32_32x32x8_bf16 v[16:31], v[152:153], v[138:139], v[112:127]\nE 0\nE 1\nE 2\nE 3\nE 4\n"
"v_mfma_f32_32x32x16_bf16 v[16:31], v[144:147], v[132:135], v[16:31]\nds_read_b64 v[144:145], v227 offset:1024\n"
"ds_read_b64 v[146:147], v227 offset:5120\nds_read_b64 v[152:153], v228 offset:1024\nE 5\nE 6\nE 7\n"
"v_smfmac_f32_32x32x32_bf16 v[80:95], v[220:223], v[56:63], v205\ns_mov_b64 exec, s[62:63]\nds_read_b128 v[216:219], v229 offset:3456\n"
"ds_read_b128 v[220:223], v229 offset:4032\ns_mov_b64 exec, -1\nE 8\nE 9\nE 10\nE 11\nW 5\n"
"v_smfmac_f32_32x32x32_bf16 v[64:79], v[208:211], v[0:7], v205\nE 12\nE 13\nE 14\nE 15\ns_setprio 2\n"
"v_mfma_f32_32x32x8_bf16 v[32:47], v[154:155], v[136:137], v[96:111]\nE 16\nE 17\nE 18\nE 19\nE 20\n"
"v_mfma_f32_32x32x16_bf16 v[32:47], v[148:151], v[128:131], v[32:47]\ns_add_u32 m0, s47, 8192\ns_add_u32 s41, s41, 4096\n"
"buffer_load_dwordx4 v224, s[52:55], s41 offen lds\nE 21\nE 22\nE 23\nv_smfmac_f32_32x32x32_bf16 v[64:79], v[212:215], v[8:15], v205\nE 24\nE 25\nE 26\n"
"E 27\nv_smfmac_f32_32x32x32_bf16 v[80:95], v[208:211], v[16:23], v205\nE 28\nE 29\nE 30\nE 31\ns_setprio 1\n"
"v_mfma_f32_32x32x8_bf16 v[48:63], v[154:155], v[138:139], v[112:127]\nE 32\nE 33\nE 34\nE 35\nE 36\n"
"v_mfma_f32_32x32x16_bf16 v[48:63], v[148:151], v[132:135], v[48:63]\nds_read_b64 v[148:149], v227 offset:1280\n"
"ds_read_b64 v[150:151], v227 offset:5376\nds_read_b64 v[154:155], v228 offset:1280\nE 37\nE 38\nE 39\n"
"v_smfmac_f32_32x32x32_bf16 v[80:95], v[212:215], v[24:31], v205\ns_mov_b64 exec, s[62:63]\nds_read_b128 v[208:211], v229 offset:4608\n"
"ds_read_b128 v[212:215], v229 offset:5184\ns_mov_b64 exec, -1\nE 40\nE 41\nE 42\nE 43\nW 5\n"
"v_smfmac_f32_32x32x32_bf16 v[64:79], v[216:219], v[32:39], v205\nE 44\nE 45\nE 46\nE 47\ns_setprio 0\n"
"v_mfma_f32_32x32x8_bf16 v[0:15], v[152:153], v[136:137], v[96:111]\nE 48\nE 49\nE 50\nE 51\nE 52\n"
"v_mfma_f32_32x32x16_bf16 v[0:15], v[144:147], v[128:131], v[0:15]\nE 53\nE 54\nE 55\nv_smfmac_f32_32x32x32_bf16 v[64:79], v[220:223], v[40:47], v205\n"
"E 56\nE 57\nE 58\nE 59\nv_smfmac_f32_32x32x32_bf16 v[80:95], v[216:219], v[48:55], v205\nE 60\nE 61\nE 62\nE 63\ns_setprio 3\n"
"v_mfma_f32_32x32x8_bf16 v[16:31], v[152:153], v[138:139], v[112:127]\nE 0\nE 1\nE 2\nE 3\nE 4\n"
"v_mfma_f32_32x32x16_bf16 v[16:31], v[144:147], v[132:135], v[16:31]\nds_read_b64 v[144:145], v227 offset:1536\n"
"ds_read_b64 v[146:147], v227 offset:5632\nds_read_b64 v[152:153], v228 offset:1536\nE 5\nE 6\nE 7\n"
"v_smfmac_f32_32x32x32_bf16 v[80:95], v[220:223], v[56:63], v205\ns_mov_b64 exec, s[62:63]\nds_read_b128 v[216:219], v229 offset:5760\n"
"ds_read_b128 v[220:223], v229 offset:6336\ns_mov_b64 exec, -1\nE 8\nE 9\nE 10\nE 11\nW 5\n"
"v_smfmac_f32_32x32x32_bf16 v[64:79], v[208:211], v[0:7], v205\nE 12\nE 13\nE 14\nE 15\ns_setprio 2\n"
"v_mfma_f32_32x32x8_bf16 v[32:47], v[154:155], v[136:137], v[96:111]\nE 16\nE 17\nE 18\nE 19\nE 20\n"
"v_mfma_f32_32x32x16_bf16 v[32:47], v[148:151], v[128:131], v[32:47]\ns_add_u32 m0, s47, 16384\ns_add_u32 s42, s42, s45\n"
"buffer_load_dwordx4 v224, s[52:55], s42 offen lds\nE 21\nE 22\nE 23\nv_smfmac_f32_32x32x32_bf16 v[64:79], v[212:215], v[8:15], v205\nE 24\nE 25\nE 26\n"
"E 27\nv_smfmac_f32_32x32x32_bf16 v[80:95], v[208:211], v[16:23], v205\nE 28\nE 29\nE 30\nE 31\ns_setprio 1\n"
"v_mfma_f32_32x32x8_bf16 v[48:63], v[154:155], v[138:139], v[112:127]\nE 32\nE 33\nE 34\nE 35\nE 36\n"
"v_mfma_f32_32x32x16_bf16 v[48:63], v[148:151], v[132:135], v[48:63]\nds_read_b64 v[148:149], v227 offset:1792\n"
"ds_read_b64 v[150:151], v227 offset:5888\nds_read_b64 v[154:155], v228 offset:1792\nE 37\nE 38\nE 39\n"
"v_smfmac_f32_32x32x32_bf16 v[80:95], v[212:215], v[24:31], v205\ns_mov_b64 exec, s[62:63]\nds_read_b128 v[208:211], v229 offset:6912\n"
"ds_read_b128 v[212:215], v229 offset:7488\ns_mov_b64 exec, -1\nE 40\nE 41\nE 42\nE 43\nW 5\n"
"v_smfmac_f32_32x32x32_bf16 v[64:79], v[216:219], v[32:39], v205\nE 44\nE 45\nE 46\nE 47\ns_setprio 0\n"
"v_mfma_f32_32x32x8_bf16 v[0:15], v[152:153], v[136:137], v[96:111]\nE 48\nE 49\nE 50\nE 51\nE 52\n"
"v_mfma_f32_32x32x16_bf16 v[0:15], v[144:147], v[128:131], v[0:15]\nE 53\nE 54\nE 55\nv_smfmac_f32_32x32x32_bf16 v[64:79], v[220:223], v[40:47], v205\n"
"E 56\nE 57\nE 58\nE 59\nv_smfmac_f32_32x32x32_bf16 v[80:95], v[216:219], v[48:55], v205\nE 60\nE 61\nE 62\nE 63\ns_setprio 3\n"
"v_mfma_f32_32x32x8_bf16 v[16:31], v[152:153], v[138:139], v[112:127]\nE 0\nE 1\nE 2\nE 3\nE 4\n"
"v_mfma_f32_32x32x16_bf16 v[16:31], v[144:147], v[132:135], v[16:31]\nds_read_b64 v[144:145], v227 offset:2048\n"
"ds_read_b64 v[146:147], v227 offset:6144\nds_read_b64 v[152:153], v228 offset:2048\nE 5\nE 6\nE 7\n"
"v_smfmac_f32_32x32x32_bf16 v[80:95], v[220:223], v[56:63], v205\ns_mov_b64 exec, s[62:63]\nds_read_b128 v[216:219], v229 offset:8064\n"
"ds_read_b128 v[220:223], v229 offset:8640\ns_mov_b64 exec, -1\nE 8\nE 9\nE 10\nE 11\nW 5\n"
"v_smfmac_f32_32x32x32_bf16 v[64:79], v[208:211], v[0:7], v205\nE 12\nE 13\nE 14\nE 15\ns_setprio 2\n"
"v_mfma_f32_32x32x8_bf16 v[32:47], v[154:155], v[136:137], v[96:111]\nE 16\nE 17\nE 18\nE 19\nE 20\n"
"v_mfma_f32_32x32x16_bf16 v[32:47], v[148:151], v[128:131], v[32:47]\ns_add_u32 m0, s47, 24576\ns_add_u32 s43, s43, 18432\n"
"buffer_load_dwordx4 v224, s[52:55], s43 offen lds\nE 21\nE 22\nE 23\nv_smfmac_f32_32x32x32_bf16 v[64:79], v[212:215], v[8:15], v205\nE 24\nE 25\nE 26\n"
"E 27\nv_smfmac_f32_32x32x32_bf16 v[80:95], v[208:211], v[16:23], v205\nE 28\nE 29\nE 30\nE 31\ns_setprio 1\n"
"v_mfma_f32_32x32x8_bf16 v[48:63], v[154:155], v[138:139], v[112:127]\nE 32\nE 33\nE 34\nE 35\nE 36\n"
"v_mfma_f32_32x32x16_bf16 v[48:63], v[148:151], v[132:135], v[48:63]\nds_read_b64 v[148:149], v227 offset:2304\n"
"ds_read_b64 v[150:151], v227 offset:6400\nds_read_b64 v[154:155], v228 offset:2304\nE 37\nE 38\nE 39\n"
"v_smfmac_f32_32x32x32_bf16 v[80:95], v[212:215], v[24:31], v205\ns_mov_b64 exec, s[62:63]\nds_read_b128 v[208:211], v229 offset:9216\n"
"ds_read_b128 v[212:215], v229 offset:9792\ns_mov_b64 exec, -1\nE 40\nE 41\nE 42\nE 43\nW 5\n"
"v_smfmac_f32_32x32x32_bf16 v[64:79], v[216:219], v[32:39], v205\nE 44\nE 45\nE 46\nE 47\ns_setprio 0\n"
"v_mfma_f32_32x32x8_bf16 v[0:15], v[152:153], v[136:137], v[96:111]\nE 48\nE 49\nE 50\nE 51\nE 52\n"
"v_mfma_f32_32x32x16_bf16 v[0:15], v[144:147], v[128:131], v[0:15]\nE 53\nE 54\nE 55\nv_smfmac_f32_32x32x32_bf16 v[64:79], v[220:223], v[40:47], v205\n"
"E 56\nE 57\nE 58\nE 59\nv_smfmac_f32_32x32x32_bf16 v[80:95], v[216:219], v[48:55], v205\nE 60\nE 61\nE 62\nE 63\ns_setprio 3\n"
"v_mfma_f32_32x32x8_bf16 v[16:31], v[152:153], v[138:139], v[112:127]\nE 0\nE 1\nE 2\nE 3\nE 4\n"
"v_mfma_f32_32x32x16_bf16 v[16:31], v[144:147], v[132:135], v[16:31]\nds_read_b64 v[144:145], v227 offset:2560\n"
"ds_read_b64 v[146:147], v227 offset:6656\nds_read_b64 v[152:153], v228 offset:2560\nE 5\nE 6\nE 7\n"
"v_smfmac_f32_32x32x32_bf16 v[80:95], v[220:223], v[56:63], v205\ns_mov_b64 exec, s[62:63]\nds_read_b128 v[216:219], v229 offset:10368\n"
"ds_read_b128 v[220:223], v229 offset:10944\ns_mov_b64 exec, -1\nE 8\nE 9\nE 10\nE 11\nW 5\n"
"v_smfmac_f32_32x32x32_bf16 v[64:79], v[208:211], v[0:7], v205\nE 12\nE 13\nE 14\nE 15\ns_setprio 2\n"
"v_mfma_f32_32x32x8_bf16 v[32:47], v[154:155], v[136:137], v[96:111]\nE 16\nE 17\nE 18\nE 19\nE 20\n"
"v_mfma_f32_32x32x16_bf16 v[32:47], v[148:151], v[128:131], v[32:47]\ns_mov_b32 m0, s46\ns_add_u32 s44, s44, 18432\n"
"buffer_load_dwordx4 v224, s[52:55], s44 offen lds\nE 21\nE 22\nE 23\nv_smfmac_f32_32x32x32_bf16 v[64:79], v[212:215], v[8:15], v205\nE 24\nE 25\nE 26\n"
"E 27\nv_smfmac_f32_32x32x32_bf16 v[80:95], v[208:211], v[16:23], v205\nE 28\nE 29\nE 30\nE 31\ns_setprio 1\n"
"v_mfma_f32_32x32x8_bf16 v[48:63], v[154:155], v[138:139], v[112:127]\nE 32\nE 33\nE 34\nE 35\nE 36\n"
"v_mfma_f32_32x32x16_bf16 v[48:63], v[148:151], v[132:135], v[48:63]\nds_read_b64 v[148:149], v227 offset:2816\n"
"ds_read_b64 v[150:151], v227 offset:6912\nds_read_b64 v[154:155], v228 offset:2816\nE 37\nE 38\nE 39\n"
"v_smfmac_f32_32x32x32_bf16 v[80:95], v[212:215], v[24:31], v205\ns_mov_b64 exec, s[62:63]\nds_read_b128 v[208:211], v229 offset:11520\n"
"ds_read_b128 v[212:215], v229 offset:12096\ns_mov_b64 exec, -1\nE 40\nE 41\nE 42\nE 43\nW 5\n"
"v_smfmac_f32_32x32x32_bf16 v[64:79], v[216:219], v[32:39], v205\nE 44\nE 45\nE 46\nE 47\ns_setprio 0\n"
"v_mfma_f32_32x32x8_bf16 v[0:15], v[152:153], v[136:137], v[96:111]\nE 48\nE 49\nE 50\nE 51\nE 52\n"
"v_mfma_f32_32x32x16_bf16 v[0:15], v[144:147], v[128:131], v[0:15]\ns_add_u32 s65, s67, 0\nbuffer_load_dword v238, v234, s[56:59], s65 offen\nE 53\n"
"E 54\nE 55\nv_smfmac_f32_32x32x32_bf16 v[64:79], v[220:223], v[40:47], v205\nE 56\nE 57\nE 58\nE 59\n"
"v_smfmac_f32_32x32x32_bf16 v[80:95], v[216:219], v[48:55], v205\nE 60\nE 61\nE 62\nE 63\ns_setprio 3\n"
"v_mfma_f32_32x32x8_bf16 v[16:31], v[152:153], v[138:139], v[112:127]\nE 0\nE 1\nE 2\nE 3\nE 4\n"
"v_mfma_f32_32x32x16_bf16 v[16:31], v[144:147], v[132:135], v[16:31]\nds_read_b64 v[144:145], v227 offset:3072\n"
"ds_read_b64 v[146:147], v227 offset:7168\nds_read_b64 v[152:153], v228 offset:3072\nE 5\nE 6\nE 7\n"
"v_smfmac_f32_32x32x32_bf16 v[80:95], v[220:223], v[56:63], v205\ns_mov_b64 exec, s[62:63]\nds_read_b128 v[216:219], v229 offset:12672\n"
"ds_read_b128 v[220:223], v229 offset:13248\ns_mov_b64 exec, -1\nE 8\nE 9\nE 10\nE 11\nW 5\n"
"v_smfmac_f32_32x32x32_bf16 v[64:79], v[208:211], v[0:7], v205\nE 12\nE 13\nE 14\nE 15\ns_setprio 2\n"
"v_mfma_f32_32x32x8_bf16 v[32:47], v[154:155], v[136:137], v[96:111]\nE 16\nE 17\nE 18\nE 19\nE 20\n"
"v_mfma_f32_32x32x16_bf16 v[32:47], v[148:151], v[128:131], v[32:47]\ns_add_u32 s65, s67, 4096\nbuffer_load_dword v238, v234, s[56:59], s65 offen\nE 21\n"
"E 22\nE 23\nv_smfmac_f32_32x32x32_bf16 v[64:79], v[212:215], v[8:15], v205\nE 24\nE 25\nE 26\nE 27\n"
"v_smfmac_f32_32x32x32_bf16 v[80:95], v[208:211], v[16:23], v205\nE 28\nE 29\nE 30\nE 31\ns_setprio 1\n"
"v_mfma_f32_32x32x8_bf16 v[48:63], v[154:155], v[138:139], v[112:127]\nE 32\nE 33\nE 34\nE 35\nE 36\n"
"v_mfma_f32_32x32x16_bf16 v[48:63], v[148:151], v[132:135], v[48:63]\nds_read_b64 v[148:149], v227 offset:3328\n"
"ds_read_b64 v[150:151], v227 offset:7424\nds_read_b64 v[154:155], v228 offset:3328\nE 37\nE 38\nE 39\n"
"v_smfmac_f32_32x32x32_bf16 v[80:95], v[212:215], v[24:31], v205\ns_mov_b64 exec, s[62:63]\nds_read_b128 v[208:211], v229 offset:13824\n"
"ds_read_b128 v[212:215], v229 offset:14400\ns_mov_b64 exec, -1\nE 40\nE 41\nE 42\nE 43\nW 5\n"
"v_smfmac_f32_32x32x32_bf16 v[64:79], v[216:219], v[32:39], v205\nE 44\nE 45\nE 46\nE 47\ns_setprio 0\n"
"v_mfma_f32_32x32x8_bf16 v[0:15], v[152:153], v[136:137], v[96:111]\nE 48\nE 49\nE 50\nE 51\nE 52\n"
"v_mfma_f32_32x32x16_bf16 v[0:15], v[144:147], v[128:131], v[0:15]\ns_add_u32 s65, s67, 8192\nbuffer_load_dword v238, v234, s[56:59], s65 offen\nE 53\n"
"E 54\nE 55\nv_smfmac_f32_32x32x32_bf16 v[64:79], v[220:223], v[40:47], v205\nE 56\nE 57\nE 58\nE 59\n"
"v_smfmac_f32_32x32x32_bf16 v[80:95], v[216:219], v[48:55], v205\nE 60\nE 61\nE 62\nE 63\ns_setprio 3\n"
"v_mfma_f32_32x32x8_bf16 v[16:31], v[152:153], v[138:139], v[112:127]\nE 0\nE 1\nE 2\nE 3\nE 4\n"
"v_mfma_f32_32x32x16_bf16 v[16:31], v[144:147], v[132:135], v[16:31]\nds_read_b64 v[144:145], v227 offset:3584\n"
"ds_read_b64 v[146:147], v227 offset:7680\nds_read_b64 v[152:153], v228 offset:3584\nE 5\nE 6\nE 7\n"
"v_smfmac_f32_32x32x32_bf16 v[80:95], v[220:223], v[56:63], v205\ns_mov_b64 exec, s[62:63]\nds_read_b128 v[216:219], v229 offset:14976\n"
"ds_read_b128 v[220:223], v229 offset:15552\ns_mov_b64 exec, -1\nE 8\nE 9\nE 10\nE 11\nW 5\n"
"v_smfmac_f32_32x32x32_bf16 v[64:79], v[208:211], v[0:7], v205\nE 12\nE 13\nE 14\nE 15\ns_setprio 2\n"
"v_mfma_f32_32x32x8_bf16 v[32:47], v[154:155], v[136:137], v[96:111]\nE 16\nE 17\nE 18\nE 19\nE 20\n"
"v_mfma_f32_32x32x16_bf16 v[32:47], v[148:151], v[128:131], v[32:47]\nE 21\nE 22\nE 23\nv_smfmac_f32_32x32x32_bf16 v[64:79], v[212:215], v[8:15], v205\n"
"E 24\nE 25\nE 26\nE 27\nv_smfmac_f32_32x32x32_bf16 v[80:95], v[208:211], v[16:23], v205\nE 28\nE 29\nE 30\nE 31\ns_setprio 1\n"
"v_mfma_f32_32x32x8_bf16 v[48:63], v[154:155], v[138:139], v[112:127]\nE 32\nE 33\nE 34\nE 35\nE 36\n"
"v_mfma_f32_32x32x16_bf16 v[48:63], v[148:151], v[132:135], v[48:63]\nds_read_b64 v[148:149], v227 offset:3840\n"
"ds_read_b64 v[150:151], v227 offset:7936\nds_read_b64 v[154:155], v228 offset:3840\nE 37\nE 38\nE 39\n"
"v_smfmac_f32_32x32x32_bf16 v[80:95], v[212:215], v[24:31], v205\ns_mov_b64 exec, s[62:63]\nds_read_b128 v[208:211], v229 offset:16128\n"
"ds_read_b128 v[212:215], v229 offset:16704\ns_mov_b64 exec, -1\nE 40\nE 41\nE 42\nE 43\nW 5\n"
"v_smfmac_f32_32x32x32_bf16 v[64:79], v[216:219], v[32:39], v205\nE 44\nE 45\nE 46\nE 47\ns_setprio 0\n"
"v_mfma_f32_32x32x8_bf16 v[0:15], v[152:153], v[136:137], v[96:111]\nE 48\nE 49\nE 50\nE 51\nE 52\n"
"v_mfma_f32_32x32x16_bf16 v[0:15], v[144:147], v[128:131], v[0:15]\nE 53\nE 54\nE 55\nv_smfmac_f32_32x32x32_bf16 v[64:79], v[220:223], v[40:47], v205\n"
"E 56\nE 57\nE 58\nE 59\nv_smfmac_f32_32x32x32_bf16 v[80:95], v[216:219], v[48:55], v205\nE 60\nE 61\nE 62\nE 63\ns_setprio 3\n"
"v_mfma_f32_32x32x8_bf16 v[16:31], v[152:153], v[138:139], v[112:127]\nE 0\nE 1\nE 2\nE 3\nE 4\n"
"v_mfma_f32_32x32x16_bf16 v[16:31], v[144:147], v[132:135], v[16:31]\nv_subrev_u32 v227, 38912, v227\nv_subrev_u32 v228, 38912, v228\nE 5\nE 6\nE 7\n"
"v_smfmac_f32_32x32x32_bf16 v[80:95], v[220:223], v[56:63], v205\ns_mov_b64 exec, s[62:63]\nds_read_b128 v[216:219], v229 offset:17280\n"
"ds_read_b128 v[220:223], v229 offset:17856\ns_mov_b64 exec, -1\nE 8\nE 9\nE 10\nE 11\ns_waitcnt vmcnt(3) lgkmcnt(0)\ns_barrier\n"
"ds_read_b64 v[144:145], v227 offset:0\nds_read_b64 v[146:147], v227 offset:4096\nds_read_b64 v[152:153], v228 offset:0\n"
"v_smfmac_f32_32x32x32_bf16 v[64:79], v[208:211], v[0:7], v205\nE 12\nE 13\nE 14\nE 15\ns_setprio 2\n"
"v_mfma_f32_32x32x8_bf16 v[32:47], v[154:155], v[136:137], v[96:111]\nE 16\nE 17\nE 18\nE 19\nE 20\n"
"v_mfma_f32_32x32x16_bf16 v[32:47], v[148:151], v[128:131], v[32:47]\ns_mov_b64 exec, 1\nv_mov_b32 v237, s72\n"
"buffer_store_dword v237, off, s[52:55], s73 sc1\ns_mov_b64 exec, -1\nE 21\nE 22\nE 23\nv_smfmac_f32_32x32x32_bf16 v[64:79], v[212:215], v[8:15], v205\n"
"E 24\nE 25\nE 26\nE 27\nv_smfmac_f32_32x32x32_bf16 v[80:95], v[208:211], v[16:23], v205\nE 28\nE 29\nE 30\nE 31\nv_subrev_u32 v229, 38912, v229\n"
"s_setprio 1\nv_mfma_f32_32x32x8_bf16 v[48:63], v[154:155], v[138:139], v[112:127]\nE 32\nE 33\nE 34\nE 35\nE 36\n"
"v_mfma_f32_32x32x16_bf16 v[48:63], v[148:151], v[132:135], v[48:63]\nds_read_b64 v[148:149], v227 offset:256\nds_read_b64 v[150:151], v227 offset:4352\n"
"ds_read_b64 v[154:155], v228 offset:256\nE 37\nE 38\nE 39\nv_smfmac_f32_32x32x32_bf16 v[80:95], v[212:215], v[24:31], v205\ns_mov_b64 exec, s[62:63]\n"
"ds_read_b128 v[208:211], v229 offset:0\nds_read_b128 v[212:215], v229 offset:576\ns_mov_b64 exec, -1\nE 40\nE 41\nE 42\nE 43\nW 5\n"
"v_smfmac_f32_32x32x32_bf16 v[64:79], v[216:219], v[32:39], v205\nE 44\nE 45\nE 46\nE 47\ns_setprio 0\n"
"v_mfma_f32_32x32x8_bf16 v[0:15], v[152:153], v[136:137], v[96:111]\nE 48\nE 49\nE 50\nE 51\nE 52\n"
"v_mfma_f32_32x32x16_bf16 v[0:15], v[144:147], v[128:131], v[0:15]\nE 53\nE 54\nE 55\nv_smfmac_f32_32x32x32_bf16 v[64:79], v[220:223], v[40:47], v205\n"
"E 56\nE 57\nE 58\nE 59\nv_smfmac_f32_32x32x32_bf16 v[80:95], v[216:219], v[48:55], v205\ns_sub_u32 s46, s46, 38912\ns_sub_u32 s47, s47, 38912\n"
"s_sub_u32 s64, s64, 1\ns_cmp_lg_u32 s64, 0\nE 60\nE 61\nE 62\nE 63\ns_cbranch_scc1 1b\ns_setprio 3\n"
"v_mfma_f32_32x32x8_bf16 v[16:31], v[152:153], v[138:139], v[112:127]\nE 0\nE 1\nE 2\nE 3\nE 4\n"
"v_mfma_f32_32x32x16_bf16 v[16:31], v[144:147], v[132:135], v[16:31]\nds_read_b64 v[144:145], v227 offset:512\nds_read_b64 v[146:147], v227 offset:4608\n"
"ds_read_b64 v[152:153], v228 offset:512\nE 5\nE 6\nE 7\nv_smfmac_f32_32x32x32_bf16 v[80:95], v[220:223], v[56:63], v205\ns_mov_b64 exec, s[62:63]\n"
"ds_read_b128 v[216:219], v229 offset:1152\nds_read_b128 v[220:223], v229 offset:1728\ns_mov_b64 exec, -1\nE 8\nE 9\nE 10\nE 11\nW 5\n"
"v_smfmac_f32_32x32x32_bf16 v[64:79], v[208:211], v[0:7], v205\nE 12\nE 13\nE 14\nE 15\ns_setprio 2\n"
"v_mfma_f32_32x32x8_bf16 v[32:47], v[154:155], v[136:137], v[96:111]\nE 16\nE 17\nE 18\nE 19\nE 20\n"
"v_mfma_f32_32x32x16_bf16 v[32:47], v[148:151], v[128:131], v[32:47]\ns_add_u32 m0, s60, 45056\ns_mov_b32 s65, 0\n"
"buffer_load_dwordx4 v230, s[48:51], s65 offen lds\nE 21\nE 22\nE 23\nv_smfmac_f32_32x32x32_bf16 v[64:79], v[212:215], v[8:15], v205\nE 24\nE 25\nE 26\n"
"E 27\nv_smfmac_f32_32x32x32_bf16 v[80:95], v[208:211], v[16:23], v205\nE 28\nE 29\nE 30\nE 31\ns_setprio 1\n"
"v_mfma_f32_32x32x8_bf16 v[48:63], v[154:155], v[138:139], v[112:127]\nE 32\nE 33\nE 34\nE 35\nE 36\n"
"v_mfma_f32_32x32x16_bf16 v[48:63], v[148:151], v[132:135], v[48:63]\nds_read_b64 v[148:149], v227 offset:768\nds_read_b64 v[150:151], v227 offset:4864\n"
"ds_read_b64 v[154:155], v228 offset:768\nE 37\nE 38\nE 39\nv_smfmac_f32_32x32x32_bf16 v[80:95], v[212:215], v[24:31], v205\ns_mov_b64 exec, s[62:63]\n"
"ds_read_b128 v[208:211], v229 offset:2304\nds_read_b128 v[212:215], v229 offset:2880\ns_mov_b64 exec, -1\nE 40\nE 41\nE 42\nE 43\nW 5\n"
"v_smfmac_f32_32x32x32_bf16 v[64:79], v[216:219], v[32:39], v205\nE 44\nE 45\nE 46\nE 47\ns_setprio 0\n"
"v_mfma_f32_32x32x8_bf16 v[0:15], v[152:153], v[136:137], v[96:111]\nE 48\nE 49\nE 50\nE 51\nE 52\n"
"v_mfma_f32_32x32x16_bf16 v[0:15], v[144:147], v[128:131], v[0:15]\ns_add_u32 m0, s60, 53248\ns_mov_b32 s65, 73728\n"
"buffer_load_dwordx4 v230, s[48:51], s65 offen lds\nE 53\nE 54\nE 55\nv_smfmac_f32_32x32x32_bf16 v[64:79], v[220:223], v[40:47], v205\nE 56\nE 57\nE 58\n"
"E 59\nv_smfmac_f32_32x32x32_bf16 v[80:95], v[216:219], v[48:55], v205\nE 60\nE 61\nE 62\nE 63\ns_setprio 3\n"
"v_mfma_f32_32x32x8_bf16 v[16:31], v[152:153], v[138:139], v[112:127]\nE 0\nE 1\nE 2\nE 3\nE 4\n"
"v_mfma_f32_32x32x16_bf16 v[16:31], v[144:147], v[132:135], v[16:31]\nds_read_b64 v[144:145], v227 offset:1024\n"
"ds_read_b64 v[146:147], v227 offset:5120\nds_read_b64 v[152:153], v228 offset:1024\nE 5\nE 6\nE 7\n"
"v_smfmac_f32_32x32x32_bf16 v[80:95], v[220:223], v[56:63], v205\ns_mov_b64 exec, s[62:63]\nds_read_b128 v[216:219], v229 offset:3456\n"
"ds_read_b128 v[220:223], v229 offset:4032\ns_mov_b64 exec, -1\nE 8\nE 9\nE 10\nE 11\nW 5\n"
"v_smfmac_f32_32x32x32_bf16 v[64:79], v[208:211], v[0:7], v205\nE 12\nE 13\nE 14\nE 15\ns_setprio 2\n"
"v_mfma_f32_32x32x8_bf16 v[32:47], v[154:155], v[136:137], v[96:111]\nE 16\nE 17\nE 18\nE 19\nE 20\n"
"v_mfma_f32_32x32x16_bf16 v[32:47], v[148:151], v[128:131], v[32:47]\ns_add_u32 m0, s60, 61440\ns_mov_b32 s65, 147456\n"
"buffer_load_dwordx4 v230, s[48:51], s65 offen lds\nE 21\nE 22\nE 23\nv_smfmac_f32_32x32x32_bf16 v[64:79], v[212:215], v[8:15], v205\nE 24\nE 25\nE 26\n"
"E 27\nv_smfmac_f32_32x32x32_bf16 v[80:95], v[208:211], v[16:23], v205\nE 28\nE 29\nE 30\nE 31\ns_setprio 1\n"
"v_mfma_f32_32x32x8_bf16 v[48:63], v[154:155], v[138:139], v[112:127]\nE 32\nE 33\nE 34\nE 35\nE 36\n"
"v_mfma_f32_32x32x16_bf16 v[48:63], v[148:151], v[132:135], v[48:63]\nds_read_b64 v[148:149], v227 offset:1280\n"
"ds_read_b64 v[150:151], v227 offset:5376\nds_read_b64 v[154:155], v228 offset:1280\nE 37\nE 38\nE 39\n"
"v_smfmac_f32_32x32x32_bf16 v[80:95], v[212:215], v[24:31], v205\ns_mov_b64 exec, s[62:63]\nds_read_b128 v[208:211], v229 offset:4608\n"
"ds_read_b128 v[212:215], v229 offset:5184\ns_mov_b64 exec, -1\nE 40\nE 41\nE 42\nE 43\nW 5\n"
"v_smfmac_f32_32x32x32_bf16 v[64:79], v[216:219], v[32:39], v205\nE 44\nE 45\nE 46\nE 47\ns_setprio 0\n"
"v_mfma_f32_32x32x8_bf16 v[0:15], v[152:153], v[136:137], v[96:111]\nE 48\nE 49\nE 50\nE 51\nE 52\n"
"v_mfma_f32_32x32x16_bf16 v[0:15], v[144:147], v[128:131], v[0:15]\ns_add_u32 m0, s60, 69632\ns_mov_b32 s65, 221184\n"
"buffer_load_dwordx4 v230, s[48:51], s65 offen lds\nE 53\nE 54\nE 55\nv_smfmac_f32_32x32x32_bf16 v[64:79], v[220:223], v[40:47], v205\nE 56\nE 57\nE 58\n"
"E 59\nv_smfmac_f32_32x32x32_bf16 v[80:95], v[216:219], v[48:55], v205\nE 60\nE 61\nE 62\nE 63\ns_setprio 3\n"
"v_mfma_f32_32x32x8_bf16 v[16:31], v[152:153], v[138:139], v[112:127]\nE 0\nE 1\nE 2\nE 3\nE 4\n"
"v_mfma_f32_32x32x16_bf16 v[16:31], v[144:147], v[132:135], v[16:31]\nds_read_b64 v[144:145], v227 offset:1536\n"
"ds_read_b64 v[146:147], v227 offset:5632\nds_read_b64 v[152:153], v228 offset:1536\nE 5\nE 6\nE 7\n"
"v_smfmac_f32_32x32x32_bf16 v[80:95], v[220:223], v[56:63], v205\ns_mov_b64 exec, s[62:63]\nds_read_b128 v[216:219], v229 offset:5760\n"
"ds_read_b128 v[220:223], v229 offset:6336\ns_mov_b64 exec, -1\nE 8\nE 9\nE 10\nE 11\nW 5\n"
"v_smfmac_f32_32x32x32_bf16 v[64:79], v[208:211], v[0:7], v205\nE 12\nE 13\nE 14\nE 15\ns_setprio 2\n"
"v_mfma_f32_32x32x8_bf16 v[32:47], v[154:155], v[136:137], v[96:111]\nE 16\nE 17\nE 18\nE 19\nE 20\n"
"v_mfma_f32_32x32x16_bf16 v[32:47], v[148:151], v[128:131], v[32:47]\ns_add_u32 m0, s60, 77824\ns_mov_b32 s65, 294912\n"
"buffer_load_dwordx4 v230, s[48:51], s65 offen lds\nE 21\nE 22\nE 23\nv_smfmac_f32_32x32x32_bf16 v[64:79], v[212:215], v[8:15], v205\nE 24\nE 25\nE 26\n"
"E 27\nv_smfmac_f32_32x32x32_bf16 v[80:95], v[208:211], v[16:23], v205\nE 28\nE 29\nE 30\nE 31\ns_setprio 1\n"
"v_mfma_f32_32x32x8_bf16 v[48:63], v[154:155], v[138:139], v[112:127]\nE 32\nE 33\nE 34\nE 35\nE 36\n"
"v_mfma_f32_32x32x16_bf16 v[48:63], v[148:151], v[132:135], v[48:63]\nds_read_b64 v[148:149], v227 offset:1792\n"
"ds_read_b64 v[150:151], v227 offset:5888\nds_read_b64 v[154:155], v228 offset:1792\nE 37\nE 38\nE 39\n"
"v_smfmac_f32_32x32x32_bf16 v[80:95], v[212:215], v[24:31], v205\ns_mov_b64 exec, s[62:63]\nds_read_b128 v[208:211], v229 offset:6912\n"
"ds_read_b128 v[212:215], v229 offset:7488\ns_mov_b64 exec, -1\nE 40\nE 41\nE 42\nE 43\nW 5\n"
"v_smfmac_f32_32x32x32_bf16 v[64:79], v[216:219], v[32:39], v205\nE 44\nE 45\nE 46\nE 47\ns_setprio 0\n"
"v_mfma_f32_32x32x8_bf16 v[0:15], v[152:153], v[136:137], v[96:111]\nE 48\nE 49\nE 50\nE 51\nE 52\n"
"v_mfma_f32_32x32x16_bf16 v[0:15], v[144:147], v[128:131], v[0:15]\ns_add_u32 m0, s60, 86016\ns_mov_b32 s65, 368640\n"
"buffer_load_dwordx4 v230, s[48:51], s65 offen lds\nE 53\nE 54\nE 55\nv_smfmac_f32_32x32x32_bf16 v[64:79], v[220:223], v[40:47], v205\nE 56\nE 57\nE 58\n"
"E 59\nv_smfmac_f32_32x32x32_bf16 v[80:95], v[216:219], v[48:55], v205\nE 60\nE 61\nE 62\nE 63\ns_setprio 3\n"
"v_mfma_f32_32x32x8_bf16 v[16:31], v[152:153], v[138:139], v[112:127]\nE 0\nE 1\nE 2\nE 3\nE 4\n"
"v_mfma_f32_32x32x16_bf16 v[16:31], v[144:147], v[132:135], v[16:31]\nds_read_b64 v[144:145], v227 offset:2048\n"
"ds_read_b64 v[146:147], v227 offset:6144\nds_read_b64 v[152:153], v228 offset:2048\nE 5\nE 6\nE 7\n"
"v_smfmac_f32_32x32x32_bf16 v[80:95], v[220:223], v[56:63], v205\ns_mov_b64 exec, s[62:63]\nds_read_b128 v[216:219], v229 offset:8064\n"
"ds_read_b128 v[220:223], v229 offset:8640\ns_mov_b64 exec, -1\nE 8\nE 9\nE 10\nE 11\nW 5\n"
"v_smfmac_f32_32x32x32_bf16 v[64:79], v[208:211], v[0:7], v205\nE 12\nE 13\nE 14\nE 15\ns_setprio 2\n"
"v_mfma_f32_32x32x8_bf16 v[32:47], v[154:155], v[136:137], v[96:111]\nE 16\nE 17\nE 18\nE 19\nE 20\n"
"v_mfma_f32_32x32x16_bf16 v[32:47], v[148:151], v[128:131], v[32:47]\ns_add_u32 m0, s60, 94208\ns_mov_b32 s65, 442368\n"
"buffer_load_dwordx4 v230, s[48:51], s65 offen lds\nE 21\nE 22\nE 23\nv_smfmac_f32_32x32x32_bf16 v[64:79], v[212:215], v[8:15], v205\nE 24\nE 25\nE 26\n"
"E 27\nv_smfmac_f32_32x32x32_bf16 v[80:95], v[208:211], v[16:23], v205\nE 28\nE 29\nE 30\nE 31\ns_setprio 1\n"
"v_mfma_f32_32x32x8_bf16 v[48:63], v[154:155], v[138:139], v[112:127]\nE 32\nE 33\nE 34\nE 35\nE 36\n"
"v_mfma_f32_32x32x16_bf16 v[48:63], v[148:151], v[132:135], v[48:63]\nds_read_b64 v[148:149], v227 offset:2304\n"
"ds_read_b64 v[150:151], v227 offset:6400\nds_read_b64 v[154:155], v228 offset:2304\nE 37\nE 38\nE 39\n"
"v_smfmac_f32_32x32x32_bf16 v[80:95], v[212:215], v[24:31], v205\ns_mov_b64 exec, s[62:63]\nds_read_b128 v[208:211], v229 offset:9216\n"
"ds_read_b128 v[212:215], v229 offset:9792\ns_mov_b64 exec, -1\nE 40\nE 41\nE 42\nE 43\nW 5\n"
"v_smfmac_f32_32x32x32_bf16 v[64:79], v[216:219], v[32:39], v205\nE 44\nE 45\nE 46\nE 47\ns_setprio 0\n"
"v_mfma_f32_32x32x8_bf16 v[0:15], v[152:153], v[136:137], v[96:111]\nE 48\nE 49\nE 50\nE 51\nE 52\n"
"v_mfma_f32_32x32x16_bf16 v[0:15], v[144:147], v[128:131], v[0:15]\ns_add_u32 m0, s60, 102400\ns_mov_b32 s65, 516096\n"
"buffer_load_dwordx4 v230, s[48:51], s65 offen lds\nE 53\nE 54\nE 55\nv_smfmac_f32_32x32x32_bf16 v[64:79], v[220:223], v[40:47], v205\nE 56\nE 57\nE 58\n"
"E 59\nv_smfmac_f32_32x32x32_bf16 v[80:95], v[216:219], v[48:55], v205\nE 60\nE 61\nE 62\nE 63\ns_setprio 3\n"
"v_mfma_f32_32x32x8_bf16 v[16:31], v[152:153], v[138:139], v[112:127]\nE 0\nE 1\nE 2\nE 3\nE 4\n"
"v_mfma_f32_32x32x16_bf16 v[16:31], v[144:147], v[132:135], v[16:31]\nds_read_b64 v[144:145], v227 offset:2560\n"
"ds_read_b64 v[146:147], v227 offset:6656\nds_read_b64 v[152:153], v228 offset:2560\nE 5\nE 6\nE 7\n"
"v_smfmac_f32_32x32x32_bf16 v[80:95], v[220:223], v[56:63], v205\ns_mov_b64 exec, s[62:63]\nds_read_b128 v[216:219], v229 offset:10368\n"
"ds_read_b128 v[220:223], v229 offset:10944\ns_mov_b64 exec, -1\nE 8\nE 9\nE 10\nE 11\nW 5\n"
"v_smfmac_f32_32x32x32_bf16 v[64:79], v[208:211], v[0:7], v205\nE 12\nE 13\nE 14\nE 15\ns_setprio 2\n"
"v_mfma_f32_32x32x8_bf16 v[32:47], v[154:155], v[136:137], v[96:111]\nE 16\nE 17\nE 18\nE 19\nE 20\n"
"v_mfma_f32_32x32x16_bf16 v[32:47], v[148:151], v[128:131], v[32:47]\ns_add_u32 m0, s60, 110592\ns_mov_b32 s65, 589824\n"
"buffer_load_dwordx4 v230, s[48:51], s65 offen lds\nE 21\nE 22\nE 23\nv_smfmac_f32_32x32x32_bf16 v[64:79], v[212:215], v[8:15], v205\nE 24\nE 25\nE 26\n"
"E 27\nv_smfmac_f32_32x32x32_bf16 v[80:95], v[208:211], v[16:23], v205\nE 28\nE 29\nE 30\nE 31\ns_setprio 1\n"
"v_mfma_f32_32x32x8_bf16 v[48:63], v[154:155], v[138:139], v[112:127]\nE 32\nE 33\nE 34\nE 35\nE 36\n"
"v_mfma_f32_32x32x16_bf16 v[48:63], v[148:151], v[132:135], v[48:63]\nds_read_b64 v[148:149], v227 offset:2816\n"
"ds_read_b64 v[150:151], v227 offset:6912\nds_read_b64 v[154:155], v228 offset:2816\nE 37\nE 38\nE 39\n"
"v_smfmac_f32_32x32x32_bf16 v[80:95], v[212:215], v[24:31], v205\ns_mov_b64 exec, s[62:63]\nds_read_b128 v[208:211], v229 offset:11520\n"
"ds_read_b128 v[212:215], v229 offset:12096\ns_mov_b64 exec, -1\nE 40\nE 41\nE 42\nE 43\nW 5\n"
"v_smfmac_f32_32x32x32_bf16 v[64:79], v[216:219], v[32:39], v205\nE 44\nE 45\nE 46\nE 47\ns_setprio 0\n"
"v_mfma_f32_32x32x8_bf16 v[0:15], v[152:153], v[136:137], v[96:111]\nE 48\nE 49\nE 50\nE 51\nE 52\n"
"v_mfma_f32_32x32x16_bf16 v[0:15], v[144:147], v[128:131], v[0:15]\ns_add_u32 m0, s60, 118784\ns_mul_i32 s65, s71, 73728\n"
"buffer_load_dwordx4 v231, s[48:51], s65 offen lds\ns_cmp_lg_u32 s71, 0\ns_cbranch_scc1 64f\ns_mov_b32 m0, 126976\ns_mov_b32 s65, 589824\n"
"buffer_load_dwordx4 v231, s[48:51], s65 offen lds\n64:\nE 53\nE 54\nE 55\nv_smfmac_f32_32x32x32_bf16 v[64:79], v[220:223], v[40:47], v205\nE 56\nE 57\n"
"E 58\nE 59\nv_smfmac_f32_32x32x32_bf16 v[80:95], v[216:219], v[48:55], v205\nE 60\nE 61\nE 62\nE 63\ns_setprio 3\n"
"v_mfma_f32_32x32x8_bf16 v[16:31], v[152:153], v[138:139], v[112:127]\nE 0\nE 1\nE 2\nE 3\nE 4\n"
"v_mfma_f32_32x32x16_bf16 v[16:31], v[144:147], v[132:135], v[16:31]\nds_read_b64 v[144:145], v227 offset:3072\n"
"ds_read_b64 v[146:147], v227 offset:7168\nds_read_b64 v[152:153], v228 offset:3072\nE 5\nE 6\nE 7\n"
"v_smfmac_f32_32x32x32_bf16 v[80:95], v[220:223], v[56:63], v205\ns_mov_b64 exec, s[62:63]\nds_read_b128 v[216:219], v229 offset:12672\n"
"ds_read_b128 v[220:223], v229 offset:13248\ns_mov_b64 exec, -1\nE 8\nE 9\nE 10\nE 11\nW 5\n"
"v_smfmac_f32_32x32x32_bf16 v[64:79], v[208:211], v[0:7], v205\nE 12\nE 13\nE 14\nE 15\ns_setprio 2\n"
"v_mfma_f32_32x32x8_bf16 v[32:47], v[154:155], v[136:137], v[96:111]\nE 16\nE 17\nE 18\nE 19\nE 20\n"
"v_mfma_f32_32x32x16_bf16 v[32:47], v[148:151], v[128:131], v[32:47]\nE 21\nE 22\nE 23\nv_smfmac_f32_32x32x32_bf16 v[64:79], v[212:215], v[8:15], v205\n"
"E 24\nE 25\nE 26\nE 27\nv_smfmac_f32_32x32x32_bf16 v[80:95], v[208:211], v[16:23], v205\nE 28\nE 29\nE 30\nE 31\ns_setprio 1\n"
"v_mfma_f32_32x32x8_bf16 v[48:63], v[154:155], v[138:139], v[112:127]\nE 32\nE 33\nE 34\nE 35\nE 36\n"
"v_mfma_f32_32x32x16_bf16 v[48:63], v[148:151], v[132:135], v[48:63]\nds_read_b64 v[148:149], v227 offset:3328\n"
"ds_read_b64 v[150:151], v227 offset:7424\nds_read_b64 v[154:155], v228 offset:3328\nE 37\nE 38\nE 39\n"
"v_smfmac_f32_32x32x32_bf16 v[80:95], v[212:215], v[24:31], v205\ns_mov_b64 exec, s[62:63]\nds_read_b128 v[208:211], v229 offset:13824\n"
"ds_read_b128 v[212:215], v229 offset:14400\ns_mov_b64 exec, -1\nE 40\nE 41\nE 42\nE 43\nW 5\n"
"v_smfmac_f32_32x32x32_bf16 v[64:79], v[216:219], v[32:39], v205\nE 44\nE 45\nE 46\nE 47\ns_setprio 0\n"
"v_mfma_f32_32x32x8_bf16 v[0:15], v[152:153], v[136:137], v[96:111]\nE 48\nE 49\nE 50\nE 51\nE 52\n"
"v_mfma_f32_32x32x16_bf16 v[0:15], v[144:147], v[128:131], v[0:15]\nE 53\nE 54\nE 55\nv_smfmac_f32_32x32x32_bf16 v[64:79], v[220:223], v[40:47], v205\n"
"E 56\nE 57\nE 58\nE 59\nv_smfmac_f32_32x32x32_bf16 v[80:95], v[216:219], v[48:55], v205\nE 60\nE 61\nE 62\nE 63\ns_setprio 3\n"
"v_mfma_f32_32x32x8_bf16 v[16:31], v[152:153], v[138:139], v[112:127]\nE 0\nE 1\nE 2\nE 3\nE 4\n"
"v_mfma_f32_32x32x16_bf16 v[16:31], v[144:147], v[132:135], v[16:31]\nds_read_b64 v[144:145], v227 offset:3584\n"
"ds_read_b64 v[146:147], v227 offset:7680\nds_read_b64 v[152:153], v228 offset:3584\nE 5\nE 6\nE 7\n"
"v_smfmac_f32_32x32x32_bf16 v[80:95], v[220:223], v[56:63], v205\ns_mov_b64 exec, s[62:63]\nds_read_b128 v[216:219], v229 offset:14976\n"
"ds_read_b128 v[220:223], v229 offset:15552\ns_mov_b64 exec, -1\nE 8\nE 9\nE 10\nE 11\nW 5\n"
"v_smfmac_f32_32x32x32_bf16 v[64:79], v[208:211], v[0:7], v205\nE 12\nE 13\nE 14\nE 15\ns_setprio 2\n"
"v_mfma_f32_32x32x8_bf16 v[32:47], v[154:155], v[136:137], v[96:111]\nE 16\nE 17\nE 18\nE 19\nE 20\n"
"v_mfma_f32_32x32x16_bf16 v[32:47], v[148:151], v[128:131], v[32:47]\nE 21\nE 22\nE 23\nv_smfmac_f32_32x32x32_bf16 v[64:79], v[212:215], v[8:15], v205\n"
"E 24\nE 25\nE 26\nE 27\nv_smfmac_f32_32x32x32_bf16 v[80:95], v[208:211], v[16:23], v205\nE 28\nE 29\nE 30\nE 31\ns_setprio 1\n"
"v_mfma_f32_32x32x8_bf16 v[48:63], v[154:155], v[138:139], v[112:127]\nE 32\nE 33\nE 34\nE 35\nE 36\n"
"v_mfma_f32_32x32x16_bf16 v[48:63], v[148:151], v[132:135], v[48:63]\nds_read_b64 v[148:149], v227 offset:3840\n"
"ds_read_b64 v[150:151], v227 offset:7936\nds_read_b64 v[154:155], v228 offset:3840\nE 37\nE 38\nE 39\n"
"v_smfmac_f32_32x32x32_bf16 v[80:95], v[212:215], v[24:31], v205\ns_mov_b64 exec, s[62:63]\nds_read_b128 v[208:211], v229 offset:16128\n"
"ds_read_b128 v[212:215], v229 offset:16704\ns_mov_b64 exec, -1\nE 40\nE 41\nE 42\nE 43\nW 5\n"
"v_smfmac_f32_32x32x32_bf16 v[64:79], v[216:219], v[32:39], v205\nE 44\nE 45\nE 46\nE 47\ns_setprio 0\n"
"v_mfma_f32_32x32x8_bf16 v[0:15], v[152:153], v[136:137], v[96:111]\nE 48\nE 49\nE 50\nE 51\nE 52\n"
"v_mfma_f32_32x32x16_bf16 v[0:15], v[144:147], v[128:131], v[0:15]\nE 53\nE 54\nE 55\nv_smfmac_f32_32x32x32_bf16 v[64:79], v[220:223], v[40:47], v205\n"
"E 56\nE 57\nE 58\nE 59\nv_smfmac_f32_32x32x32_bf16 v[80:95], v[216:219], v[48:55], v205\nE 60\nE 61\nE 62\nE 63\ns_setprio 3\n"
"v_mfma_f32_32x32x8_bf16 v[16:31], v[152:153], v[138:139], v[112:127]\nE 0\nE 1\nE 2\nE 3\nE 4\n"
"v_mfma_f32_32x32x16_bf16 v[16:31], v[144:147], v[132:135], v[16:31]\nE 5\nE 6\nE 7\nv_smfmac_f32_32x32x32_bf16 v[80:95], v[220:223], v[56:63], v205\n"
"s_mov_b64 exec, s[62:63]\nds_read_b128 v[216:219], v229 offset:17280\nds_read_b128 v[220:223], v229 offset:17856\ns_mov_b64 exec, -1\nE 8\nE 9\nE 10\n"
"E 11\nW 2\nv_smfmac_f32_32x32x32_bf16 v[64:79], v[208:211], v[0:7], v205\nE 12\nE 13\nE 14\nE 15\ns_setprio 2\n"
"v_mfma_f32_32x32x8_bf16 v[32:47], v[154:155], v[136:137], v[96:111]\nE 16\nE 17\nE 18\nE 19\nE 20\n"
"v_mfma_f32_32x32x16_bf16 v[32:47], v[148:151], v[128:131], v[32:47]\nE 21\nE 22\nE 23\nv_smfmac_f32_32x32x32_bf16 v[64:79], v[212:215], v[8:15], v205\n"
"E 24\nE 25\nE 26\nE 27\nv_smfmac_f32_32x32x32_bf16 v[80:95], v[208:211], v[16:23], v205\nE 28\nE 29\nE 30\nE 31\ns_setprio 1\n"
"v_mfma_f32_32x32x8_bf16 v[48:63], v[154:155], v[138:139], v[112:127]\nE 32\nE 33\nE 34\nE 35\nE 36\n"
"v_mfma_f32_32x32x16_bf16 v[48:63], v[148:151], v[132:135], v[48:63]\nE 37\nE 38\nE 39\nv_smfmac_f32_32x32x32_bf16 v[80:95], v[212:215], v[24:31], v205\n"
"E 40\nE 41\nE 42\nE 43\nW 0\nv_smfmac_f32_32x32x32_bf16 v[64:79], v[216:219], v[32:39], v205\nE 44\nE 45\nE 46\nE 47\ns_setprio 0\nE 48\nE 49\nE 50\n"
"E 51\nE 52\nE 53\nE 54\nE 55\nv_smfmac_f32_32x32x32_bf16 v[64:79], v[220:223], v[40:47], v205\nE 56\nE 57\nE 58\nE 59\n"
"v_smfmac_f32_32x32x32_bf16 v[80:95], v[216:219], v[48:55], v205\nE 60\nE 61\nE 62\nE 63\ns_setprio 0\ns_nop 0\n"
"v_smfmac_f32_32x32x32_bf16 v[80:95], v[220:223], v[56:63], v205\ns_mov_b32 s62, 0\ns_mov_b32 s63, -1\ns_mov_b32 s65, 8126464\ns_mov_b32 s66, 10223616\n"
"s_mov_b32 s67, 0x7f61b1e6\nv_cndmask_b32 v0, v74, v72, s[62:63]\nv_add_f32 v2, |v64|, v0\nv_add_f32 v2, |v65|, v2\nv_add_f32 v2, |v66|, v2\n"
"v_add_f32 v2, |v67|, v2\nv_add_f32 v2, |v68|, v2\nv_add_f32 v2, |v69|, v2\nv_add_f32 v2, |v70|, v2\nv_add_f32 v2, |v71|, v2\nv_add_f32 v2, |v72|, v2\n"
"v_add_f32 v2, |v73|, v2\nv_rcp_f32 v4, v0\nv_cndmask_b32 v1, v90, v88, s[62:63]\nv_add_f32 v3, |v80|, v1\nv_add_f32 v3, |v81|, v3\n"
"v_add_f32 v3, |v82|, v3\nv_add_f32 v3, |v83|, v3\nv_add_f32 v3, |v84|, v3\nv_add_f32 v3, |v85|, v3\nv_add_f32 v3, |v86|, v3\nv_add_f32 v3, |v87|, v3\n"
"v_add_f32 v3, |v88|, v3\nv_add_f32 v3, |v89|, v3\nv_rcp_f32 v5, v1\nv_add_f32 v2, v2, v3\nv_min_f32 v0, v0, v1\nv_cmp_gt_f32 s[74:75], s67, v2\n"
"v_cmp_lt_f32 vcc, 0.5, v0\ns_and_b64 vcc, vcc, s[74:75]\ns_cmp_eq_u64 vcc, -1\ns_cselect_b32 s64, 2, 0\ns_cbranch_scc0 65f\nv_mul_f32 v64, v4, v64\n"
"v_mul_f32 v65, v4, v65\nv_mul_f32 v66, v4, v66\nv_mul_f32 v67, v4, v67\nv_mul_f32 v68, v4, v68\nv_mul_f32 v69, v4, v69\nv_mul_f32 v70, v4, v70\n"
"v_mul_f32 v71, v4, v71\nv_mul_f32 v72, v4, v72\nv_mul_f32 v73, v4, v73\nv_cvt_pk_bf16_f32 v8, v64, v65\nv_cvt_pk_bf16_f32 v9, v66, v67\n"
"v_cvt_pk_bf16_f32 v10, v68, v69\nv_cvt_pk_bf16_f32 v11, v70, v71\nv_cvt_pk_bf16_f32 v12, v72, v73\n"
"buffer_store_dwordx4 v[8:11], v235, s[52:55], s65 offen offset:0 sc1\ns_mov_b32 exec_hi, 0\n"
"buffer_store_dword v12, v236, s[52:55], s66 offen offset:0 sc1\ns_mov_b32 exec_hi, -1\nv_mul_f32 v80, v5, v80\nv_mul_f32 v81, v5, v81\n"
"v_mul_f32 v82, v5, v82\nv_mul_f32 v83, v5, v83\nv_mul_f32 v84, v5, v84\nv_mul_f32 v85, v5, v85\nv_mul_f32 v86, v5, v86\nv_mul_f32 v87, v5, v87\n"
"v_mul_f32 v88, v5, v88\nv_mul_f32 v89, v5, v89\nv_cvt_pk_bf16_f32 v16, v80, v81\nv_cvt_pk_bf16_f32 v17, v82, v83\nv_cvt_pk_bf16_f32 v18, v84, v85\n"
"v_cvt_pk_bf16_f32 v19, v86, v87\nv_cvt_pk_bf16_f32 v20, v88, v89\nbuffer_store_dwordx4 v[16:19], v235, s[52:55], s65 offen offset:512 sc1\n"
"s_mov_b32 exec_hi, 0\nbuffer_store_dword v20, v236, s[52:55], s66 offen offset:256 sc1\ns_mov_b32 exec_hi, -1\n65:\nv_mov_b32 v254, s72\n"
"v_mov_b32 v253, s64\nv_mov_b32 v252, s71\ns_nop 0\n99:\n.purgem M\n.purgem Z\n.purgem R\n.purgem W\n.purgem E\n"
        : "={v[252:255]}"(st), "+{v240}"(tid), "+{s[40:41]}"(wg), "+{s[44:45]}"(wu), "+{s[48:49]}"(wqkv), "+{s[56:57]}"(x)
        : "{s[52:53]}"(ws), "{s[82:83]}"(wo), "{s84}"(blockIdx.x), "{s[86:87]}"(wd), "{s[88:89]}"(gw)
        : "memory", "scc", "vcc",
"s42","s43","s46","s47","s50","s51","s54","s55","s58","s59","s60","s61","s62","s63","s64","s65","s66","s67","s68","s69","s70","s71","s72","s73","s74","s75","s76","s77","s80","s81","s84","s85","s90","s91","s92","s93","s94","s95","s96","s97","s98","s99","v0","v1","v2","v3","v4","v5","v6","v7","v8","v9","v10","v11","v12","v13","v14","v15","v16","v17","v18","v19","v20","v21","v22","v23","v24","v25","v26","v27","v28","v29","v30","v31","v32","v33","v34","v35","v36","v37","v38","v39","v40","v41","v42","v43","v44","v45","v46","v47","v48","v49","v50","v51","v52","v53","v54","v55","v56","v57","v58","v59","v60","v61","v62","v63","v64","v65","v66","v67","v68","v69","v70","v71","v72","v73","v74","v75","v76","v77","v78","v79","v80","v81","v82","v83","v84","v85","v86","v87","v88","v89","v90","v91","v92","v93","v94","v95","v96","v97","v98","v99","v100","v101","v102","v103","v104","v105","v106","v107","v108","v109","v110","v111","v112","v113","v114","v115","v116","v117","v118","v119","v120","v121","v122","v123","v124","v125","v126","v127","v128","v129","v130","v131","v132","v133","v134","v135","v136","v137","v138","v139","v140","v141","v142","v143","v144","v145","v146","v147","v148","v149","v150","v151","v152","v153","v154","v155","v156","v157","v158","v159","v160","v161","v162","v163","v164","v165","v166","v167","v168","v169","v170","v171","v172","v173","v174","v175","v176","v177","v178","v179","v180","v181","v182","v183","v184","v185","v186","v187","v188","v189","v190","v191","v192","v193","v194","v195","v196","v197","v198","v199","v200","v201","v202","v203","v204","v205","v206","v207","v208","v209","v210","v211","v212","v213","v214","v215","v216","v217","v218","v219","v220","v221","v222","v223","v224","v225","v226","v227","v228","v229","v230","v231","v232","v233","v234","v235","v236","v237","v238","v239","v241","v242","v243","v244","v245","v246","v247","v248","v249","v250","v251"
    );
    const unsigned tag = __builtin_amdgcn_readfirstlane(st[2]), status = __builtin_amdgcn_readfirstlane(st[1]);
    const int w = __builtin_amdgcn_readfirstlane(st[0]), xcd = blockIdx.x & 7, j = blockIdx.x >> 4;
    if (__builtin_expect(!(status & 2), 0)) {
      const __amdgpu_buffer_rsrc_t rws = __builtin_amdgcn_make_buffer_rsrc(ws, 0, WS_USED, 0x20000);
      const int head = xcd * 4 + (j >> 2), odd = head & 1, g0 = 9 * (head >> 1), lane = lane_id(), qrow = (j & 3) * 512 + w * 64;
      const unsigned ro = (qrow + lane) * 8;
      float qf[18], acc[18];
#pragma unroll
      for (int e = 0; e < 4; ++e) {
        const u32x2 a = __builtin_amdgcn_raw_buffer_load_b64(rws, (g0 + 5 * odd + e) * 16384 + ro, OFF_Q, 0);
        qf[4 * e] = bflo(a[0]); qf[4 * e + 1] = bfhi(a[0]); qf[4 * e + 2] = bflo(a[1]); qf[4 * e + 3] = bfhi(a[1]);
      }
      { const unsigned a = __builtin_amdgcn_raw_buffer_load_b32(rws, (g0 + 4) * 16384 + ro + 4 * odd, OFF_Q, 0); qf[16] = bflo(a); qf[17] = bfhi(a); }
#pragma unroll
      for (int e = 0; e < 18; ++e) acc[e] = 0.f;
      float m = -INFINITY, l = 0.f;
#pragma unroll 1
      for (int k = 0; k < 2048; ++k) {
        float s = 0.f;
#pragma unroll
        for (int e = 0; e < 4; ++e) {
          const u32x2 a = __builtin_amdgcn_raw_buffer_load_b64(rws, 0, OFF_K + (g0 + 5 * odd + e) * 16384 + k * 8, 0);
          s += qf[4 * e] * bflo(a[0]) + qf[4 * e + 1] * bfhi(a[0]) + qf[4 * e + 2] * bflo(a[1]) + qf[4 * e + 3] * bfhi(a[1]);
        }
        { const unsigned a = __builtin_amdgcn_raw_buffer_load_b32(rws, 0, OFF_K + (g0 + 4) * 16384 + k * 8 + 4 * odd, 0); s += qf[16] * bflo(a) + qf[17] * bfhi(a); }
        const float mn = fmaxf(m, s), al = __builtin_amdgcn_exp2f(m - mn), pe = __builtin_amdgcn_exp2f(s - mn);
        l = l * al + pe; m = mn;
#pragma unroll
        for (int e = 0; e < 18; ++e)
          acc[e] = acc[e] * al + pe * bflo(__builtin_amdgcn_raw_buffer_load_b16(rws, e * 16, OFF_V + head * 73728 + (k >> 3) * 288 + (k & 7) * 2, 0));
      }
      const float r = 1.f / l;
      const int pos = qrow + (lane & 32) + (lane & 1) * 16 + ((lane & 31) >> 1);
#pragma unroll
      for (int h = 0; h < 2; ++h)
        __builtin_amdgcn_raw_buffer_store_b128(u32x4{cvtpk(acc[4 * h] * r, acc[4 * h + 1] * r), cvtpk(acc[4 * h + 2] * r, acc[4 * h + 3] * r),
                                                     cvtpk(acc[8 + 4 * h] * r, acc[9 + 4 * h] * r), cvtpk(acc[10 + 4 * h] * r, acc[11 + 4 * h] * r)},
                                               rws, pos * 16, OFF_O + (2 * head + h) * 32768, 16);
      __builtin_amdgcn_raw_buffer_store_b32(cvtpk(acc[16] * r, acc[17] * r), rws, pos * 8, OEXT + (head >> 1) * 16384 + 4 * odd, 16);
    }
    asm volatile(
".macro M d,a,b\nv_mfma_f32_16x16x32_bf16 v[\\d:\\d+3], v[\\a:\\a+3], v[\\b:\\b+3], v[\\d:\\d+3]\n.endm\n.macro Z d,a,b\n"
"v_mfma_f32_16x16x32_bf16 v[\\d:\\d+3], v[\\a:\\a+3], v[\\b:\\b+3], 0\n.endm\n.macro R d,a,o\nds_read_b64_tr_b16 v[\\d:\\d+1], v\\a offset:\\o\n.endm\n"
".macro W n\ns_waitcnt lgkmcnt(\\n)\n.endm\n.macro E r\nv_exp_f32 v\\r, v\\r\n.endm\ns_mov_b32 s54, 31522816\ns_mov_b32 s55, 0x20000\n"
"v_mbcnt_lo_u32_b32 v0, -1, 0\nv_mbcnt_hi_u32_b32 v0, -1, v0\nv_lshlrev_b32 v250, 4, v0\ns_and_b32 s80, s84, 7\ns_lshl_b32 s80, s80, 1\n"
"s_lshr_b32 s74, s84, 7\ns_add_u32 s80, s80, s74\ns_bfe_u32 s81, s84, 0x30004\ns_and_b32 s77, s71, 3\ns_lshl_b32 s77, s77, 1\ns_lshr_b32 s74, s71, 2\n"
"s_add_u32 s77, s77, s74\ns_mul_i32 s70, s80, 1179648\ns_add_u32 s70, s70, 12648448\nv_and_b32 v253, 0x300, v250\nv_lshlrev_b32 v251, 3, v253\n"
"v_and_b32 v253, 0xf0, v250\nv_add_u32 v251, v251, v253\ns_lshl_b32 s74, s77, 8\ns_lshl_b32 s75, s81, 14\ns_add_u32 s74, s74, s75\n"
"v_add_u32 v251, s74, v251\nv_and_b32 v252, 0x100, v250\nv_lshl_add_u32 v252, v252, 2, v253\nv_lshrrev_b32 v253, 9, v250\n"
"v_mul_u32_u24 v253, 589824, v253\nv_add_u32 v252, v252, v253\ns_lshr_b32 s74, s77, 1\ns_lshl_b32 s74, s74, 8\ns_and_b32 s75, s77, 1\n"
"s_lshl_b32 s75, s75, 3\ns_add_u32 s74, s74, s75\ns_lshl_b32 s75, s81, 11\ns_add_u32 s74, s74, s75\ns_add_u32 s74, s74, 131072\n"
"v_add_u32 v252, s74, v252\nv_and_b32 v174, 0x70, v250\nv_lshlrev_b32 v174, 2, v174\ns_lshl_b32 s65, s80, 9\ns_add_u32 s65, s65, 65536\n"
"buffer_load_dword v175, v174, s[52:55], s65 offen sc1\nv_and_b32 v1, 15, v0\nv_lshrrev_b32 v2, 4, v0\nv_lshrrev_b32 v3, 2, v1\nv_and_b32 v4, 3, v0\n"
"s_lshr_b32 s68, s71, 2\ns_lshl_b32 s64, s80, 7\ns_lshr_b32 s74, s77, 1\ns_lshl_b32 s74, s74, 5\ns_add_u32 s74, s74, s68\ns_add_u32 s67, s74, s64\n"
"s_lshl_b32 s74, s67, 2\ns_add_u32 s74, s74, 12582912\nv_lshlrev_b32 v5, 3, v1\nbuffer_load_dword v168, v5, s[52:55], s74 offen sc1\n"
"s_lshl_b32 s74, s77, 4\ns_add_u32 s74, s74, s64\nv_add_u32 v5, s74, v1\nv_lshlrev_b32 v169, 15, v2\nv_lshl_add_u32 v169, v5, 4, v169\n"
"v_xor_b32 v170, 0x100, v169\nv_lshrrev_b32 v171, 1, v169\nv_lshrrev_b32 v172, 1, v170\ns_mul_i32 s74, s68, 288\nv_lshrrev_b32 v5, 1, v2\n"
"v_mul_u32_u24 v5, 12, v5\nv_lshl_add_u32 v5, v2, 2, v5\nv_add3_u32 v5, v5, v3, s74\nv_mul_u32_u24 v6, 36, v2\ns_add_u32 s74, s74, 16\n"
"v_add3_u32 v6, v6, v3, s74\nv_and_b32 v7, 1, v4\nv_lshlrev_b32 v7, 3, v7\nv_add_u32 v7, 118784, v7\nv_lshl_add_u32 v248, v5, 4, v7\n"
"v_lshl_add_u32 v249, v6, 4, v7\nv_and_b32 v7, 6, v5\nv_lshlrev_b32 v7, 4, v7\nv_lshl_add_u32 v7, v4, 3, v7\nv_lshl_add_u32 v7, v5, 7, v7\n"
"v_add_u32 v240, 45056, v7\nv_xor_b32 v241, 32, v240\nv_xor_b32 v242, 64, v240\nv_xor_b32 v243, 96, v240\nv_and_b32 v7, 6, v6\nv_lshlrev_b32 v7, 4, v7\n"
"v_lshl_add_u32 v7, v4, 3, v7\nv_lshl_add_u32 v7, v6, 7, v7\nv_add_u32 v244, 45056, v7\nv_xor_b32 v245, 32, v244\nv_xor_b32 v246, 64, v244\n"
"v_xor_b32 v247, 96, v244\nv_and_b32 v5, 1, v1\nv_lshlrev_b32 v5, 4, v5\nv_mov_b32 v6, 0x3f80\nv_lshlrev_b32 v5, v5, v6\nv_and_b32 v6, 2, v1\n"
"v_cmp_eq_u32 s[62:63], 0, v6\nv_and_b32 v7, 1, v2\nv_cmp_eq_u32 s[74:75], v3, v2\nv_cndmask_b32 v6, 0, v5, s[62:63]\nv_xor_b32 v5, v5, v6\n"
"v_cmp_eq_u32 s[62:63], v3, v7\nv_cndmask_b32 v156, 0, v6, s[74:75]\nv_cndmask_b32 v157, 0, v5, s[74:75]\nv_mov_b32 v158, 0\nv_mov_b32 v159, 0\n"
"v_mov_b32 v160, 0\nv_mov_b32 v161, 0\nv_mov_b32 v162, v156\nv_mov_b32 v163, v157\nv_cndmask_b32 v164, 0, v6, s[62:63]\n"
"v_cndmask_b32 v165, 0, v5, s[62:63]\nv_mov_b32 v166, v164\nv_mov_b32 v167, v165\ns_mul_i32 s74, s71, 5120\ns_add_u32 s74, s74, 0\n"
"v_add_u32 v173, s74, v250\ns_sub_u32 s88, s81, s80\ns_sub_u32 s88, s88, 5\ns_and_b32 s88, s88, 7\ns_mul_i32 s88, s88, 147456\ns_add_u32 s88, s88, s70\n"
"s_add_u32 s89, s88, 8192\ns_sub_u32 s90, s81, s80\ns_sub_u32 s90, s90, 6\ns_and_b32 s90, s90, 7\ns_mul_i32 s90, s90, 147456\ns_add_u32 s90, s90, s70\n"
"s_add_u32 s91, s90, 8192\ns_sub_u32 s92, s81, s80\ns_sub_u32 s92, s92, 7\ns_and_b32 s92, s92, 7\ns_mul_i32 s92, s92, 147456\ns_add_u32 s92, s92, s70\n"
"s_add_u32 s93, s92, 8192\nv_and_b32 v5, 3, v0\nv_min_u32 v5, 2, v5\ns_sub_u32 s74, s81, s80\ns_sub_u32 s74, s74, 5\nv_sub_u32 v6, s74, v5\n"
"v_and_b32 v6, 7, v6\nv_lshlrev_b32 v6, 6, v6\nv_lshl_add_u32 v253, v5, 13, v6\ns_lshl_b32 s73, s80, 9\ns_add_u32 s73, s73, 106496\n"
"v_mov_b32 v10, 163776\ns_lshr_b32 s74, s80, 2\ns_lshl_b32 s74, s74, 11\ns_add_u32 s74, s74, 28672\nv_lshlrev_b32 v8, 6, v0\nv_add_u32 v9, s74, v8\n"
"v_add_u32 v8, s73, v253\ns_mov_b32 exec_hi, 0\nv_mov_b32 v8, v9\ns_mov_b32 exec_hi, -1\ns_bfe_u32 s74, s84, 0x20004\ns_lshl_b32 s74, s74, 5\n"
"s_and_b32 s75, s84, 7\ns_lshl_b32 s75, s75, 2\ns_add_u32 s74, s74, s75\ns_lshr_b32 s75, s84, 6\ns_add_u32 s74, s74, s75\ns_lshl_b32 s74, s74, 6\n"
"s_add_u32 s74, s74, 28672\nv_mov_b32 v11, s72\ns_lshl_b32 s64, s68, 20\ns_add_u32 s64, s64, 8126464\ns_lshl_b32 s69, s68, 17\n"
"s_add_u32 s69, s69, 10223616\ns_mov_b32 s66, 0x40000\ns_cmp_lg_u32 s71, 0\ns_waitcnt vmcnt(2)\ns_barrier\ns_cbranch_scc1 10f\ns_mov_b64 exec, 1\n"
"buffer_store_dword v11, off, s[52:55], s74 sc1\ns_mov_b64 exec, -1\n10:\nR 176,240,0\nR 178,240,1024\nR 180,241,0\nR 182,241,1024\nR 184,242,0\n"
"R 186,242,1024\nR 188,243,0\nR 190,243,1024\nR 192,248,0\nR 194,248,128\nR 196,242,4608\nR 198,242,5632\nR 200,243,4608\nR 202,243,5632\n"
"R 204,240,4608\nR 206,240,5632\nR 208,241,4608\nR 210,241,5632\nR 212,248,576\nR 214,248,704\nR 216,240,9216\nR 218,240,10240\nR 220,241,9216\n"
"R 222,241,10240\nR 224,242,9216\nR 226,242,10240\ns_cmp_lg_u32 s71, 7\ns_cbranch_scc1 17f\n15:\nbuffer_load_dword v9, v8, s[52:55], 0 offen sc1\n"
"s_waitcnt vmcnt(0)\nv_cmp_eq_u32 s[62:63], s72, v9\ns_sub_u32 s66, s66, 1\ns_cmp_eq_u32 s66, 0\ns_cbranch_scc1 16f\ns_cmp_lg_u32 s62, -1\n"
"s_cbranch_scc1 15b\n16:\nv_mov_b32 v9, s63\nds_write_b32 v10, v9\nW 0\n17:\ns_mov_b32 s66, 0x40000\n11:\ns_waitcnt vmcnt(0)\ns_sub_u32 s66, s66, 1\n"
"s_cmp_eq_u32 s66, 0\ns_cbranch_scc1 13f\nv_cmp_ne_u32 s[74:75], s72, v175\ns_nop 0\ns_cmp_eq_u64 s[74:75], 0\ns_cbranch_scc1 13f\ns_sleep 1\n"
"buffer_load_dword v175, v174, s[52:55], s65 offen sc1\ns_branch 11b\n13:\ns_barrier\nds_read_b32 v175, v10\n"
"buffer_load_dwordx4 v[0:3], v169, s[52:55], s64 offen sc1\nbuffer_load_dwordx4 v[4:7], v170, s[52:55], s64 offen sc1\ns_add_u32 s64, s64, 131072\n"
"buffer_load_dwordx4 v[8:11], v169, s[52:55], s64 offen sc1\nbuffer_load_dwordx4 v[12:15], v170, s[52:55], s64 offen sc1\ns_add_u32 s64, s64, 131072\n"
"buffer_load_dwordx4 v[16:19], v169, s[52:55], s64 offen sc1\nbuffer_load_dwordx4 v[20:23], v170, s[52:55], s64 offen sc1\ns_add_u32 s64, s64, 131072\n"
"W 0\nv_readfirstlane_b32 s66, v175\ns_waitcnt vmcnt(4) \nZ 72,176,0\nR 228,243,9216\nZ 92,176,4\nR 230,243,10240\n"
"buffer_load_dwordx4 v[24:27], v169, s[52:55], s64 offen sc1\nbuffer_load_dwordx4 v[28:31], v170, s[52:55], s64 offen sc1\ns_add_u32 s64, s64, 131072\n"
"Z 76,180,0\nR 176,248,1152\nZ 96,180,4\nR 178,248,1280\nZ 80,184,0\nR 180,242,13824\nZ 100,184,4\nR 182,242,14848\nZ 84,188,0\nR 184,243,13824\n"
"Z 104,188,4\nR 186,243,14848\nZ 88,192,0\nR 188,240,13824\nZ 108,192,4\nR 190,240,14848\ns_waitcnt vmcnt(4) \nM 72,196,8\nR 192,241,13824\nM 92,196,12\n"
"R 194,241,14848\nbuffer_load_dwordx4 v[32:35], v169, s[52:55], s64 offen sc1\nbuffer_load_dwordx4 v[36:39], v170, s[52:55], s64 offen sc1\n"
"s_add_u32 s64, s64, 131072\nM 76,200,8\nR 196,248,1728\nM 96,200,12\nR 198,248,1856\nM 80,204,8\nR 200,240,18432\nM 100,204,12\nR 202,240,19456\n"
"M 84,208,8\nR 204,241,18432\nM 104,208,12\nR 206,241,19456\nM 88,212,8\nR 208,242,18432\nM 108,212,12\nR 210,242,19456\ns_waitcnt vmcnt(4) \n"
"M 72,216,16\nR 212,243,18432\nM 92,216,20\nR 214,243,19456\nbuffer_load_dwordx4 v[40:43], v169, s[52:55], s64 offen sc1\n"
"buffer_load_dwordx4 v[44:47], v170, s[52:55], s64 offen sc1\ns_add_u32 s64, s64, 131072\nM 76,220,16\nR 216,248,2304\nM 96,220,20\nR 218,248,2432\n"
"M 80,224,16\nR 220,242,23040\nM 100,224,20\nR 222,242,24064\nW 15\nM 84,228,16\nR 224,243,23040\nM 104,228,20\nR 226,243,24064\nW 15\nM 88,176,16\n"
"R 228,240,23040\nM 108,176,20\nR 230,240,24064\ns_waitcnt vmcnt(4) lgkmcnt(15)\nM 72,180,24\nR 176,241,23040\nM 92,180,28\nR 178,241,24064\n"
"buffer_load_dwordx4 v[48:51], v169, s[52:55], s64 offen sc1\nbuffer_load_dwordx4 v[52:55], v170, s[52:55], s64 offen sc1\ns_add_u32 s64, s64, 131072\n"
"W 15\nM 76,184,24\nR 180,248,2880\nM 96,184,28\nR 182,248,3008\nW 15\nM 80,188,24\nR 184,240,27648\nM 100,188,28\nR 186,240,28672\nW 15\nM 84,192,24\n"
"R 188,241,27648\nM 104,192,28\nR 190,241,28672\nW 15\nM 88,196,24\nR 192,242,27648\nM 108,196,28\nR 194,242,28672\ns_waitcnt vmcnt(4) lgkmcnt(15)\n"
"M 72,200,32\nR 196,243,27648\nM 92,200,36\nR 198,243,28672\nbuffer_load_dwordx4 v[56:59], v169, s[52:55], s64 offen sc1\n"
"buffer_load_dwordx4 v[60:63], v170, s[52:55], s64 offen sc1\ns_add_u32 s64, s64, 131072\nW 15\nM 76,204,32\nR 200,248,3456\nM 96,204,36\n"
"R 202,248,3584\nW 15\nM 80,208,32\nR 204,242,32256\nM 100,208,36\nR 206,242,33280\nW 15\nM 84,212,32\nR 208,243,32256\nM 104,212,36\nR 210,243,33280\n"
"W 15\nM 88,216,32\nR 212,240,32256\nM 108,216,36\nR 214,240,33280\ns_waitcnt vmcnt(4) lgkmcnt(15)\nM 72,220,40\nR 216,241,32256\nM 92,220,44\n"
"R 218,241,33280\nbuffer_load_dwordx2 v[64:65], v171, s[52:55], s69 offen sc1\nbuffer_load_dwordx2 v[68:69], v172, s[52:55], s69 offen sc1\n"
"s_add_u32 s69, s69, 65536\nbuffer_load_dwordx2 v[66:67], v171, s[52:55], s69 offen sc1\nbuffer_load_dwordx2 v[70:71], v172, s[52:55], s69 offen sc1\n"
"s_add_u32 s69, s69, 65536\nW 15\nM 76,224,40\nR 220,248,4032\nM 96,224,44\nR 222,248,4160\nW 15\nM 80,228,40\nR 224,244,0\nM 100,228,44\n"
"R 226,244,18432\nW 15\nM 84,176,40\nR 228,245,0\nM 104,176,44\nR 230,245,18432\nW 15\nM 88,180,40\nR 176,246,0\nM 108,180,44\nR 178,246,18432\n"
"s_waitcnt vmcnt(6) lgkmcnt(15)\nM 72,184,48\nR 180,247,0\nM 92,184,52\nR 182,247,18432\ns_mov_b32 s65, s70\n"
"buffer_load_dwordx2 v[232:233], v252, s[52:55], s65 offen sc1\ns_add_u32 s65, s65, 147456\n"
"buffer_load_dwordx2 v[234:235], v252, s[52:55], s65 offen sc1\ns_add_u32 s65, s65, 147456\n"
"buffer_load_dwordx2 v[236:237], v252, s[52:55], s65 offen sc1\ns_add_u32 s65, s65, 147456\n"
"buffer_load_dwordx2 v[238:239], v252, s[52:55], s65 offen sc1\ns_add_u32 s65, s65, 147456\ns_and_b32 s66, s66, 7\ns_ff0_i32_b32 s66, s66\n"
"s_mov_b32 s69, s66\ns_cmp_gt_u32 s69, 0\ns_cselect_b32 s54, s54, 0\nbuffer_load_dwordx4 v[132:135], v251, s[52:55], s88 offen sc1\n"
"buffer_load_dwordx4 v[136:139], v251, s[52:55], s89 offen sc1\ns_cmp_gt_u32 s69, 1\ns_cselect_b32 s54, s54, 0\n"
"buffer_load_dwordx4 v[140:143], v251, s[52:55], s90 offen sc1\nbuffer_load_dwordx4 v[144:147], v251, s[52:55], s91 offen sc1\ns_cmp_gt_u32 s69, 2\n"
"s_cselect_b32 s54, s54, 0\nbuffer_load_dwordx4 v[148:151], v251, s[52:55], s92 offen sc1\n"
"buffer_load_dwordx4 v[152:155], v251, s[52:55], s93 offen sc1\ns_mov_b32 s54, 31522816\nW 15\nM 76,188,48\nR 184,249,0\nM 96,188,52\nR 186,249,2304\n"
"W 15\nM 80,192,48\nM 100,192,52\nW 15\nM 84,196,48\nM 104,196,52\nW 15\nM 88,200,48\nM 108,200,52\ns_waitcnt vmcnt(14) lgkmcnt(15)\nM 72,204,56\n"
"M 92,204,60\nbuffer_load_dword v175, v253, s[52:55], s73 offen sc1\nW 15\nM 76,208,56\nM 96,208,60\nW 14\nM 80,212,56\nM 100,212,60\nW 12\nM 84,216,56\n"
"M 104,216,60\nW 10\nM 88,220,56\nM 108,220,60\ns_waitcnt vmcnt(11) lgkmcnt(8)\nM 72,224,64\nM 92,224,68\nW 6\nM 76,228,64\nM 96,228,68\nW 4\n"
"M 80,176,64\nM 100,176,68\nW 2\nM 84,180,64\nM 104,180,68\nW 0\nM 88,184,64\nM 108,184,68\ns_waitcnt vmcnt(7)\nZ 128,164,232\nM 128,164,236\n"
"s_cmp_eq_u32 s69, 3\ns_cbranch_scc1 22f\ns_waitcnt vmcnt(0)\nv_cmp_eq_u32 s[74:75], s72, v175\ns_nop 0\ns_and_b32 s66, s74, 7\ns_ff0_i32_b32 s66, s66\n"
"s_cmp_lg_u32 s69, 0\ns_cbranch_scc1 30f\ns_cmp_le_u32 s66, 0\ns_cbranch_scc1 32f\nbuffer_load_dwordx4 v[132:135], v251, s[52:55], s88 offen sc1\n"
"buffer_load_dwordx4 v[136:139], v251, s[52:55], s89 offen sc1\ns_mov_b32 s69, 1\n30:\ns_cmp_lg_u32 s69, 1\ns_cbranch_scc1 31f\ns_cmp_le_u32 s66, 1\n"
"s_cbranch_scc1 32f\nbuffer_load_dwordx4 v[140:143], v251, s[52:55], s90 offen sc1\nbuffer_load_dwordx4 v[144:147], v251, s[52:55], s91 offen sc1\n"
"s_mov_b32 s69, 2\n31:\ns_cmp_lg_u32 s69, 2\ns_cbranch_scc1 32f\ns_cmp_le_u32 s66, 2\ns_cbranch_scc1 32f\n"
"buffer_load_dwordx4 v[148:151], v251, s[52:55], s92 offen sc1\nbuffer_load_dwordx4 v[152:155], v251, s[52:55], s93 offen sc1\ns_mov_b32 s69, 3\n32:\n"
"22:\ns_nop 4\nds_write_b128 v173, v[92:95] offset:0\nds_write_b128 v173, v[96:99] offset:1024\nds_write_b128 v173, v[100:103] offset:2048\n"
"ds_write_b128 v173, v[104:107] offset:3072\nds_write_b128 v173, v[108:111] offset:4096\nv_lshrrev_b32 v0, 4, v250\nv_and_b32 v1, 15, v0\n"
"v_lshrrev_b32 v2, 4, v0\ns_mul_i32 s64, s71, 2304\ns_add_u32 s64, s64, 128000\nv_mul_u32_u24 v3, 144, v1\nv_add_u32 v3, s64, v3\n"
"v_lshl_add_u32 v4, v2, 3, v3\nv_lshrrev_b32 v5, 3, v0\nv_and_b32 v6, 7, v0\nv_mul_u32_u24 v7, 144, v5\nv_lshl_add_u32 v7, v6, 4, v7\n"
"v_add_u32 v7, s64, v7\nv_mul_u32_u24 v8, 2304, v5\nv_lshl_add_u32 v8, v6, 4, v8\nv_mul_u32_u24 v9, 2304, v1\ns_lshl_b32 s64, s68, 1\n"
"s_sub_u32 s64, 1, s64\ns_mul_i32 s64, s64, 20480\nv_add_u32 v173, s64, v173\ns_cmp_lg_u32 s68, 0\ns_cbranch_scc1 24f\ns_cmp_eq_u32 s69, 3\n"
"s_cbranch_scc1 24f\nbuffer_load_dword v175, v253, s[52:55], s73 offen sc1\ns_waitcnt vmcnt(0)\nv_cmp_eq_u32 s[74:75], s72, v175\ns_nop 0\n"
"s_and_b32 s66, s74, 7\ns_ff0_i32_b32 s66, s66\ns_cmp_lg_u32 s69, 0\ns_cbranch_scc1 40f\ns_cmp_le_u32 s66, 0\ns_cbranch_scc1 42f\n"
"buffer_load_dwordx4 v[132:135], v251, s[52:55], s88 offen sc1\nbuffer_load_dwordx4 v[136:139], v251, s[52:55], s89 offen sc1\ns_mov_b32 s69, 1\n40:\n"
"s_cmp_lg_u32 s69, 1\ns_cbranch_scc1 41f\ns_cmp_le_u32 s66, 1\ns_cbranch_scc1 42f\nbuffer_load_dwordx4 v[140:143], v251, s[52:55], s90 offen sc1\n"
"buffer_load_dwordx4 v[144:147], v251, s[52:55], s91 offen sc1\ns_mov_b32 s69, 2\n41:\ns_cmp_lg_u32 s69, 2\ns_cbranch_scc1 42f\ns_cmp_le_u32 s66, 2\n"
"s_cbranch_scc1 42f\nbuffer_load_dwordx4 v[148:151], v251, s[52:55], s92 offen sc1\nbuffer_load_dwordx4 v[152:155], v251, s[52:55], s93 offen sc1\n"
"s_mov_b32 s69, 3\n42:\n24:\nW 0\ns_barrier\nds_read_b128 v[92:95], v173 offset:0\nds_read_b128 v[96:99], v173 offset:1024\n"
"ds_read_b128 v[100:103], v173 offset:2048\nds_read_b128 v[104:107], v173 offset:3072\nds_read_b128 v[108:111], v173 offset:4096\n"
"s_mov_b32 s58, 2359296\ns_mov_b32 s59, 0x20000\ns_mul_i32 s67, s67, 1152\ns_lshl_b32 s74, s81, 7\ns_add_u32 s74, s74, s67\ns_add_u32 s75, s74, 18432\n"
"s_lshl_b32 s64, s81, 4\ns_add_u32 s64, s64, s67\nW 0\nv_add_f32 v72, v72, v92\nv_add_f32 v73, v73, v93\nv_add_f32 v74, v74, v94\n"
"v_add_f32 v75, v75, v95\nv_add_f32 v76, v76, v96\nv_add_f32 v77, v77, v97\nv_add_f32 v78, v78, v98\nv_add_f32 v79, v79, v99\nv_add_f32 v80, v80, v100\n"
"v_add_f32 v81, v81, v101\nv_add_f32 v82, v82, v102\nv_add_f32 v83, v83, v103\nv_add_f32 v84, v84, v104\nv_add_f32 v85, v85, v105\n"
"v_add_f32 v86, v86, v106\nv_add_f32 v87, v87, v107\nv_add_f32 v88, v88, v108\nv_add_f32 v89, v89, v109\nv_add_f32 v90, v90, v110\n"
"v_add_f32 v91, v91, v111\nv_sub_f32 v10, 1.0, v168\nv_mul_f32 v72, v168, v72\nv_mul_f32 v73, v168, v73\nv_mul_f32 v74, v168, v74\n"
"v_mul_f32 v75, v168, v75\nv_mul_f32 v76, v168, v76\nv_mul_f32 v77, v168, v77\nv_mul_f32 v78, v168, v78\nv_mul_f32 v79, v168, v79\n"
"v_mul_f32 v80, v168, v80\nv_mul_f32 v81, v168, v81\nv_mul_f32 v82, v168, v82\nv_mul_f32 v83, v168, v83\nv_mul_f32 v84, v168, v84\n"
"v_mul_f32 v85, v168, v85\nv_mul_f32 v86, v168, v86\nv_mul_f32 v87, v168, v87\nv_mul_f32 v88, v168, v88\nv_mul_f32 v89, v168, v89\n"
"v_mul_f32 v90, v168, v90\nv_mul_f32 v91, v168, v91\nv_fmac_f32 v88, v10, v128\nv_fmac_f32 v89, v10, v129\nv_fmac_f32 v90, v10, v130\n"
"v_fmac_f32 v91, v10, v131\nv_cvt_pk_bf16_f32 v20, v88, v89\nv_cvt_pk_bf16_f32 v21, v90, v91\ns_mov_b32 exec_hi, 0\n"
"ds_write_b64 v4, v[20:21] offset:128\ns_mov_b32 exec_hi, -1\ns_mov_b32 s62, 0x40000\n18:\ns_cmp_eq_u32 s69, 3\ns_cbranch_scc1 20f\n"
"buffer_load_dword v175, v253, s[52:55], s73 offen sc1\ns_waitcnt vmcnt(0)\nv_cmp_eq_u32 s[86:87], s72, v175\ns_nop 0\ns_and_b32 s66, s86, 7\n"
"s_ff0_i32_b32 s66, s66\ns_sub_u32 s62, s62, 1\ns_cmp_eq_u32 s62, 0\ns_cselect_b32 s66, 3, s66\ns_cmp_lg_u32 s69, 0\ns_cbranch_scc1 35f\n"
"s_cmp_le_u32 s66, 0\ns_cbranch_scc1 37f\nbuffer_load_dwordx4 v[132:135], v251, s[52:55], s88 offen sc1\n"
"buffer_load_dwordx4 v[136:139], v251, s[52:55], s89 offen sc1\ns_mov_b32 s69, 1\n35:\ns_cmp_lg_u32 s69, 1\ns_cbranch_scc1 36f\ns_cmp_le_u32 s66, 1\n"
"s_cbranch_scc1 37f\nbuffer_load_dwordx4 v[140:143], v251, s[52:55], s90 offen sc1\nbuffer_load_dwordx4 v[144:147], v251, s[52:55], s91 offen sc1\n"
"s_mov_b32 s69, 2\n36:\ns_cmp_lg_u32 s69, 2\ns_cbranch_scc1 37f\ns_cmp_le_u32 s66, 2\ns_cbranch_scc1 37f\n"
"buffer_load_dwordx4 v[148:151], v251, s[52:55], s92 offen sc1\nbuffer_load_dwordx4 v[152:155], v251, s[52:55], s93 offen sc1\ns_mov_b32 s69, 3\n37:\n"
"s_branch 18b\n20:\ns_waitcnt vmcnt(4)\nZ 112,156,132\nZ 116,160,132\nZ 120,156,136\nZ 124,160,136\ns_waitcnt vmcnt(2)\nM 112,156,140\nM 116,160,140\n"
"M 120,156,144\nM 124,160,144\ns_waitcnt vmcnt(0)\nM 112,156,148\nM 116,160,148\nM 120,156,152\nM 124,160,152\ns_nop 4\nv_fmac_f32 v72, v10, v112\n"
"v_fmac_f32 v73, v10, v113\nv_fmac_f32 v74, v10, v114\nv_fmac_f32 v75, v10, v115\nv_cvt_pk_bf16_f32 v12, v72, v73\nv_cvt_pk_bf16_f32 v13, v74, v75\n"
"ds_write_b64 v4, v[12:13] offset:0\nv_fmac_f32 v76, v10, v116\nv_fmac_f32 v77, v10, v117\nv_fmac_f32 v78, v10, v118\nv_fmac_f32 v79, v10, v119\n"
"v_cvt_pk_bf16_f32 v14, v76, v77\nv_cvt_pk_bf16_f32 v15, v78, v79\nds_write_b64 v4, v[14:15] offset:32\nv_fmac_f32 v80, v10, v120\n"
"v_fmac_f32 v81, v10, v121\nv_fmac_f32 v82, v10, v122\nv_fmac_f32 v83, v10, v123\nv_cvt_pk_bf16_f32 v16, v80, v81\nv_cvt_pk_bf16_f32 v17, v82, v83\n"
"ds_write_b64 v4, v[16:17] offset:64\nv_fmac_f32 v84, v10, v124\nv_fmac_f32 v85, v10, v125\nv_fmac_f32 v86, v10, v126\nv_fmac_f32 v87, v10, v127\n"
"v_cvt_pk_bf16_f32 v18, v84, v85\nv_cvt_pk_bf16_f32 v19, v86, v87\nds_write_b64 v4, v[18:19] offset:96\nW 0\nds_read_b128 v[24:27], v7\n"
"ds_read_b128 v[28:31], v7 offset:1152\nds_read_b128 v[32:35], v3 offset:128\nW 2\nbuffer_store_dwordx4 v[24:27], v8, s[56:59], s74 offen\nW 1\n"
"buffer_store_dwordx4 v[28:31], v8, s[56:59], s75 offen\ns_mov_b64 exec, 0xffff\nW 0\n"
"buffer_store_dwordx4 v[32:35], v9, s[56:59], s64 offen offset:1024\ns_mov_b64 exec, -1\ns_endpgm\n.purgem M\n.purgem Z\n.purgem R\n.purgem W\n"
".purgem E\n"
        :
        : "{s[52:53]}"(ws), "{s[56:57]}"(out), "{s71}"(w), "{s72}"(tag), "{s84}"(blockIdx.x)
        : "memory", "scc", "vcc");
    __builtin_unreachable();
  }
}

extern "C" void kernel_launch(void* const* d_in, const int* in_sizes, int n_in, void* d_out, int out_size, void* d_ws, size_t ws_size, hipStream_t stream) {
  (void)hipFuncSetAttribute((const void*)fused_kernel, hipFuncAttributeMaxDynamicSharedMemorySize, LDS_BYTES);
#define ARGS (const char*)d_in[0], (const char*)d_in[1], (const char*)d_in[2], (const char*)d_in[4], (const char*)d_in[5], (const char*)d_in[6], (const char*)d_in[7], (char*)d_out, (char*)d_ws
  fused_kernel<<<256, 512, LDS_BYTES, stream>>>(ARGS);
}
```
